```python
import torch

def _softmax(x, axis=-1):
    return torch.softmax(x, dim=axis)
def _gelu(x):
    return torch.nn.functional.gelu(x, approximate="tanh")
def _silu(x):
    return torch.nn.functional.silu(x)
def _sigmoid(x):
    return torch.sigmoid(x)
def _layernorm(x, axis=-1, eps=1e-5):
    m = x.mean(dim=axis, keepdim=True)
    v = x.var(dim=axis, keepdim=True, unbiased=False)
    return (x - m) / torch.sqrt(v + eps)
def _scatter_add(tgt, idx, src, axis=0):
    return tgt.clone().index_add_(axis, idx, src)
def _segment_sum(src, seg, n):
    out = torch.zeros((n,) + src.shape[1:], dtype=src.dtype)
    return out.index_add_(0, seg, src)
def _pad(x, pw):
    flat = tuple(v for pair in reversed(pw) for v in pair)
    return torch.nn.functional.pad(x, flat)

kernel_name = 'sequential_preLN__ssm_conv_s4__moe_arch_mod__prompt'

def setup_inputs():

    _w = (lambda a: (lambda t: t.to(torch.bfloat16) if t.is_floating_point() else t)(torch.as_tensor(a)))
    def _A_setup():
        B, L, D, N = 1, 2048, 576, 128
        A_re = -(lambda _x: torch.exp(torch.as_tensor(_x)))((lambda *s: torch.randn(*s, dtype=torch.bfloat16) * 1.6)(D, N))
        A_im = torch.pi * (lambda *s: torch.randn(*s, dtype=torch.bfloat16) * 0.2)(D, N)
        B_re = (lambda *s: torch.randn(*s, dtype=torch.bfloat16) * 0.2)(D, N)
        B_im = (lambda *s: torch.randn(*s, dtype=torch.bfloat16) * 0.2)(D, N)
        C_re = (lambda *s: torch.randn(*s, dtype=torch.bfloat16) * 0.2)(D, N)
        C_im = (lambda *s: torch.randn(*s, dtype=torch.bfloat16) * 0.2)(D, N)
        log_dt = (lambda *s: torch.randn(*s, dtype=torch.bfloat16) * 0.2)(D) * 0.5 - 4.0
        Dskip = (lambda *s: torch.randn(*s, dtype=torch.bfloat16) * 0.2)(D)
        return {
            "u": _w((lambda *s: torch.randn(*s, dtype=torch.bfloat16) * 0.05)(B, L, D)),
            "A_re": _w(A_re),
            "A_im": _w(A_im),
            "B_re": _w(B_re),
            "B_im": _w(B_im),
            "C_re": _w(C_re),
            "C_im": _w(C_im),
            "log_dt": _w(log_dt),
            "Dskip": _w(Dskip),
        }
    _A_in = _A_setup()

    def _B_setup():
        B, S, D, F, C = 1, 2048, 576, 4608, 8
        return {
            "x": _w((lambda *s: torch.randn(*s, dtype=torch.bfloat16) * 0.05)(B, S, D)),
            "w_r": _w((lambda *s: torch.randn(*s, dtype=torch.bfloat16) * 0.2)(D)),
            "w_gate": _w((lambda *s: torch.randn(*s, dtype=torch.bfloat16) * 0.2)(D, F)),
            "w_up": _w((lambda *s: torch.randn(*s, dtype=torch.bfloat16) * 0.2)(D, F)),
            "w_down": _w((lambda *s: torch.randn(*s, dtype=torch.bfloat16) * 0.2)(F, D)),
        }
    _B_in = _B_setup()

    return {
        "A_u": _A_in["u"],
        "A_A_re": _A_in["A_re"],
        "A_A_im": _A_in["A_im"],
        "A_B_re": _A_in["B_re"],
        "A_B_im": _A_in["B_im"],
        "A_C_re": _A_in["C_re"],
        "A_C_im": _A_in["C_im"],
        "A_log_dt": _A_in["log_dt"],
        "A_Dskip": _A_in["Dskip"],
        "B_x": _B_in["x"],
        "B_w_r": _B_in["w_r"],
        "B_w_gate": _B_in["w_gate"],
        "B_w_up": _B_in["w_up"],
        "B_w_down": _B_in["w_down"],
    }

def reference(A_u, A_A_re, A_A_im, A_B_re, A_B_im, A_C_re, A_C_im, A_log_dt, A_Dskip, B_x, B_w_r, B_w_gate, B_w_up, B_w_down):
    def _A_ref(u, A_re, A_im, B_re, B_im, C_re, C_im, log_dt, Dskip):
        Bsz, L, D = u.shape
        dt = torch.exp(log_dt)[:, None]
        A = A_re + 1j * A_im
        Bc = B_re + 1j * B_im
        Cc = C_re + 1j * C_im
        logdA = dt * A
        dB = dt * Bc
        l = (lambda x, dt: x.to(dt))(torch.arange(L), A_re.dtype)
        P = torch.exp(logdA[:, :, None] * l[None, None, :])
        K = torch.einsum("dn,dnl,dn->dl", Cc, P, dB).real
        L2 = 2 * L
        Kp = _pad(K, ((0, 0), (0, L)))
        up = (lambda x, *d: x.permute(*d))(u, 0, 2, 1)
        up = _pad(up, ((0, 0), (0, 0), (0, L)))
        Kf = (lambda x, axes=(-2,-1): torch.fft.rfft2(x.float(), dim=axes))(Kp[:, None, :], axes=(-2, -1))
        Uf = (lambda x, axes=(-2,-1): torch.fft.rfft2(x.float(), dim=axes))(up[:, :, None, :], axes=(-2, -1))
        Yf = Uf * Kf[None, :, :, :]
        y = (lambda x, s, axes=(-2,-1): torch.fft.irfft2(x, s=s, dim=axes).to(torch.bfloat16))(Yf, (1, L2), axes=(-2, -1))[:, :, 0, :L]
        y = (lambda x, *d: x.permute(*d))(y, 0, 2, 1) + u * Dskip[None, None, :]
        return y

    def _B_ref(x, w_r, w_gate, w_up, w_down):
        B, S, D = x.shape
        C = 8
        r = x @ w_r
        idx = torch.argsort(-r, axis=-1)[:, :C]
        idx_e = (lambda x, n, axis: torch.repeat_interleave(x, n, dim=axis))(idx.reshape(B, C, 1), D, axis=2)
        x_sel = (lambda x, i, axis: torch.take_along_dim(x, i, dim=axis))(x, idx_e, axis=1)
        g = _silu(x_sel @ w_gate)
        u = x_sel @ w_up
        y = (g * u) @ w_down
        r_sel = _sigmoid((lambda x, i, axis: torch.take_along_dim(x, i, dim=axis))(r, idx, axis=1))
        y = y * r_sel.reshape(B, C, 1)
        b_off = (torch.arange(B) * S).reshape(B, 1)
        flat_idx = (idx + b_off).reshape(B * C)
        zero = (x * 0.0).reshape(B * S, D)
        upd = _scatter_add(zero, flat_idx, y.reshape(B * C, D), axis=0)
        return x + upd.reshape(B, S, D)

    x = A_u
    h = x + _A_ref(_layernorm(x, axis=-1), A_A_re, A_A_im, A_B_re, A_B_im, A_C_re, A_C_im, A_log_dt, A_Dskip)
    h2 = h + _B_ref(_layernorm(h, axis=-1), B_w_r, B_w_gate, B_w_up, B_w_down)
    return h2
```

```cpp
#define TSLOT_LO 0x2010000
#define TSLOT_HI 0x300
#define TWHEN_LO 0xf2f3f0ff
#define TWHEN_HI 0xf5ff
#define KOFF 4
#define VT 70
#define KT 420
#define UT 225
#include <hip/hip_runtime.h>
#include <hip/hip_bf16.h>
#include <stdint.h>

typedef __attribute__((ext_vector_type(8))) short bf16x8;
typedef __attribute__((ext_vector_type(4))) float f32x4;
typedef uint16_t u16;
typedef __attribute__((ext_vector_type(4))) uint32_t u32x4;
typedef __attribute__((ext_vector_type(3))) uint32_t u32x3;
typedef __attribute__((ext_vector_type(2))) uint32_t u32x2;
#define STR2(x) #x
#define STR(x) STR2(x)
__device__ __forceinline__ int tidx() { int t = threadIdx.x; asm volatile("" : "+v"(t)); return t; }
#define TSD0
#define TSCL
#define TS(i)
#define TSV(i, v)
#define TSQ(i)
#define TSE()

#define LIKELY(x) __builtin_expect(!!(x), 1)
constexpr int L_ = 2048, D_ = 576, N_ = 128, F_ = 4608, C_ = 8;

__device__ __forceinline__ float bf2f(u16 v) { return __uint_as_float((uint32_t)v << 16); }
__device__ __forceinline__ float bflo(uint32_t v) { return __uint_as_float(v << 16); }
__device__ __forceinline__ float bfhi(uint32_t v) { return __uint_as_float(v & 0xffff0000u); }
__device__ __forceinline__ u16 f2bf(float f) { return __builtin_bit_cast(u16, (__bf16)f); }
__device__ __forceinline__ float rbf(float f) { return bf2f(f2bf(f)); }
typedef __attribute__((ext_vector_type(2))) float f32x2;
typedef __attribute__((ext_vector_type(2))) __bf16 bf16x2v;
__device__ __forceinline__ uint32_t pack2(float a, float b) {
  return __builtin_bit_cast(uint32_t, __builtin_convertvector((f32x2){a, b}, bf16x2v));
}

#define DPP_I(v, ctrl, rmask) __builtin_amdgcn_update_dpp(0, (v), (ctrl), (rmask), 0xf, true)
__device__ __forceinline__ uint32_t wave_maxu(uint32_t v) {
  v = max(v, (uint32_t)DPP_I((int)v, 0x111, 0xf));
  v = max(v, (uint32_t)DPP_I((int)v, 0x112, 0xf));
  v = max(v, (uint32_t)DPP_I((int)v, 0x114, 0xf));
  v = max(v, (uint32_t)DPP_I((int)v, 0x118, 0xf));
  v = max(v, (uint32_t)DPP_I((int)v, 0x142, 0xa));
  v = max(v, (uint32_t)DPP_I((int)v, 0x143, 0xc));
  return (uint32_t)__builtin_amdgcn_readlane((int)v, 63);
}
__device__ __forceinline__ void wave_lds_sync() {
  __builtin_amdgcn_s_waitcnt(0xc07f);
  __builtin_amdgcn_wave_barrier();
}
__device__ __forceinline__ void st2(void* p, uint32_t v) {
  asm volatile("global_store_short %0, %1, off sc1" : : "v"(p), "v"(v) : "memory");
}

typedef __attribute__((ext_vector_type(8))) _Float16 f16x8;
typedef __attribute__((ext_vector_type(2))) _Float16 f16x2;
typedef __attribute__((ext_vector_type(2))) unsigned short u16x2;
#define LDS_AS __attribute__((address_space(3)))
constexpr int PS = 288;
constexpr int OPW = 32 * PS;
constexpr int CS = 2112;
constexpr int OFF_F3 = 1184;
constexpr int OFF_F4 = OFF_F3 + 416;
constexpr int OFF_X = 2064;
constexpr int OFF_U = CS + 480;
constexpr int CHS = 4 * CS + 480 + 4096;
constexpr int G_CNT = 0;
constexpr int G_BAND = 512;
constexpr int G_OP = G_BAND + 2304;
constexpr int G_C0 = G_OP + 12 * OPW + 3 * CS;
constexpr int GRP = G_OP + 12 * OPW + 3 * CHS + 7168;
constexpr int G_IMG = G_OP + 2 * OPW, G_CH = G_OP + 6 * OPW;
static_assert(G_OP % 128 == 0 && G_C0 % 16 == 0 && CHS % 16 == 0 && GRP <= 160 * 1024, "lds map");
__device__ __forceinline__ uint32_t packh2(float a, float b) {
  return __builtin_bit_cast(uint32_t, __builtin_convertvector((f32x2){a, b}, f16x2));
}
__device__ __forceinline__ f32x2 cmul(f32x2 a, f32x2 b) {
  f32x2 t, r;
  asm("v_pk_mul_f32 %0, %1, %2 op_sel_hi:[0,1]" : "=v"(t) : "v"(a), "v"(b));
  asm("v_pk_fma_f32 %0, %1, %2, %3 op_sel:[1,1,0] op_sel_hi:[1,0,1] neg_lo:[1,0,0]" : "=v"(r) : "v"(a), "v"(b), "v"(t));
  return r;
}
__device__ __forceinline__ void cmul2(f32x2& r0, f32x2& r1, f32x2 a0, f32x2 b0, f32x2 a1, f32x2 b1) {
  asm("v_pk_mul_f32 %0, %2, %3 op_sel_hi:[0,1]\n\tv_pk_mul_f32 %1, %4, %5 op_sel_hi:[0,1]\n\t"
      "v_pk_fma_f32 %0, %2, %3, %0 op_sel:[1,1,0] op_sel_hi:[1,0,1] neg_lo:[1,0,0]\n\t"
      "v_pk_fma_f32 %1, %4, %5, %1 op_sel:[1,1,0] op_sel_hi:[1,0,1] neg_lo:[1,0,0]"
      : "=&v"(r0), "=&v"(r1) : "v"(a0), "v"(b0), "v"(a1), "v"(b1));
}
__device__ __forceinline__ f32x2 cexp2(f32x2 a, float l) {
  const f32x2 t = a * l;
  const float f = __builtin_amdgcn_fractf(t[1]);
  return (f32x2){__builtin_amdgcn_cosf(f), __builtin_amdgcn_sinf(f)} * __builtin_amdgcn_exp2f(t[0]);
}
#define MFMA16(a, b, c) __builtin_amdgcn_mfma_f32_16x16x32_f16(__builtin_bit_cast(f16x8, a), __builtin_bit_cast(f16x8, b), c, 0, 0, 0)
__device__ __forceinline__ f32x2 bf2x(uint32_t v) { return (f32x2){bflo(v), bfhi(v)}; }
__device__ __forceinline__ float half_sum(float v) {
  v += __int_as_float(DPP_I(__float_as_int(v), 0xb1, 0xf));
  v += __int_as_float(DPP_I(__float_as_int(v), 0x4e, 0xf));
  v += __int_as_float(DPP_I(__float_as_int(v), 0x141, 0xf));
  v += __int_as_float(DPP_I(__float_as_int(v), 0x140, 0xf));
  const auto r = __builtin_amdgcn_permlane16_swap(__float_as_uint(v), __float_as_uint(v), false, false);
  return __uint_as_float(r[0]) + __uint_as_float(r[1]);
}

#define DADD(v, ctrl) v += __int_as_float(DPP_I(__float_as_int(v), ctrl, 0xf))
#define DOT2(a, b, c) __builtin_amdgcn_fdot2_f32_bf16(__builtin_bit_cast(bf16x2v, a), __builtin_bit_cast(bf16x2v, b), c, false)
typedef const volatile LDS_AS u32x2* afrag_p;
typedef const volatile LDS_AS u32x4* frag_p;
__device__ __forceinline__ u32x4 ld_af(afrag_p p, int off) {
  const u32x2 lo = p[off / 8], hi = p[off / 8 + 1];
  return (u32x4){lo[0], lo[1], hi[0], hi[1]};
}
__device__ __forceinline__ u32x4 pack_w(f32x4 a, f32x4 b) {
  return (u32x4){packh2(a[0], a[1]), packh2(a[2], a[3]), packh2(b[0], b[1]), packh2(b[2], b[3])};
}
#define SB0() __builtin_amdgcn_sched_barrier(0)
__device__ __forceinline__ uint32_t perm_lo(uint32_t a, uint32_t b) { return __builtin_amdgcn_perm(b, a, 0x05040100u); }
__device__ __forceinline__ uint32_t perm_hi(uint32_t a, uint32_t b) { return __builtin_amdgcn_perm(b, a, 0x07060302u); }
__device__ __forceinline__ void st16s(void* sbase, uint32_t voff, u32x4 v) {
  asm volatile("global_store_dwordx4 %0, %1, %2 sc1\n\ts_nop 1" : : "v"(voff), "v"(v), "s"(sbase) : "memory");
}
constexpr int SPIN_CAP = 1 << 18;
extern "C" __device__ uint64_t amdgcn_dispatch_id() __asm("llvm.amdgcn.dispatch.id");
#define OT 170
#define FW 60
#define PF 1
#define P2 "1"
#define P0 "0"
#define PT "3"
#define P1 "2"
#define PM "3"
#define PW "3"
#define WD 1
#define VT5 (VT - 20)
#define VT4 (VT - 25)
#define MK 0
#define HT 110
#define HF 450
#define PE "0"
#define PL "2"
#define PL1 "3"
#define EM 2
#define UD 0
#define PN "3"
#define TOPCAP "8"
#define LE 0xf70
#define WNT
#define TNT
#define WSOFS 4096
#define AV0 "1"
#define ESL "1"
#define FW2 20
#define USLEEP 4
#define PSLEEP 2
#define PSLEEP2 1
#define NCOPY 2
#define XR 4
#define FORCE1 0
#define NROT 8
#define VMWAIT(n) do { asm volatile("s_waitcnt vmcnt(" #n ")" ::: "memory"); __builtin_amdgcn_sched_barrier(0); } while (0)
struct KaArgs {
  const u16 *x, *Are, *Aim, *Bre, *Bim, *Cre, *Cim, *logdt, *Dskip;
  char* ws;
  const u16 *wr, *wg, *wu, *wd;
  u16* out;
  void* spare;
};
constexpr uint32_t WS_UG = 0, WS_VG = 3u << 20, WS_XN = 6u << 20, WS_SLAB = 9u << 20, WS_KEYS = 11u << 20, WS_HINT = 12u << 20, WS_SET = 16u << 20, WSOFF = 4096;
constexpr uint32_t SLABB = C_ * D_ * 4;
#define CEX(a, b) { const uint32_t hi_ = max(a, b), lo_ = min(a, b); a = hi_; b = lo_; }
#define MFMAB(a, b, c) __builtin_amdgcn_mfma_f32_16x16x32_bf16(__builtin_bit_cast(bf16x8, a), __builtin_bit_cast(bf16x8, b), c, 0, 0, 0)

__device__ __forceinline__ u32x4 tag_bits(uint32_t t) {
  return (u32x4){t & 0x70007u, (t >> 3) & 0x70007u, (t >> 6) & 0x70007u, (t >> 9) & 0x70007u};
}
__device__ __forceinline__ uint32_t spat(uint32_t t, int j) { return ((j == 0) ? t : (j == 1) ? t >> 16 : (j == 2) ? t >> 8 : t >> 22) & 1023u; }
#define XOR_OR(acc, a, b) acc = __builtin_amdgcn_bitop3_b32(acc, a, b, 0xf6)
#define NOW() ((uint32_t)__builtin_amdgcn_s_memrealtime())
#define WAIT_T(t0, dt) do { while ((int)(NOW() - (t0)) < (dt) - 30) __builtin_amdgcn_s_sleep(8); \
    while ((int)(NOW() - (t0)) < (dt)) __builtin_amdgcn_s_sleep(1); } while (0)
#define SETPRIO(n) __builtin_amdgcn_s_setprio(n)
typedef __attribute__((ext_vector_type(4))) short s16x4;
__device__ __forceinline__ u32x2 ld_tr(const char* p) {
  return __builtin_bit_cast(u32x2, __builtin_amdgcn_ds_read_tr16_b64_v4i16((LDS_AS s16x4*)p));
}
#define LARRIVE(adr, off) asm volatile("s_mov_b64 exec, 1\n\tds_add_u32 %0, %1 offset:%2\n\ts_mov_b64 exec, -1" : : "v"(adr), "v"(1u), "i"(off) : "memory")
#define LWAIT(adr, off, n) LWAITS(adr, off, n, 1)
#define LWAITS(adr, off, n, sl) do { uint32_t t_; asm volatile("1:\n\tds_read_b32 %0, %1 offset:%2\n\ts_waitcnt lgkmcnt(0)\n\tv_cmp_gt_u32 vcc, %3, %0\n\t" \
      "s_cbranch_vccz 2f\n\ts_sleep " #sl "\n\ts_branch 1b\n2:" : "=&v"(t_) : "v"(adr), "i"(off), "i"(n) : "vcc", "memory"); SB0(); } while (0)
#define RAWBAR() do { asm volatile("s_waitcnt lgkmcnt(0)\n\ts_barrier" ::: "memory"); SB0(); } while (0)
#define PIN9(v) asm volatile("" : "+v"(v[0]), "+v"(v[1]), "+v"(v[2]), "+v"(v[3]), "+v"(v[4]), "+v"(v[5]), "+v"(v[6]), "+v"(v[7]), "+v"(v[8]))
#define LD9(v, o, base) asm volatile("global_load_dword %0, %9, %10\n\tglobal_load_dword %1, %9, %10 offset:128\n\t" \
                 "global_load_dword %2, %9, %10 offset:256\n\tglobal_load_dword %3, %9, %10 offset:384\n\t" \
                 "global_load_dword %4, %9, %10 offset:512\n\tglobal_load_dword %5, %9, %10 offset:640\n\t" \
                 "global_load_dword %6, %9, %10 offset:768\n\tglobal_load_dword %7, %9, %10 offset:896\n\t" \
                 "global_load_dword %8, %9, %10 offset:1024" \
                 : "=&v"(v[0]), "=&v"(v[1]), "=&v"(v[2]), "=&v"(v[3]), "=&v"(v[4]), "=&v"(v[5]), "=&v"(v[6]), "=&v"(v[7]), "=&v"(v[8]) \
                 : "v"(o), "s"(base))
#define KSZ "24000"
__device__ __forceinline__ uint32_t vrun(int p) { return (uint32_t)(((p & 1) * 96 + (p >> 1)) * 64); }
__device__ __forceinline__ int tok0(int q) { return 256 * (q >> 4) + 16 * (q & 12) + 4 * (q & 3); }

#define ENTRY_ASM \
  "s_load_dwordx16 s[36:51], %[ka], 0x0\n\t" \
  "s_load_dwordx16 s[52:67], %[ka], 0x40\n\t" \
  "s_memrealtime s[68:69]\n\t" \
  "v_readfirstlane_b32 %[w], v0\n\t" \
  "v_and_b32 v1, 31, v0\n\t" \
  "v_and_b32 v2, 63, v0\n\t" \
  "s_lshl2_add_u32 s99, %[w], %[b]\n\t" \
  "s_sub_u32 s99, s99, 448\n\t" \
  "s_cmp_ge_u32 s99, 64\n\t" \
  "s_cbranch_scc1 1f\n\t" \
  "s_lshl_b32 s99, s99, 4\n\t" \
  "v_lshl_or_b32 v6, v1, 10, s99\n\t" \
  "v_min_u32 v6, " KSZ ", v6\n\t" \
  "s_getpc_b64 s[98:99]\n\t" \
  "global_load_dword v166, v6, s[98:99]\n\t" \
  "1:\n\t" \
  "s_lshr_b32 s98, %[w], 5\n\t" \
  "s_lshr_b32 %[w], %[w], 6\n\t" \
  "s_lshr_b32 %[c], 0x924924, s98\n\t" \
  "s_and_b32 %[c], %[c], 3\n\t" \
  TSD0 \
  "s_add_u32 %[wk], %[w], %[c]\n\t" \
  "s_and_b32 %[wk], %[wk], 3\n\t" \
  "s_cmp_lt_u32 %[b], 192\n\t" \
  "s_cselect_b32 s98, 0, %[c]\n\t" \
  "s_cmp_lg_u32 s98, 0\n\t" \
  "s_cbranch_scc1 50f\n\t" \
  "s_mul_i32 %[d], %[c], 192\n\t" \
  "s_add_u32 %[d], %[d], %[b]\n\t" \
  "s_lshl2_add_u32 s98, %[d], %[wk]\n\t" \
  "s_lshl_b32 s98, s98, 6\n\t" \
  "v_lshl_add_u32 v3, v1, 1, s98\n\t" \
  "v_mov_b64 v[26:27], 0\n\t" \
  "v_mov_b32 v28, 0x42800000\n\t" \
  "v_mov_b32 v29, 0\n\t" \
  "v_mov_b32 v30, 1.0\n\t" \
  "v_mov_b32 v31, 0\n\t" \
  "s_lshl_b32 %[av], %[d], 1\n\t" \
  "s_and_b32 %[av], %[av], -4\n\t" \
  "v_mov_b64 v[36:37], 0\n\t" \
  "v_mov_b64 v[38:39], 0\n\t" \
  "s_mul_i32 %[chb], %[c], 13024\n\t" \
  "s_add_u32 %[chb], %[chb], 119744\n\t" \
  "s_lshl_b32 s96, %[wk], 10\n\t" \
  "s_add_u32 s98, s96, %[chb]\n\t" \
  "s_addk_i32 s98, -9696\n\t" \
  "v_lshl_add_u32 v5, v2, 4, s98\n\t" \
  "ds_write_b128 v5, v[36:39] offset:4096\n\t" \
  "ds_write_b128 v5, v[36:39] offset:8192\n\t" \
  "s_cmp_lg_u32 %[wk], 3\n\t" \
  "s_cbranch_scc1 1f\n\t" \
  "ds_write_b128 v5, v[36:39]\n\t" \
  "1:\n\t" \
  "s_add_u32 s96, s96, 152480\n\t" \
  "v_lshl_add_u32 v5, v2, 4, s96\n\t" \
  "s_cmp_eq_u32 %[c], 0\n\t" \
  "s_cbranch_scc1 7f\n\t" \
  "s_cmp_eq_u32 %[c], 2\n\t" \
  "s_cbranch_scc1 3f\n\t" \
  "s_setprio " P1 "\n\t" \
  "s_mul_i32 s99, %[wk], 576\n\t" \
  "v_lshrrev_b32 v6, 4, v2\n\t" \
  "v_and_b32 v7, 15, v0\n\t" \
  "v_lshl_or_b32 v7, v7, 2, %[wk]\n\t" \
  "v_min_u32 v7, 31, v7\n\t" \
  "v_lshlrev_b32 v8, 1, v6\n\t" \
  "v_sub_u32 v6, 22, v6\n\t" \
  "s_mov_b64 exec, 0xffff\n\t" \
  "v_mov_b32 v6, 26\n\t" \
  "s_mov_b64 exec, -1\n\t" \
  "v_lshlrev_b32 v8, v8, 8\n\t" \
  "v_add_lshl_u32 v8, v8, -1, v6\n\t" \
  "v_lshrrev_b32 v8, v7, v8\n\t" \
  "v_and_b32 v6, 3, v8\n\t" \
  "v_bfe_u32 v7, v8, 2, 2\n\t" \
  "v_mul_u32_u24 v6, 0x8001, v6\n\t" \
  "v_mul_u32_u24 v7, 0x8001, v7\n\t" \
  "v_and_b32 v6, 0x10001, v6\n\t" \
  "v_and_b32 v7, 0x10001, v7\n\t" \
  "v_mul_u32_u24 v6, 0x3c00, v6\n\t" \
  "v_mul_u32_u24 v7, 0x3c00, v7\n\t" \
  "v_lshl_add_u32 v8, v2, 3, s99\n\t" \
  "ds_write_b64 v8, v[6:7] offset:512\n\t" \
  "s_cmp_lg_u32 %[wk], 0\n\t" \
  "s_cbranch_scc1 1f\n\t" \
  "ds_write_b64 v8, v[36:37]\n\t" \
  "1:\n\t" \
  "s_cmp_lg_u32 %[wk], 2\n\t" \
  "s_cbranch_scc1 8f\n\t" \
  "v_and_b32 v6, 48, v0\n\t" \
  "v_and_b32 v7, 15, v0\n\t" \
  "v_lshl_add_u32 v36, v7, 5, v6\n\t" \
  "v_add_u32 v36, 2112, v36\n\t" \
  "v_and_b32 v7, 12, v0\n\t" \
  "v_lshlrev_b32 v7, 1, v7\n\t" \
  "v_sub_u32 v7, v6, v7\n\t" \
  "v_bitop3_b32 v8, v0, 3, v0 bitop3:0xc\n\t" \
  "s_movk_i32 s98, -2112\n\t" \
  "v_mad_i32_i24 v37, v8, s98, v7\n\t" \
  "v_lshrrev_b32 v6, 1, v6\n\t" \
  "v_sub_u32 v38, v37, v6\n\t" \
  "s_movk_i32 s98, 576\n\t" \
  "v_mad_u32_u24 v39, v8, s98, v7\n\t" \
  "v_add_u32 v39, 536, v39\n\t" \
  "ds_write_b128 v5, v[36:39] offset:3072\n\t" \
  "s_branch 8f\n\t" \
  "3:\n\t" \
  "s_setprio " P2 "\n\t" \
  "s_and_b32 s98, %[wk], 1\n\t" \
  "s_mulk_i32 s98, 4608\n\t" \
  "s_lshl_b32 s99, %[wk], 4\n\t" \
  "s_and_b32 s99, s99, 32\n\t" \
  "v_and_b32 v6, 15, v0\n\t" \
  "v_and_or_b32 v7, v0, 48, s98\n\t" \
  "s_movk_i32 s98, 288\n\t" \
  "v_mad_u32_u24 v36, v6, s98, v7\n\t" \
  "v_and_b32 v6, 47, v0\n\t" \
  "v_lshl_or_b32 v6, v6, 1, s99\n\t" \
  "v_bfe_u32 v7, v0, 4, 1\n\t" \
  "v_or_b32 v7, v7, v6\n\t" \
  "s_bitcmp1_b32 %[wk], 0\n\t" \
  "s_cbranch_scc1 5f\n\t" \
  "v_lshlrev_b32 v7, 3, v7\n\t" \
  "v_sub_u32 v37, -5198, v7\n\t" \
  "v_mov_b32 v38, v37\n\t" \
  "s_mov_b32 exec_lo, 0\n\t" \
  "v_add_u32 v38, 8, v37\n\t" \
  "s_cmp_lg_u32 %[wk], 0\n\t" \
  "s_cbranch_scc1 6f\n\t" \
  "s_mov_b32 exec_hi, 1\n\t" \
  "v_mov_b32 v38, -4270\n\t" \
  "s_branch 6f\n\t" \
  "5:\n\t" \
  "v_sub_u32 v7, 192, v7\n\t" \
  "v_and_b32 v8, 3, v7\n\t" \
  "v_lshrrev_b32 v7, 2, v7\n\t" \
  "v_mul_u32_u24 v8, 48, v8\n\t" \
  "v_add_lshl_u32 v37, v7, v8, 1\n\t" \
  "v_add_u32 v37, -5158, v37\n\t" \
  "v_lshrrev_b32 v6, 1, v6\n\t" \
  "v_and_b32 v8, 1, v0\n\t" \
  "v_mul_u32_u24 v8, 159, v8\n\t" \
  "v_sub_u32 v8, v8, v6\n\t" \
  "v_add_u32 v39, -4678, v8\n\t" \
  "v_add_u32 v38, -4614, v8\n\t" \
  "s_mov_b32 exec_lo, 0xffff0000\n\t" \
  "s_mov_b32 exec_hi, 0xffff0000\n\t" \
  "v_mov_b32 v38, -4270\n\t" \
  "v_mov_b32 v39, -4270\n\t" \
  "s_cmp_lg_u32 %[wk], 1\n\t" \
  "s_cbranch_scc1 6f\n\t" \
  "s_mov_b64 exec, 1\n\t" \
  "v_mov_b32 v37, -4270\n\t" \
  "v_mov_b32 v38, -4270\n\t" \
  "s_mov_b64 exec, -1\n\t" \
  "v_and_b32 v6, 3, v0\n\t" \
  "v_lshl_add_u32 v7, v6, 1, 54\n\t" \
  "s_mov_b32 exec_lo, 0x0f0f0f0f\n\t" \
  "v_add_u32 v7, 68, v7\n\t" \
  "s_mov_b32 exec_lo, 0x40404040\n\t" \
  "v_mov_b32 v7, 1216\n\t" \
  "s_mov_b32 exec_lo, 0x80808080\n\t" \
  "v_mov_b32 v7, 1600\n\t" \
  "s_mov_b32 exec_lo, -1\n\t" \
  "v_lshrrev_b32 v6, 3, v2\n\t" \
  "s_movk_i32 s98, -2114\n\t" \
  "v_mad_i32_i24 v7, v6, s98, v7\n\t" \
  "ds_write_b32 v5, v7 offset:3072\n\t" \
  "6:\n\t" \
  "s_mov_b64 exec, -1\n\t" \
  "ds_write_b128 v5, v[36:39]\n\t" \
  "s_branch 8f\n\t" \
  "7:\n\t" \
  "s_setprio " PN "\n\t" \
  "s_lshl2_add_u32 s98, %[b], %[wk]\n\t" \
  "s_mulk_i32 s98, 2304\n\t" \
  "v_lshl_add_u32 v4, v1, 2, s98\n\t" \
  "v_and_b32 v6, 32, v0\n\t" \
  "v_mad_u32_u24 v4, v6, 36, v4\n\t" \
  "s_waitcnt lgkmcnt(0)\n\t" \
  "global_load_dword v16, v4, s[36:37] offset:0\n\t" \
  "global_load_dword v17, v4, s[36:37] offset:128\n\t" \
  "global_load_dword v18, v4, s[36:37] offset:256\n\t" \
  "global_load_dword v19, v4, s[36:37] offset:384\n\t" \
  "global_load_dword v20, v4, s[36:37] offset:512\n\t" \
  "global_load_dword v21, v4, s[36:37] offset:640\n\t" \
  "global_load_dword v22, v4, s[36:37] offset:768\n\t" \
  "global_load_dword v23, v4, s[36:37] offset:896\n\t" \
  "global_load_dword v24, v4, s[36:37] offset:1024\n\t" \
  "s_lshl_b32 s98, %[wk], 18\n\t" \
  "s_lshl_b32 s99, %[b], 6\n\t" \
  "s_add_u32 s98, s98, s99\n\t" \
  "v_and_b32 v6, 3, v0\n\t" \
  "v_lshl_add_u32 v12, v6, 4, s98\n\t" \
  "v_lshrrev_b32 v7, 2, v2\n\t" \
  "v_lshl_add_u32 v12, v7, 14, v12\n\t" \
  "v_lshrrev_b32 v6, 3, v2\n\t" \
  "v_xor_b32 v14, v6, v2\n\t" \
  "v_and_or_b32 v7, v6, 4, %[wk]\n\t" \
  "v_and_b32 v8, 7, v0\n\t" \
  "v_xor_b32 v7, v7, v8\n\t" \
  "v_lshl_or_b32 v7, v8, 3, v7\n\t" \
  "v_and_b32 v6, 3, v6\n\t" \
  "v_mul_u32_u24 v6, 576, v6\n\t" \
  "v_add_lshl_u32 v13, v6, v7, 4\n\t" \
  "s_mul_i32 s98, %[wk], 9216\n\t" \
  "s_add_u32 s98, s98, 2816\n\t" \
  "v_lshl_add_u32 v14, v14, 4, s98\n\t" \
  "v_add_u32 v13, 2816, v13\n\t" \
  "8:\n\t" \
  "s_cmp_gt_u32 %[b], 191\n\t" \
  "s_waitcnt lgkmcnt(0)\n\t" \
  "s_cbranch_scc1 2f\n\t" \
  "global_load_short_d16_hi v26, v3, s[38:39]\n\t" \
  "global_load_short_d16_hi v27, v3, s[40:41]\n\t" \
  "s_mov_b32 exec_lo, 0\n\t" \
  "global_load_short_d16_hi v28, v3, s[42:43]\n\t" \
  "global_load_short_d16_hi v29, v3, s[44:45]\n\t" \
  "global_load_short_d16_hi v30, v3, s[46:47]\n\t" \
  "global_load_short_d16_hi v31, v3, s[48:49]\n\t" \
  "s_mov_b32 exec_lo, -1\n\t" \
  "2:\n\t" \
  "s_load_dword %[sdt], s[50:51], %[av]\n\t" \
  "s_load_dword %[sd], s[52:53], %[av]\n\t" \
  "s_load_dword %[ep], s[54:55], 0x0\n\t" \
  "s_lshl_b32 s98, %[w], 2\n\t" \
  "s_mov_b32 s72, " STR(TSLOT_LO) "\n\t" \
  "s_mov_b32 s73, " STR(TSLOT_HI) "\n\t" \
  "s_lshr_b64 s[72:73], s[72:73], s98\n\t" \
  "s_and_b32 s99, s72, 15\n\t" \
  "s_mov_b32 s72, " STR(TWHEN_LO) "\n\t" \
  "s_mov_b32 s73, " STR(TWHEN_HI) "\n\t" \
  "s_lshr_b64 s[72:73], s[72:73], s98\n\t" \
  "s_and_b32 s74, s72, 15\n\t" \
  "s_mulk_i32 s99, 24\n\t" \
  "s_lshr_b32 s98, %[b], 3\n\t" \
  "s_add_u32 s99, s99, s98\n\t" \
  "s_add_u32 s98, s99, 8*" STR(KOFF) "-27\n\t" \
  "s_cmp_lt_u32 s99, 27\n\t" \
  "s_cselect_b32 s96, 8, s98\n\t" \
  "s_cselect_b32 s98, 0, 3\n\t" \
  "s_cselect_b32 s97, 15, 7\n\t" \
  "s_cselect_b32 s99, s99, s96\n\t" \
  "s_lshr_b32 s99, s99, s98\n\t" \
  "s_min_u32 s99, s99, 26\n\t" \
  "s_and_b32 s96, s96, s97\n\t" \
  "s_and_b32 s97, %[b], 7\n\t" \
  "s_lshl3_add_u32 s96, s96, s97\n\t" \
  "s_sub_u32 s98, s99, 10\n\t" \
  "s_cmp_lt_u32 s98, 9\n\t" \
  "s_cbranch_scc1 1f\n\t" \
  "s_bitcmp1_b32 s99, 0\n\t" \
  "s_cselect_b64 s[72:73], s[58:59], s[60:61]\n\t" \
  "s_lshl_b32 s96, s96, 7\n\t" \
  "s_sub_u32 s98, 26, s99\n\t" \
  "s_add_u32 s97, s99, 8\n\t" \
  "s_cmp_lt_u32 s99, 10\n\t" \
  "s_cselect_b32 s98, s97, s98\n\t" \
  "s_lshr_b32 s98, s98, 1\n\t" \
  "s_cmp_eq_u32 s98, 8\n\t" \
  "s_cselect_b32 s98, 64, s98\n\t" \
  "s_cselect_b32 s97, 0, 56\n\t" \
  "s_mul_i32 s98, s98, 73728\n\t" \
  "v_lshrrev_b32 v165, 3, v2\n\t" \
  "v_mad_u32_u24 v165, v165, s97, v2\n\t" \
  "v_mul_u32_u24 v165, 9216, v165\n\t" \
  "s_branch 2f\n\t" \
  "1:\n\t" \
  "s_mov_b64 s[72:73], s[62:63]\n\t" \
  "s_lshl_b32 s98, s98, 13\n\t" \
  "s_mul_i32 s96, s96, 73728\n\t" \
  "v_lshlrev_b32 v165, 7, v2\n\t" \
  "2:\n\t" \
  "s_add_u32 s98, s98, s96\n\t" \
  "s_add_u32 s72, s72, s98\n\t" \
  "s_addc_u32 s73, s73, 0\n\t" \
  "s_cmp_gt_u32 %[b], 191\n\t" \
  "s_cselect_b32 s74, 15, s74\n\t" \
  "s_barrier\n\t" \
  "s_cmp_lg_u32 s74, 0\n\t" \
  "s_cbranch_scc1 1f\n\t" \
  "global_load_dword v166, v165, s[72:73]" TNT "\n\t" \
  "1:\n\t" \
  "ds_read_b128 v[36:39], v5\n\t" \
  "ds_read_b32 v35, v5 offset:3072\n\t" \
  "v_mov_b32 v32, 0x3fb8aa3b\n\t" \
  "v_mov_b32 v33, 0.15915494\n\t" \
  "v_mov_b32 v34, 0x43800000\n\t" \
  "s_mov_b32 exec_lo, 0\n\t" \
  "v_mov_b32 v32, 0x4138aa3b\n\t" \
  "v_mov_b32 v33, 0xbfa2f983\n\t" \
  "v_mov_b32 v34, 0x41000000\n\t" \
  "s_mov_b32 exec_lo, -1\n\t" \
  "s_and_b32 s70, %[id], %[rot]\n\t" \
  "s_lshl_b32 s70, s70, 24\n\t" \
  "s_add_u32 s70, s70, " STR(WSOFS) "\n\t" \
  "s_add_u32 s70, s70, s54\n\t" \
  "s_addc_u32 s71, s55, 0\n\t" \
  "s_mul_i32 %[opc], %[c], 36864\n\t" \
  "s_add_u32 %[opc], %[opc], 2816\n\t" \
  "s_mul_i32 %[tag], %[id], 0x5bd1e996\n\t" \
  "v_lshlrev_b32 v11, 2, %[c]\n\t" \
  "s_mul_i32 %[m0v], %[wk], 9216\n\t" \
  "s_add_u32 %[m0v], %[m0v], %[opc]\n\t" \
  "s_lshr_b32 s96, %[wk], 1\n\t" \
  "s_and_b32 s97, %[wk], 1\n\t" \
  "s_sub_u32 s96, s96, s97\n\t" \
  "s_mulk_i32 s96, 4608\n\t" \
  "s_add_u32 s96, s96, %[opc]\n\t" \
  "s_lshl_b32 s97, %[d], 4\n\t" \
  "s_mov_b32 %[av], " AV0 "\n\t" \
  "v_mov_b32 v15, 0\n\t" \
  "v_mov_b32 v6, 0x3fb8aa3b\n\t" \
  "s_waitcnt lgkmcnt(0)\n\t" \
  "s_lshr_b32 %[sdt], %[sdt], s97\n\t" \
  "s_lshl_b32 %[sdt], %[sdt], 16\n\t" \
  "v_mul_f32 v6, %[sdt], v6\n\t" \
  "s_add_u32 %[tag], %[tag], %[ep]\n\t" \
  "v_exp_f32 v6, v6\n\t" \
  "s_lshl_b32 %[tag], %[tag], 1\n\t" \
  "v_add_u32 v10, s96, v36\n\t" \
  "s_bitcmp0_b32 %[tag], 16\n\t" \
  "v_cvt_pk_bf16_f32 v6, 0, v6\n\t" \
  "s_addc_u32 %[tag], %[tag], 0\n\t" \
  "v_add_u32 v36, %[opc], v36\n\t" \
  "s_lshr_b32 %[sd], %[sd], s97\n\t" \
  "v_pk_mul_f32 v[32:33], v[32:33], v[6:7] op_sel_hi:[1,0]\n\t" \
  "s_lshl_b32 %[sd], %[sd], 16\n\t" \
  "v_readfirstlane_b32 %[sdt], v6\n\t" \
  "v_pk_mul_f32 v[8:9], v[32:33], v[34:35] op_sel_hi:[1,0]\n\t" \
  "v_add_u32 v37, %[chb], v37\n\t" \
  "v_add_u32 v38, %[chb], v38\n\t" \
  "v_add_u32 v39, %[chb], v39\n\t" \
  "v_add_u32 v35, %[chb], v35\n\t" \
  "s_cmp_lg_u32 %[wk], 0\n\t" \
  "s_cbranch_scc1 1f\n\t" \
  "s_mov_b64 exec, 1\n\t" \
  "v_rcp_f32 v6, v6\n\t" \
  "s_add_u32 s96, %[sdt], 0xf0000000\n\t" \
  "v_mul_f32 v6, %[sd], v6\n\t" \
  "v_mul_f32 v15, 0x42800000, v6\n\t" \
  "v_and_b32 v6, 0x7fffffff, v15\n\t" \
  "v_cmp_lt_u32 vcc, 0x46800000, v6\n\t" \
  "s_mov_b64 exec, -1\n\t" \
  "s_cmp_ge_u32 s96, 0x60000000\n\t" \
  "s_cselect_b32 %[av], 0x101, %[av]\n\t" \
  "s_cmp_lg_u32 vcc_lo, 0\n\t" \
  "s_cselect_b32 %[av], 0x101, %[av]\n\t" \
  "1:\n\t" \
  "s_cmp_lg_u32 %[c], 0\n\t" \
  "s_cbranch_scc1 60f\n\t" \
  "s_mov_b32 s96, 0x3f803f80\n\t" \
  "v_mov_b32 v63, %[tag]\n\t" \
  "v_mov_b32 v67, %[tag]\n\t" \
  "v_mov_b32 v71, %[tag]\n\t" \
  "v_mov_b32 v87, 1\n\t" \
  "v_add_u32 v85, 64*16384, v12\n\t" \
  "v_add_u32 v86, 128*16384, v12\n\t" \
  "s_cmp_lt_u32 %[b], 192\n\t" \
  "s_cbranch_scc1 1f\n\t" \
  "s_waitcnt vmcnt(0)\n\t" \
  "1:\n\t" \
  "s_waitcnt vmcnt(6)\n\t" \
  TSQ(1) \
  "v_dot2_f32_bf16 v72, v16, s96, 0\n\t" \
  "v_dot2_f32_bf16 v73, v16, v16, 0\n\t" \
  "v_dot2_f32_bf16 v72, v17, s96, v72\n\t" \
  "v_dot2_f32_bf16 v73, v17, v17, v73\n\t" \
  "v_dot2_f32_bf16 v72, v18, s96, v72\n\t" \
  "v_dot2_f32_bf16 v73, v18, v18, v73\n\t" \
  "v_dot2_f32_bf16 v72, v19, s96, v72\n\t" \
  "v_dot2_f32_bf16 v73, v19, v19, v73\n\t" \
  "v_dot2_f32_bf16 v72, v20, s96, v72\n\t" \
  "v_dot2_f32_bf16 v73, v20, v20, v73\n\t" \
  "v_dot2_f32_bf16 v72, v21, s96, v72\n\t" \
  "v_dot2_f32_bf16 v73, v21, v21, v73\n\t" \
  "v_dot2_f32_bf16 v72, v22, s96, v72\n\t" \
  "v_dot2_f32_bf16 v73, v22, v22, v73\n\t" \
  "v_dot2_f32_bf16 v72, v23, s96, v72\n\t" \
  "v_dot2_f32_bf16 v73, v23, v23, v73\n\t" \
  "v_dot2_f32_bf16 v72, v24, s96, v72\n\t" \
  "v_dot2_f32_bf16 v73, v24, v24, v73\n\t" \
  "v_lshlrev_b32 v40, 16, v16\n\t" \
  "v_and_b32 v41, 0xffff0000, v16\n\t" \
  "v_lshlrev_b32 v46, 16, v19\n\t" \
  "v_permlane16_swap_b32 v72, v73\n\t" \
  "v_add_f32 v72, v72, v73\n\t" \
  "v_and_b32 v47, 0xffff0000, v19\n\t" \
  "v_permlane32_swap_b32 v40, v41\n\t" \
  "v_add_f32_dpp v72, v72, v72 quad_perm:[1,0,3,2] row_mask:0xf bank_mask:0xf bound_ctrl:1\n\t" \
  "v_lshlrev_b32 v52, 16, v22\n\t" \
  "v_and_b32 v53, 0xffff0000, v22\n\t" \
  "v_add_f32_dpp v72, v72, v72 quad_perm:[2,3,0,1] row_mask:0xf bank_mask:0xf bound_ctrl:1\n\t" \
  "v_permlane32_swap_b32 v46, v47\n\t" \
  "v_lshlrev_b32 v42, 16, v17\n\t" \
  "v_add_f32_dpp v72, v72, v72 row_half_mirror row_mask:0xf bank_mask:0xf bound_ctrl:1\n\t" \
  "v_and_b32 v43, 0xffff0000, v17\n\t" \
  "v_permlane32_swap_b32 v52, v53\n\t" \
  "v_add_f32_dpp v72, v72, v72 row_mirror row_mask:0xf bank_mask:0xf bound_ctrl:1\n\t" \
  "v_mov_b32 v73, v72\n\t" \
  "v_lshlrev_b32 v48, 16, v20\n\t" \
  "v_and_b32 v49, 0xffff0000, v20\n\t" \
  "v_permlane16_swap_b32 v72, v73\n\t" \
  "v_permlane32_swap_b32 v42, v43\n\t" \
  "v_mul_f32 v74, 0x3ae38e39, v72\n\t" \
  "v_lshlrev_b32 v54, 16, v23\n\t" \
  "v_mul_f32 v75, v74, -v74\n\t" \
  "v_and_b32 v55, 0xffff0000, v23\n\t" \
  "v_fmamk_f32 v76, v73, 0x3ae38e39, v75\n\t" \
  "v_permlane32_swap_b32 v48, v49\n\t" \
  "v_fmamk_f32 v77, v76, 0x42800000, v75\n\t" \
  "v_cvt_pk_bf16_f32 v76, 0, v76\n\t" \
  "v_cmp_nle_f32 vcc, 0, v77\n\t" \
  "v_add_f32 v76, 0x3727c5ac, v76\n\t" \
  "v_cvt_pk_bf16_f32 v78, 0, v74\n\t" \
  "v_cvt_pk_bf16_f32 v76, 0, v76\n\t" \
  "v_lshlrev_b32 v44, 16, v18\n\t" \
  "v_sqrt_f32 v76, v76\n\t" \
  "v_and_b32 v45, 0xffff0000, v18\n\t" \
  "v_permlane32_swap_b32 v54, v55\n\t" \
  "v_cvt_pk_bf16_f32 v76, 0, v76\n\t" \
  "v_lshlrev_b32 v50, 16, v21\n\t" \
  "v_rcp_f32 v76, v76\n\t" \
  "v_and_b32 v51, 0xffff0000, v21\n\t" \
  "v_permlane32_swap_b32 v44, v45\n\t" \
  "v_lshlrev_b32 v56, 16, v24\n\t" \
  "v_and_b32 v57, 0xffff0000, v24\n\t" \
  "v_permlane32_swap_b32 v50, v51\n\t" \
  "s_cbranch_vccnz 5f\n\t" \
  "6:\n\t" \
  "v_mul_f32 v80, 0x43000000, v76\n\t" \
  "v_mul_f32 v81, 0x43000000, v76\n\t" \
  "v_mul_f32 v82, v78, -v80\n\t" \
  "v_mul_f32 v83, v78, -v80\n\t" \
  "v_permlane32_swap_b32 v80, v81\n\t" \
  "v_permlane32_swap_b32 v56, v57\n\t" \
  "v_permlane32_swap_b32 v82, v83\n\t" \
  "v_pk_fma_f32 v[40:41], v[40:41], v[80:81], v[82:83]\n\t" \
  "v_pk_fma_f32 v[46:47], v[46:47], v[80:81], v[82:83]\n\t" \
  "v_pk_fma_f32 v[52:53], v[52:53], v[80:81], v[82:83]\n\t" \
  "v_cvt_pk_f16_f32 v60, v40, v41\n\t" \
  "v_cvt_pk_f16_f32 v61, v46, v47\n\t" \
  "v_cvt_pk_f16_f32 v62, v52, v53\n\t" \
  "v_pk_fma_f32 v[42:43], v[42:43], v[80:81], v[82:83]\n\t" \
  "v_pk_fma_f32 v[48:49], v[48:49], v[80:81], v[82:83]\n\t" \
  "v_pk_fma_f32 v[54:55], v[54:55], v[80:81], v[82:83]\n\t" \
  "ds_write_b128 v13, v[60:63] offset:0\n\t" \
  "v_cvt_pk_f16_f32 v64, v42, v43\n\t" \
  "v_cvt_pk_f16_f32 v65, v48, v49\n\t" \
  "v_cvt_pk_f16_f32 v66, v54, v55\n\t" \
  "v_pk_fma_f32 v[44:45], v[44:45], v[80:81], v[82:83]\n\t" \
  "v_pk_fma_f32 v[50:51], v[50:51], v[80:81], v[82:83]\n\t" \
  "v_pk_fma_f32 v[56:57], v[56:57], v[80:81], v[82:83]\n\t" \
  "ds_write_b128 v13, v[64:67] offset:1024\n\t" \
  "v_cvt_pk_f16_f32 v68, v44, v45\n\t" \
  "v_cvt_pk_f16_f32 v69, v50, v51\n\t" \
  "v_cvt_pk_f16_f32 v70, v56, v57\n\t" \
  "ds_write_b128 v13, v[68:71] offset:2048\n\t" \
  "s_mov_b64 exec, 1\n\t" \
  "ds_add_u32 v11, v87 offset:16\n\t" \
  "s_mov_b64 exec, -1\n\t" \
  "s_setprio 3\n\t" \
  "2:\n\t" \
  "ds_read_b32 v84, v11 offset:16\n\t" \
  "ds_read_b128 v[40:43], v14 offset:0\n\t" \
  "ds_read_b128 v[44:47], v14 offset:1024\n\t" \
  "ds_read_b128 v[48:51], v14 offset:2048\n\t" \
  "s_waitcnt lgkmcnt(3)\n\t" \
  "v_cmp_gt_u32 vcc, 4, v84\n\t" \
  "s_cbranch_vccnz 2b\n\t" \
  "s_waitcnt lgkmcnt(2)\n\t" \
  "global_store_dwordx4 v12, v[40:43], s[70:71] sc1\n\t" \
  "s_waitcnt lgkmcnt(1)\n\t" \
  "global_store_dwordx4 v85, v[44:47], s[70:71] sc1\n\t" \
  "s_waitcnt lgkmcnt(0)\n\t" \
  "global_store_dwordx4 v86, v[48:51], s[70:71] sc1\n\t" \
  "s_setprio " P0 "\n\t" \
  TSQ(3) \
  "s_waitcnt vmcnt(3)\n\t" \
  "s_branch 70f\n\t" \
  "5:\n\t" \
  "v_mov_b32 v88, 0\n\t" \
  "v_mov_b32 v89, 0\n\t" \
  "v_lshlrev_b32 v90, 16, v16\n\t" \
  "v_and_b32 v91, 0xffff0000, v16\n\t" \
  "v_sub_f32 v90, v90, v78\n\t" \
  "v_sub_f32 v91, v91, v78\n\t" \
  "v_pk_fma_f32 v[88:89], v[90:91], v[90:91], v[88:89]\n\t" \
  "v_lshlrev_b32 v90, 16, v17\n\t" \
  "v_and_b32 v91, 0xffff0000, v17\n\t" \
  "v_sub_f32 v90, v90, v78\n\t" \
  "v_sub_f32 v91, v91, v78\n\t" \
  "v_pk_fma_f32 v[88:89], v[90:91], v[90:91], v[88:89]\n\t" \
  "v_lshlrev_b32 v90, 16, v18\n\t" \
  "v_and_b32 v91, 0xffff0000, v18\n\t" \
  "v_sub_f32 v90, v90, v78\n\t" \
  "v_sub_f32 v91, v91, v78\n\t" \
  "v_pk_fma_f32 v[88:89], v[90:91], v[90:91], v[88:89]\n\t" \
  "v_lshlrev_b32 v90, 16, v19\n\t" \
  "v_and_b32 v91, 0xffff0000, v19\n\t" \
  "v_sub_f32 v90, v90, v78\n\t" \
  "v_sub_f32 v91, v91, v78\n\t" \
  "v_pk_fma_f32 v[88:89], v[90:91], v[90:91], v[88:89]\n\t" \
  "v_lshlrev_b32 v90, 16, v20\n\t" \
  "v_and_b32 v91, 0xffff0000, v20\n\t" \
  "v_sub_f32 v90, v90, v78\n\t" \
  "v_sub_f32 v91, v91, v78\n\t" \
  "v_pk_fma_f32 v[88:89], v[90:91], v[90:91], v[88:89]\n\t" \
  "v_lshlrev_b32 v90, 16, v21\n\t" \
  "v_and_b32 v91, 0xffff0000, v21\n\t" \
  "v_sub_f32 v90, v90, v78\n\t" \
  "v_sub_f32 v91, v91, v78\n\t" \
  "v_pk_fma_f32 v[88:89], v[90:91], v[90:91], v[88:89]\n\t" \
  "v_lshlrev_b32 v90, 16, v22\n\t" \
  "v_and_b32 v91, 0xffff0000, v22\n\t" \
  "v_sub_f32 v90, v90, v78\n\t" \
  "v_sub_f32 v91, v91, v78\n\t" \
  "v_pk_fma_f32 v[88:89], v[90:91], v[90:91], v[88:89]\n\t" \
  "v_lshlrev_b32 v90, 16, v23\n\t" \
  "v_and_b32 v91, 0xffff0000, v23\n\t" \
  "v_sub_f32 v90, v90, v78\n\t" \
  "v_sub_f32 v91, v91, v78\n\t" \
  "v_pk_fma_f32 v[88:89], v[90:91], v[90:91], v[88:89]\n\t" \
  "v_lshlrev_b32 v90, 16, v24\n\t" \
  "v_and_b32 v91, 0xffff0000, v24\n\t" \
  "v_sub_f32 v90, v90, v78\n\t" \
  "v_sub_f32 v91, v91, v78\n\t" \
  "v_pk_fma_f32 v[88:89], v[90:91], v[90:91], v[88:89]\n\t" \
  "v_add_f32 v88, v88, v89\n\t" \
  "s_nop 1\n\t" \
  "v_add_f32_dpp v88, v88, v88 quad_perm:[1,0,3,2] row_mask:0xf bank_mask:0xf bound_ctrl:1\n\t" \
  "s_nop 1\n\t" \
  "v_add_f32_dpp v88, v88, v88 quad_perm:[2,3,0,1] row_mask:0xf bank_mask:0xf bound_ctrl:1\n\t" \
  "s_nop 1\n\t" \
  "v_add_f32_dpp v88, v88, v88 row_half_mirror row_mask:0xf bank_mask:0xf bound_ctrl:1\n\t" \
  "s_nop 1\n\t" \
  "v_add_f32_dpp v88, v88, v88 row_mirror row_mask:0xf bank_mask:0xf bound_ctrl:1\n\t" \
  "v_mov_b32 v89, v88\n\t" \
  "s_nop 1\n\t" \
  "v_permlane16_swap_b32 v88, v89\n\t" \
  "v_add_f32 v88, v88, v89\n\t" \
  "v_sub_f32 v90, v74, v78\n\t" \
  "v_mul_f32 v90, v90, -v90\n\t" \
  "v_fmamk_f32 v76, v88, 0x3ae38e39, v90\n\t" \
  "v_cmp_ngt_f32 vcc, 0, v76\n\t" \
  "s_nop 1\n\t" \
  "v_cndmask_b32 v76, 0, v76, vcc\n\t" \
  "v_cvt_pk_bf16_f32 v76, 0, v76\n\t" \
  "v_add_f32 v76, 0x3727c5ac, v76\n\t" \
  "v_cvt_pk_bf16_f32 v76, 0, v76\n\t" \
  "v_sqrt_f32 v76, v76\n\t" \
  "s_nop 0\n\t" \
  "v_cvt_pk_bf16_f32 v76, 0, v76\n\t" \
  "v_rcp_f32 v76, v76\n\t" \
  "s_branch 6b\n\t" \
  "50:\n\t" \
  "v_mov_b32 v6, 0\n\t" \
  "ds_write_b32 v6, v6 offset:16\n\t" \
  "s_waitcnt lgkmcnt(0)\n\t" \
  "s_and_b32 s70, %[id], %[rot]\n\t" \
  "s_lshl_b32 s70, s70, 24\n\t" \
  "s_add_u32 s70, s70, " STR(WSOFS) "\n\t" \
  "s_add_u32 s70, s70, s54\n\t" \
  "s_addc_u32 s71, s55, 0\n\t" \
  "s_mul_i32 %[opc], %[c], 36864\n\t" \
  "s_add_u32 %[opc], %[opc], 2816\n\t" \
  "s_mul_i32 %[tag], %[id], 0x5bd1e996\n\t" \
  "v_lshlrev_b32 v11, 2, %[c]\n\t" \
  "s_barrier\n\t" \
  "s_branch 70f\n\t" \
  "60:\n\t" \
  "s_cmp_eq_u32 s74, 0\n\t" \
  "s_cbranch_scc1 1f\n\t" \
  "s_waitcnt vmcnt(0)\n\t" \
  "1:\n\t" \
  "s_waitcnt vmcnt(1)\n\t" \
  "70:\n\t"

#define LN2_ASM \
  "s_mov_b32 s96, 0x3f803f80\n\t" \
  "s_mov_b32 s97, 0x40000\n\t" \
  "ds_read_b128 v[44:47], %5 offset:0\n\t" \
  "ds_read_b128 v[48:51], %5 offset:6144\n\t" \
  "ds_read_b128 v[52:55], %5 offset:2048\n\t" \
  "ds_read_b128 v[56:59], %5 offset:8192\n\t" \
  "ds_read_b128 v[60:63], %5 offset:4096\n\t" \
  "ds_read_b128 v[64:67], %5 offset:10240\n\t" \
  "v_mov_b32 v107, 1\n\t" \
  "v_mov_b32 v108, 0\n\t" \
  "1:\n\t" \
  "s_waitcnt lgkmcnt(5)\n\t" \
  "v_perm_b32 v68, v108, v44, %14\n\t" \
  "s_waitcnt lgkmcnt(4)\n\t" \
  "v_perm_b32 v69, v108, v48, %14\n\t" \
  "v_perm_b32 v74, v108, v45, %14\n\t" \
  "v_perm_b32 v75, v108, v49, %14\n\t" \
  "v_perm_b32 v80, v108, v46, %14\n\t" \
  "v_perm_b32 v81, v108, v50, %14\n\t" \
  "v_pk_add_f32 v[68:69], v[68:69], v[16:17]\n\t" \
  "v_pk_add_f32 v[74:75], v[74:75], v[22:23]\n\t" \
  "v_pk_add_f32 v[80:81], v[80:81], v[28:29]\n\t" \
  "v_xor_b32 v102, %15, v47\n\t" \
  "v_cvt_pk_bf16_f32 v86, v68, v69\n\t" \
  "v_cvt_pk_bf16_f32 v89, v74, v75\n\t" \
  "v_cvt_pk_bf16_f32 v92, v80, v81\n\t" \
  "v_bitop3_b32 v102, v102, v51, %15 bitop3:0xf6\n\t" \
  "v_dot2_f32_bf16 v96, v86, s96, 0\n\t" \
  "v_dot2_f32_bf16 v97, v86, v86, 0\n\t" \
  "v_dot2_f32_bf16 v98, v86, v34, 0\n\t" \
  "v_dot2_f32_bf16 v96, v89, s96, v96\n\t" \
  "v_dot2_f32_bf16 v97, v89, v89, v97\n\t" \
  "v_dot2_f32_bf16 v98, v89, v37, v98\n\t" \
  "v_dot2_f32_bf16 v96, v92, s96, v96\n\t" \
  "v_dot2_f32_bf16 v97, v92, v92, v97\n\t" \
  "v_dot2_f32_bf16 v98, v92, v40, v98\n\t" \
  "s_waitcnt lgkmcnt(3)\n\t" \
  "v_perm_b32 v70, v108, v52, %14\n\t" \
  "s_waitcnt lgkmcnt(2)\n\t" \
  "v_perm_b32 v71, v108, v56, %14\n\t" \
  "v_perm_b32 v76, v108, v53, %14\n\t" \
  "v_perm_b32 v77, v108, v57, %14\n\t" \
  "v_perm_b32 v82, v108, v54, %14\n\t" \
  "v_perm_b32 v83, v108, v58, %14\n\t" \
  "v_pk_add_f32 v[70:71], v[70:71], v[18:19]\n\t" \
  "v_pk_add_f32 v[76:77], v[76:77], v[24:25]\n\t" \
  "v_pk_add_f32 v[82:83], v[82:83], v[30:31]\n\t" \
  "v_bitop3_b32 v102, v102, v55, %15 bitop3:0xf6\n\t" \
  "v_cvt_pk_bf16_f32 v87, v70, v71\n\t" \
  "v_cvt_pk_bf16_f32 v90, v76, v77\n\t" \
  "v_cvt_pk_bf16_f32 v93, v82, v83\n\t" \
  "v_bitop3_b32 v102, v102, v59, %15 bitop3:0xf6\n\t" \
  "v_dot2_f32_bf16 v96, v87, s96, v96\n\t" \
  "v_dot2_f32_bf16 v97, v87, v87, v97\n\t" \
  "v_dot2_f32_bf16 v98, v87, v35, v98\n\t" \
  "v_dot2_f32_bf16 v96, v90, s96, v96\n\t" \
  "v_dot2_f32_bf16 v97, v90, v90, v97\n\t" \
  "v_dot2_f32_bf16 v98, v90, v38, v98\n\t" \
  "v_dot2_f32_bf16 v96, v93, s96, v96\n\t" \
  "v_dot2_f32_bf16 v97, v93, v93, v97\n\t" \
  "v_dot2_f32_bf16 v98, v93, v41, v98\n\t" \
  "s_waitcnt lgkmcnt(1)\n\t" \
  "v_perm_b32 v72, v108, v60, %14\n\t" \
  "s_waitcnt lgkmcnt(0)\n\t" \
  "v_perm_b32 v73, v108, v64, %14\n\t" \
  "v_perm_b32 v78, v108, v61, %14\n\t" \
  "v_perm_b32 v79, v108, v65, %14\n\t" \
  "v_perm_b32 v84, v108, v62, %14\n\t" \
  "v_perm_b32 v85, v108, v66, %14\n\t" \
  "v_pk_add_f32 v[72:73], v[72:73], v[20:21]\n\t" \
  "v_pk_add_f32 v[78:79], v[78:79], v[26:27]\n\t" \
  "v_pk_add_f32 v[84:85], v[84:85], v[32:33]\n\t" \
  "v_bitop3_b32 v102, v102, v63, %15 bitop3:0xf6\n\t" \
  "v_cvt_pk_bf16_f32 v88, v72, v73\n\t" \
  "v_cvt_pk_bf16_f32 v91, v78, v79\n\t" \
  "v_cvt_pk_bf16_f32 v94, v84, v85\n\t" \
  "v_bitop3_b32 v102, v102, v67, %15 bitop3:0xf6\n\t" \
  "v_dot2_f32_bf16 v96, v88, s96, v96\n\t" \
  "v_dot2_f32_bf16 v97, v88, v88, v97\n\t" \
  "v_dot2_f32_bf16 v98, v88, v36, v98\n\t" \
  "v_dot2_f32_bf16 v96, v91, s96, v96\n\t" \
  "v_dot2_f32_bf16 v97, v91, v91, v97\n\t" \
  "v_dot2_f32_bf16 v98, v91, v39, v98\n\t" \
  "v_dot2_f32_bf16 v96, v94, s96, v96\n\t" \
  "v_dot2_f32_bf16 v97, v94, v94, v97\n\t" \
  "v_dot2_f32_bf16 v98, v94, v42, v98\n\t" \
  "v_cmp_ne_u32 s[98:99], 0, v102\n\t" \
  "s_nop 0\n\t" \
  "v_permlane16_swap_b32 v96, v97\n\t" \
  "v_add_f32 v96, v96, v97\n\t" \
  "v_add_f32_dpp v98, v98, v98 quad_perm:[1,0,3,2] row_mask:0xf bank_mask:0xf bound_ctrl:1\n\t" \
  "s_nop 0\n\t" \
  "v_add_f32_dpp v96, v96, v96 quad_perm:[1,0,3,2] row_mask:0xf bank_mask:0xf bound_ctrl:1\n\t" \
  "v_add_f32_dpp v98, v98, v98 quad_perm:[2,3,0,1] row_mask:0xf bank_mask:0xf bound_ctrl:1\n\t" \
  "s_nop 0\n\t" \
  "v_add_f32_dpp v96, v96, v96 quad_perm:[2,3,0,1] row_mask:0xf bank_mask:0xf bound_ctrl:1\n\t" \
  "v_add_f32_dpp v98, v98, v98 row_half_mirror row_mask:0xf bank_mask:0xf bound_ctrl:1\n\t" \
  "s_nop 0\n\t" \
  "v_add_f32_dpp v96, v96, v96 row_half_mirror row_mask:0xf bank_mask:0xf bound_ctrl:1\n\t" \
  "v_add_f32_dpp v98, v98, v98 row_mirror row_mask:0xf bank_mask:0xf bound_ctrl:1\n\t" \
  "s_nop 0\n\t" \
  "v_add_f32_dpp v96, v96, v96 row_mirror row_mask:0xf bank_mask:0xf bound_ctrl:1\n\t" \
  "v_mov_b32 v103, v98\n\t" \
  "v_mov_b32 v97, v96\n\t" \
  "s_nop 0\n\t" \
  "v_permlane16_swap_b32 v98, v103\n\t" \
  "v_permlane16_swap_b32 v96, v97\n\t" \
  "v_mul_f32 v99, 0x3ae38e39, v96\n\t" \
  "v_add_f32 v98, v98, v103\n\t" \
  "v_mul_f32 v100, v99, -v99\n\t" \
  "v_cvt_pk_bf16_f32 v104, 0, v99\n\t" \
  "v_fmamk_f32 v105, v97, 0x3ae38e39, v100\n\t" \
  "v_fma_f32 v98, -v104, %8, v98\n\t" \
  "v_fmamk_f32 v101, v105, 0x42800000, v100\n\t" \
  "v_cvt_pk_bf16_f32 v105, 0, v105\n\t" \
  "v_cmp_nle_f32 vcc, 0, v101\n\t" \
  "v_add_f32 v105, 0x3727c5ac, v105\n\t" \
  "v_cvt_pk_bf16_f32 v105, 0, v105\n\t" \
  "v_sqrt_f32 v105, v105\n\t" \
  "s_nop 0\n\t" \
  "v_cvt_pk_bf16_f32 v105, 0, v105\n\t" \
  "v_rcp_f32 v105, v105\n\t" \
  "s_or_b64 vcc, vcc, s[98:99]\n\t" \
  "s_cbranch_scc1 5f\n\t" \
  "6:\n\t" \
  "v_mul_f32 v98, v98, v105\n\t" \
  "v_cvt_pk_bf16_f32 v98, 0, v98\n\t" \
  "v_ashrrev_i32 v103, 31, v98\n\t" \
  "s_brev_b32 s96, 1\n\t" \
  "v_bitop3_b32 v106, v98, v103, s96 bitop3:0x1e\n\t" \
  "ds_write_b16_d16_hi %9, v106\n\t" \
  "v_lshrrev_b32 v106, 16, v106\n\t" \
  "v_lshl_or_b32 v106, v106, 15, %11\n\t" \
  "ds_write_b32 %10, v106\n\t" \
  "s_mov_b64 exec, 1\n\t" \
  "ds_add_rtn_u32 v103, v108, v107 offset:48\n\t" \
  "s_mov_b64 exec, -1\n\t" \
  "ds_read_b128 v[44:47], %12\n\t" \
  "v_lshlrev_b32 v16, 16, v86\n\t" \
  "v_and_b32 v17, 0xffff0000, v86\n\t" \
  "v_lshlrev_b32 v18, 16, v87\n\t" \
  "v_and_b32 v19, 0xffff0000, v87\n\t" \
  "v_lshlrev_b32 v20, 16, v88\n\t" \
  "v_and_b32 v21, 0xffff0000, v88\n\t" \
  "v_lshlrev_b32 v22, 16, v89\n\t" \
  "v_and_b32 v23, 0xffff0000, v89\n\t" \
  "s_waitcnt lgkmcnt(0)\n\t" \
  "v_readfirstlane_b32 s96, v103\n\t" \
  "s_cmp_lg_u32 s96, %17\n\t" \
  "s_cbranch_scc1 7f\n\t" \
  "s_mov_b64 exec, %18\n\t" \
  "global_store_dwordx4 %13, v[44:47], %16 sc1\n\t" \
  "s_mov_b64 exec, -1\n\t" \
  "s_branch 7f\n\t" \
  "5:\n\t" \
  "s_cmp_eq_u64 s[98:99], 0\n\t" \
  "s_cbranch_scc1 4f\n\t" \
  "s_sub_u32 s97, s97, 1\n\t" \
  "s_cbranch_scc1 4f\n\t" \
  "s_sleep 2\n\t" \
  "global_load_dwordx4 v[44:47], %6, %16 offset:-2048 sc1\n\t" \
  "global_load_dwordx4 v[48:51], %7, %16 offset:-2048 sc1\n\t" \
  "global_load_dwordx4 v[52:55], %6, %16 offset:0 sc1\n\t" \
  "global_load_dwordx4 v[56:59], %7, %16 offset:0 sc1\n\t" \
  "global_load_dwordx4 v[60:63], %6, %16 offset:2048 sc1\n\t" \
  "global_load_dwordx4 v[64:67], %7, %16 offset:2048 sc1\n\t" \
  "s_waitcnt vmcnt(0)\n\t" \
  "s_branch 1b\n\t" \
  "4:\n\t" \
  "v_mov_b32 v68, 0\n\t" \
  "v_mov_b32 v69, 0\n\t" \
  "v_mov_b32 v98, 0\n\t" \
  "v_lshlrev_b32 v70, 16, v86\n\t" \
  "v_and_b32 v71, 0xffff0000, v86\n\t" \
  "v_sub_f32 v70, v70, v104\n\t" \
  "v_sub_f32 v71, v71, v104\n\t" \
  "v_pk_fma_f32 v[68:69], v[70:71], v[70:71], v[68:69]\n\t" \
  "v_cvt_pk_bf16_f32 v70, v70, v71\n\t" \
  "v_dot2_f32_bf16 v98, v70, v34, v98\n\t" \
  "v_lshlrev_b32 v70, 16, v87\n\t" \
  "v_and_b32 v71, 0xffff0000, v87\n\t" \
  "v_sub_f32 v70, v70, v104\n\t" \
  "v_sub_f32 v71, v71, v104\n\t" \
  "v_pk_fma_f32 v[68:69], v[70:71], v[70:71], v[68:69]\n\t" \
  "v_cvt_pk_bf16_f32 v70, v70, v71\n\t" \
  "v_dot2_f32_bf16 v98, v70, v35, v98\n\t" \
  "v_lshlrev_b32 v70, 16, v88\n\t" \
  "v_and_b32 v71, 0xffff0000, v88\n\t" \
  "v_sub_f32 v70, v70, v104\n\t" \
  "v_sub_f32 v71, v71, v104\n\t" \
  "v_pk_fma_f32 v[68:69], v[70:71], v[70:71], v[68:69]\n\t" \
  "v_cvt_pk_bf16_f32 v70, v70, v71\n\t" \
  "v_dot2_f32_bf16 v98, v70, v36, v98\n\t" \
  "v_lshlrev_b32 v70, 16, v89\n\t" \
  "v_and_b32 v71, 0xffff0000, v89\n\t" \
  "v_sub_f32 v70, v70, v104\n\t" \
  "v_sub_f32 v71, v71, v104\n\t" \
  "v_pk_fma_f32 v[68:69], v[70:71], v[70:71], v[68:69]\n\t" \
  "v_cvt_pk_bf16_f32 v70, v70, v71\n\t" \
  "v_dot2_f32_bf16 v98, v70, v37, v98\n\t" \
  "v_lshlrev_b32 v70, 16, v90\n\t" \
  "v_and_b32 v71, 0xffff0000, v90\n\t" \
  "v_sub_f32 v70, v70, v104\n\t" \
  "v_sub_f32 v71, v71, v104\n\t" \
  "v_pk_fma_f32 v[68:69], v[70:71], v[70:71], v[68:69]\n\t" \
  "v_cvt_pk_bf16_f32 v70, v70, v71\n\t" \
  "v_dot2_f32_bf16 v98, v70, v38, v98\n\t" \
  "v_lshlrev_b32 v70, 16, v91\n\t" \
  "v_and_b32 v71, 0xffff0000, v91\n\t" \
  "v_sub_f32 v70, v70, v104\n\t" \
  "v_sub_f32 v71, v71, v104\n\t" \
  "v_pk_fma_f32 v[68:69], v[70:71], v[70:71], v[68:69]\n\t" \
  "v_cvt_pk_bf16_f32 v70, v70, v71\n\t" \
  "v_dot2_f32_bf16 v98, v70, v39, v98\n\t" \
  "v_lshlrev_b32 v70, 16, v92\n\t" \
  "v_and_b32 v71, 0xffff0000, v92\n\t" \
  "v_sub_f32 v70, v70, v104\n\t" \
  "v_sub_f32 v71, v71, v104\n\t" \
  "v_pk_fma_f32 v[68:69], v[70:71], v[70:71], v[68:69]\n\t" \
  "v_cvt_pk_bf16_f32 v70, v70, v71\n\t" \
  "v_dot2_f32_bf16 v98, v70, v40, v98\n\t" \
  "v_lshlrev_b32 v70, 16, v93\n\t" \
  "v_and_b32 v71, 0xffff0000, v93\n\t" \
  "v_sub_f32 v70, v70, v104\n\t" \
  "v_sub_f32 v71, v71, v104\n\t" \
  "v_pk_fma_f32 v[68:69], v[70:71], v[70:71], v[68:69]\n\t" \
  "v_cvt_pk_bf16_f32 v70, v70, v71\n\t" \
  "v_dot2_f32_bf16 v98, v70, v41, v98\n\t" \
  "v_lshlrev_b32 v70, 16, v94\n\t" \
  "v_and_b32 v71, 0xffff0000, v94\n\t" \
  "v_sub_f32 v70, v70, v104\n\t" \
  "v_sub_f32 v71, v71, v104\n\t" \
  "v_pk_fma_f32 v[68:69], v[70:71], v[70:71], v[68:69]\n\t" \
  "v_cvt_pk_bf16_f32 v70, v70, v71\n\t" \
  "v_dot2_f32_bf16 v98, v70, v42, v98\n\t" \
  "v_add_f32 v68, v68, v69\n\t" \
  "s_nop 1\n\t" \
  "v_add_f32_dpp v68, v68, v68 quad_perm:[1,0,3,2] row_mask:0xf bank_mask:0xf bound_ctrl:1\n\t" \
  "s_nop 1\n\t" \
  "v_add_f32_dpp v68, v68, v68 quad_perm:[2,3,0,1] row_mask:0xf bank_mask:0xf bound_ctrl:1\n\t" \
  "s_nop 1\n\t" \
  "v_add_f32_dpp v68, v68, v68 row_half_mirror row_mask:0xf bank_mask:0xf bound_ctrl:1\n\t" \
  "s_nop 1\n\t" \
  "v_add_f32_dpp v68, v68, v68 row_mirror row_mask:0xf bank_mask:0xf bound_ctrl:1\n\t" \
  "v_mov_b32 v103, v68\n\t" \
  "s_nop 1\n\t" \
  "v_permlane16_swap_b32 v68, v103\n\t" \
  "v_add_f32 v68, v68, v103\n\t" \
  "v_add_f32_dpp v98, v98, v98 quad_perm:[1,0,3,2] row_mask:0xf bank_mask:0xf bound_ctrl:1\n\t" \
  "s_nop 1\n\t" \
  "v_add_f32_dpp v98, v98, v98 quad_perm:[2,3,0,1] row_mask:0xf bank_mask:0xf bound_ctrl:1\n\t" \
  "s_nop 1\n\t" \
  "v_add_f32_dpp v98, v98, v98 row_half_mirror row_mask:0xf bank_mask:0xf bound_ctrl:1\n\t" \
  "s_nop 1\n\t" \
  "v_add_f32_dpp v98, v98, v98 row_mirror row_mask:0xf bank_mask:0xf bound_ctrl:1\n\t" \
  "v_mov_b32 v103, v98\n\t" \
  "s_nop 1\n\t" \
  "v_permlane16_swap_b32 v98, v103\n\t" \
  "v_add_f32 v98, v98, v103\n\t" \
  "v_sub_f32 v70, v99, v104\n\t" \
  "v_mul_f32 v70, v70, -v70\n\t" \
  "v_fmamk_f32 v105, v68, 0x3ae38e39, v70\n\t" \
  "v_cmp_gt_f32 vcc, 0, v105\n\t" \
  "s_nop 1\n\t" \
  "v_cndmask_b32 v105, v105, v108, vcc\n\t" \
  "v_cvt_pk_bf16_f32 v105, 0, v105\n\t" \
  "v_add_f32 v105, 0x3727c5ac, v105\n\t" \
  "v_cvt_pk_bf16_f32 v105, 0, v105\n\t" \
  "v_sqrt_f32 v105, v105\n\t" \
  "s_nop 0\n\t" \
  "v_cvt_pk_bf16_f32 v105, 0, v105\n\t" \
  "v_rcp_f32 v105, v105\n\t" \
  "s_branch 6b\n\t" \
  "7:\n\t" \
  "v_lshlrev_b32 v24, 16, v90\n\t" \
  "v_and_b32 v25, 0xffff0000, v90\n\t" \
  "v_lshlrev_b32 v26, 16, v91\n\t" \
  "v_and_b32 v27, 0xffff0000, v91\n\t" \
  "v_lshlrev_b32 v28, 16, v92\n\t" \
  "v_and_b32 v29, 0xffff0000, v92\n\t" \
  "v_lshlrev_b32 v30, 16, v93\n\t" \
  "v_and_b32 v31, 0xffff0000, v93\n\t" \
  "v_lshlrev_b32 v32, 16, v94\n\t" \
  "v_and_b32 v33, 0xffff0000, v94\n\t"

#define KGEN_ASM \
  "s_mov_b32 m0, %1\n\t" \
  "s_mov_b32 s96, 0\n\t" \
  "s_mov_b32 s97, -1\n\t" \
  "v_pk_mul_f32 v[104:105], v[26:27], v[32:33]\n\t" \
  "v_pk_mul_f32 v[106:107], v[26:27], v[8:9]\n\t" \
  "v_pk_mul_f32 v[40:41], v[30:31], v[28:29] op_sel_hi:[0,1] neg_hi:[1,0]\n\t" \
  "v_fract_f32 v105, v105\n\t" \
  "v_fract_f32 v107, v107\n\t" \
  "v_pk_fma_f32 v[40:41], v[30:31], v[28:29], v[40:41] op_sel:[1,1,0] op_sel_hi:[1,0,1] neg_lo:[1,0,0] neg_hi:[1,0,0]\n\t" \
  "v_exp_f32 v104, v104\n\t" \
  "v_cos_f32 v116, v105\n\t" \
  "v_sin_f32 v117, v105\n\t" \
  "v_exp_f32 v106, v106\n\t" \
  "v_cos_f32 v118, v107\n\t" \
  "v_sin_f32 v119, v107\n\t" \
  "v_pk_mul_f32 v[116:117], v[104:105], v[116:117] op_sel_hi:[0,1]\n\t" \
  "v_pk_mul_f32 v[118:119], v[106:107], v[118:119] op_sel_hi:[0,1]\n\t" \
  "v_max_f32 v108, |v40|, |v41|\n\t" \
  "v_pk_mul_f32 v[120:121], v[116:117], v[116:117] op_sel_hi:[0,1]\n\t" \
  "v_pk_mul_f32 v[122:123], v[118:119], v[118:119] op_sel_hi:[0,1]\n\t" \
  "v_pk_fma_f32 v[120:121], v[116:117], v[116:117], v[120:121] op_sel:[1,1,0] op_sel_hi:[1,0,1] neg_lo:[1,0,0]\n\t" \
  "v_pk_fma_f32 v[122:123], v[118:119], v[118:119], v[122:123] op_sel:[1,1,0] op_sel_hi:[1,0,1] neg_lo:[1,0,0]\n\t" \
  "v_cmp_nle_f32 s[98:99], v108, 4.0\n\t" \
  "v_cmp_lt_f32 vcc, 0x3b000000, v108\n\t" \
  "v_cmp_lt_f32 s[94:95], 0, v26\n\t" \
  "v_cndmask_b32 v130, v122, v116, s[96:97]\n\t" \
  "v_cndmask_b32 v131, v123, v117, s[96:97]\n\t" \
  "v_pk_mul_f32 v[42:43], v[40:41], v[116:117] op_sel_hi:[0,1]\n\t" \
  "v_pk_mul_f32 v[44:45], v[40:41], v[120:121] op_sel_hi:[0,1]\n\t" \
  "v_pk_fma_f32 v[42:43], v[40:41], v[116:117], v[42:43] op_sel:[1,1,0] op_sel_hi:[1,0,1] neg_lo:[1,0,0]\n\t" \
  "v_pk_fma_f32 v[44:45], v[40:41], v[120:121], v[44:45] op_sel:[1,1,0] op_sel_hi:[1,0,1] neg_lo:[1,0,0]\n\t" \
  "v_pk_mul_f32 v[124:125], v[120:121], v[120:121] op_sel_hi:[0,1]\n\t" \
  "v_pk_mul_f32 v[126:127], v[122:123], v[130:131] op_sel_hi:[0,1]\n\t" \
  "v_pk_fma_f32 v[124:125], v[120:121], v[120:121], v[124:125] op_sel:[1,1,0] op_sel_hi:[1,0,1] neg_lo:[1,0,0]\n\t" \
  "v_pk_fma_f32 v[126:127], v[122:123], v[130:131], v[126:127] op_sel:[1,1,0] op_sel_hi:[1,0,1] neg_lo:[1,0,0]\n\t" \
  "s_or_b32 s98, s99, s95\n\t" \
  "s_cselect_b32 s98, 0x100, 0\n\t" \
  "s_cmp_lg_u32 vcc_hi, 0\n\t" \
  "s_cselect_b32 s99, 0x10000, 0\n\t" \
  "v_pk_mul_f32 v[56:57], v[40:41], v[118:119] op_sel_hi:[0,1]\n\t" \
  "v_pk_mul_f32 v[72:73], v[40:41], v[122:123] op_sel_hi:[0,1]\n\t" \
  "v_pk_fma_f32 v[56:57], v[40:41], v[118:119], v[56:57] op_sel:[1,1,0] op_sel_hi:[1,0,1] neg_lo:[1,0,0]\n\t" \
  "v_pk_fma_f32 v[72:73], v[40:41], v[122:123], v[72:73] op_sel:[1,1,0] op_sel_hi:[1,0,1] neg_lo:[1,0,0]\n\t" \
  "v_pk_mul_f32 v[48:49], v[40:41], v[124:125] op_sel_hi:[0,1]\n\t" \
  "v_pk_mul_f32 v[74:75], v[40:41], v[126:127] op_sel_hi:[0,1]\n\t" \
  "v_pk_fma_f32 v[48:49], v[40:41], v[124:125], v[48:49] op_sel:[1,1,0] op_sel_hi:[1,0,1] neg_lo:[1,0,0]\n\t" \
  "v_pk_fma_f32 v[74:75], v[40:41], v[126:127], v[74:75] op_sel:[1,1,0] op_sel_hi:[1,0,1] neg_lo:[1,0,0]\n\t" \
  "s_add_u32 s98, s98, %2\n\t" \
  "s_add_u32 s98, s98, s99\n\t" \
  "v_cvt_pk_f16_f32 v132, v40, v41\n\t" \
  "v_cvt_pk_f16_f32 v133, v42, v43\n\t" \
  "v_cvt_pk_f16_f32 v134, v44, v45\n\t" \
  "v_cvt_pk_f16_f32 v140, v56, v57\n\t" \
  "v_pk_mul_f32 v[46:47], v[42:43], v[120:121] op_sel_hi:[0,1]\n\t" \
  "v_pk_mul_f32 v[50:51], v[42:43], v[124:125] op_sel_hi:[0,1]\n\t" \
  "v_pk_fma_f32 v[46:47], v[42:43], v[120:121], v[46:47] op_sel:[1,1,0] op_sel_hi:[1,0,1] neg_lo:[1,0,0]\n\t" \
  "v_pk_fma_f32 v[50:51], v[42:43], v[124:125], v[50:51] op_sel:[1,1,0] op_sel_hi:[1,0,1] neg_lo:[1,0,0]\n\t" \
  "v_cndmask_b32 v128, v126, v120, s[96:97]\n\t" \
  "v_cndmask_b32 v129, v127, v121, s[96:97]\n\t" \
  "v_pk_mul_f32 v[52:53], v[44:45], v[124:125] op_sel_hi:[0,1]\n\t" \
  "v_pk_mul_f32 v[58:59], v[42:43], v[118:119] op_sel_hi:[0,1]\n\t" \
  "v_pk_fma_f32 v[52:53], v[44:45], v[124:125], v[52:53] op_sel:[1,1,0] op_sel_hi:[1,0,1] neg_lo:[1,0,0]\n\t" \
  "v_pk_fma_f32 v[58:59], v[42:43], v[118:119], v[58:59] op_sel:[1,1,0] op_sel_hi:[1,0,1] neg_lo:[1,0,0]\n\t" \
  "v_cvt_pk_f16_f32 v136, v48, v49\n\t" \
  "v_cvt_pk_f16_f32 v148, v72, v73\n\t" \
  "v_cvt_pk_f16_f32 v149, v74, v75\n\t" \
  "v_cvt_pk_f16_f32 v135, v46, v47\n\t" \
  "v_pk_mul_f32 v[54:55], v[46:47], v[124:125] op_sel_hi:[0,1]\n\t" \
  "v_pk_mul_f32 v[60:61], v[44:45], v[118:119] op_sel_hi:[0,1]\n\t" \
  "v_pk_fma_f32 v[54:55], v[46:47], v[124:125], v[54:55] op_sel:[1,1,0] op_sel_hi:[1,0,1] neg_lo:[1,0,0]\n\t" \
  "v_pk_fma_f32 v[60:61], v[44:45], v[118:119], v[60:61] op_sel:[1,1,0] op_sel_hi:[1,0,1] neg_lo:[1,0,0]\n\t" \
  "v_pk_mul_f32 v[62:63], v[46:47], v[118:119] op_sel_hi:[0,1]\n\t" \
  "v_pk_mul_f32 v[64:65], v[48:49], v[118:119] op_sel_hi:[0,1]\n\t" \
  "v_pk_fma_f32 v[62:63], v[46:47], v[118:119], v[62:63] op_sel:[1,1,0] op_sel_hi:[1,0,1] neg_lo:[1,0,0]\n\t" \
  "v_pk_fma_f32 v[64:65], v[48:49], v[118:119], v[64:65] op_sel:[1,1,0] op_sel_hi:[1,0,1] neg_lo:[1,0,0]\n\t" \
  "v_cvt_pk_f16_f32 v137, v50, v51\n\t" \
  "v_cvt_pk_f16_f32 v138, v52, v53\n\t" \
  "v_cvt_pk_f16_f32 v141, v58, v59\n\t" \
  "v_cvt_pk_f16_f32 v139, v54, v55\n\t" \
  "s_cmp_eq_u32 %5, 0\n\t" \
  "s_cbranch_scc1 3f\n\t" \
  "ds_write_addtid_b32 v132 offset:0\n\t" \
  "ds_write_addtid_b32 v133 offset:288\n\t" \
  "ds_write_addtid_b32 v134 offset:576\n\t" \
  "ds_write_addtid_b32 v140 offset:2304\n\t" \
  "ds_write_addtid_b32 v136 offset:1152\n\t" \
  "ds_write_addtid_b32 v148 offset:4608\n\t" \
  "ds_write_addtid_b32 v149 offset:4896\n\t" \
  "ds_write_addtid_b32 v135 offset:864\n\t" \
  "ds_write_addtid_b32 v137 offset:1440\n\t" \
  "ds_write_addtid_b32 v138 offset:1728\n\t" \
  "ds_write_addtid_b32 v141 offset:2592\n\t" \
  "ds_write_addtid_b32 v139 offset:2016\n\t" \
  "3:\n\t" \
  "v_pk_mul_f32 v[66:67], v[50:51], v[118:119] op_sel_hi:[0,1]\n\t" \
  "v_pk_mul_f32 v[68:69], v[52:53], v[118:119] op_sel_hi:[0,1]\n\t" \
  "v_pk_fma_f32 v[66:67], v[50:51], v[118:119], v[66:67] op_sel:[1,1,0] op_sel_hi:[1,0,1] neg_lo:[1,0,0]\n\t" \
  "v_pk_fma_f32 v[68:69], v[52:53], v[118:119], v[68:69] op_sel:[1,1,0] op_sel_hi:[1,0,1] neg_lo:[1,0,0]\n\t" \
  "v_pk_mul_f32 v[70:71], v[54:55], v[118:119] op_sel_hi:[0,1]\n\t" \
  "v_pk_mul_f32 v[76:77], v[72:73], v[128:129] op_sel_hi:[0,1]\n\t" \
  "v_pk_fma_f32 v[70:71], v[54:55], v[118:119], v[70:71] op_sel:[1,1,0] op_sel_hi:[1,0,1] neg_lo:[1,0,0]\n\t" \
  "v_pk_fma_f32 v[76:77], v[72:73], v[128:129], v[76:77] op_sel:[1,1,0] op_sel_hi:[1,0,1] neg_lo:[1,0,0]\n\t" \
  "v_cvt_pk_f16_f32 v142, v60, v61\n\t" \
  "v_cvt_pk_f16_f32 v143, v62, v63\n\t" \
  "v_cvt_pk_f16_f32 v144, v64, v65\n\t" \
  "v_cvt_pk_f16_f32 v145, v66, v67\n\t" \
  "v_pk_mul_f32 v[78:79], v[74:75], v[128:129] op_sel_hi:[0,1]\n\t" \
  "v_pk_mul_f32 v[80:81], v[48:49], v[122:123] op_sel_hi:[0,1]\n\t" \
  "v_pk_fma_f32 v[78:79], v[74:75], v[128:129], v[78:79] op_sel:[1,1,0] op_sel_hi:[1,0,1] neg_lo:[1,0,0]\n\t" \
  "v_pk_fma_f32 v[80:81], v[48:49], v[122:123], v[80:81] op_sel:[1,1,0] op_sel_hi:[1,0,1] neg_lo:[1,0,0]\n\t" \
  "v_pk_mul_f32 v[82:83], v[50:51], v[122:123] op_sel_hi:[0,1]\n\t" \
  "v_pk_mul_f32 v[84:85], v[52:53], v[122:123] op_sel_hi:[0,1]\n\t" \
  "v_pk_fma_f32 v[82:83], v[50:51], v[122:123], v[82:83] op_sel:[1,1,0] op_sel_hi:[1,0,1] neg_lo:[1,0,0]\n\t" \
  "v_pk_fma_f32 v[84:85], v[52:53], v[122:123], v[84:85] op_sel:[1,1,0] op_sel_hi:[1,0,1] neg_lo:[1,0,0]\n\t" \
  "v_cvt_pk_f16_f32 v146, v68, v69\n\t" \
  "v_cvt_pk_f16_f32 v147, v70, v71\n\t" \
  "v_cvt_pk_f16_f32 v150, v76, v77\n\t" \
  "v_cvt_pk_f16_f32 v151, v78, v79\n\t" \
  "v_pk_mul_f32 v[86:87], v[54:55], v[122:123] op_sel_hi:[0,1]\n\t" \
  "v_pk_mul_f32 v[88:89], v[56:57], v[122:123] op_sel_hi:[0,1]\n\t" \
  "v_pk_fma_f32 v[86:87], v[54:55], v[122:123], v[86:87] op_sel:[1,1,0] op_sel_hi:[1,0,1] neg_lo:[1,0,0]\n\t" \
  "v_pk_fma_f32 v[88:89], v[56:57], v[122:123], v[88:89] op_sel:[1,1,0] op_sel_hi:[1,0,1] neg_lo:[1,0,0]\n\t" \
  "v_pk_mul_f32 v[90:91], v[56:57], v[126:127] op_sel_hi:[0,1]\n\t" \
  "v_pk_mul_f32 v[94:95], v[62:63], v[122:123] op_sel_hi:[0,1]\n\t" \
  "v_pk_fma_f32 v[90:91], v[56:57], v[126:127], v[90:91] op_sel:[1,1,0] op_sel_hi:[1,0,1] neg_lo:[1,0,0]\n\t" \
  "v_pk_fma_f32 v[94:95], v[62:63], v[122:123], v[94:95] op_sel:[1,1,0] op_sel_hi:[1,0,1] neg_lo:[1,0,0]\n\t" \
  "v_cvt_pk_f16_f32 v152, v80, v81\n\t" \
  "v_cvt_pk_f16_f32 v153, v82, v83\n\t" \
  "v_cvt_pk_f16_f32 v154, v84, v85\n\t" \
  "v_cvt_pk_f16_f32 v155, v86, v87\n\t" \
  "s_cmp_eq_u32 %5, 0\n\t" \
  "s_cbranch_scc1 3f\n\t" \
  "ds_write_addtid_b32 v142 offset:2880\n\t" \
  "ds_write_addtid_b32 v143 offset:3168\n\t" \
  "ds_write_addtid_b32 v144 offset:3456\n\t" \
  "ds_write_addtid_b32 v145 offset:3744\n\t" \
  "ds_write_addtid_b32 v146 offset:4032\n\t" \
  "ds_write_addtid_b32 v147 offset:4320\n\t" \
  "ds_write_addtid_b32 v150 offset:5184\n\t" \
  "ds_write_addtid_b32 v151 offset:5472\n\t" \
  "ds_write_addtid_b32 v152 offset:5760\n\t" \
  "ds_write_addtid_b32 v153 offset:6048\n\t" \
  "ds_write_addtid_b32 v154 offset:6336\n\t" \
  "ds_write_addtid_b32 v155 offset:6624\n\t" \
  "3:\n\t" \
  "v_pk_mul_f32 v[92:93], v[88:89], v[128:129] op_sel_hi:[0,1]\n\t" \
  "v_pk_mul_f32 v[96:97], v[64:65], v[122:123] op_sel_hi:[0,1]\n\t" \
  "v_pk_fma_f32 v[92:93], v[88:89], v[128:129], v[92:93] op_sel:[1,1,0] op_sel_hi:[1,0,1] neg_lo:[1,0,0]\n\t" \
  "v_pk_fma_f32 v[96:97], v[64:65], v[122:123], v[96:97] op_sel:[1,1,0] op_sel_hi:[1,0,1] neg_lo:[1,0,0]\n\t" \
  "v_pk_mul_f32 v[98:99], v[66:67], v[122:123] op_sel_hi:[0,1]\n\t" \
  "v_pk_mul_f32 v[100:101], v[68:69], v[122:123] op_sel_hi:[0,1]\n\t" \
  "v_pk_fma_f32 v[98:99], v[66:67], v[122:123], v[98:99] op_sel:[1,1,0] op_sel_hi:[1,0,1] neg_lo:[1,0,0]\n\t" \
  "v_pk_fma_f32 v[100:101], v[68:69], v[122:123], v[100:101] op_sel:[1,1,0] op_sel_hi:[1,0,1] neg_lo:[1,0,0]\n\t" \
  "v_pk_mul_f32 v[102:103], v[70:71], v[122:123] op_sel_hi:[0,1]\n\t" \
  "v_mov_b32 v130, s98\n\t" \
  "v_pk_fma_f32 v[102:103], v[70:71], v[122:123], v[102:103] op_sel:[1,1,0] op_sel_hi:[1,0,1] neg_lo:[1,0,0]\n\t" \
  "v_cvt_pk_f16_f32 v156, v88, v89\n\t" \
  "v_cvt_pk_f16_f32 v157, v90, v91\n\t" \
  "v_cvt_pk_f16_f32 v159, v94, v95\n\t" \
  "v_cvt_pk_f16_f32 v158, v92, v93\n\t" \
  "v_cvt_pk_f16_f32 v160, v96, v97\n\t" \
  "v_cvt_pk_f16_f32 v161, v98, v99\n\t" \
  "v_cvt_pk_f16_f32 v162, v100, v101\n\t" \
  "v_cvt_pk_f16_f32 v163, v102, v103\n\t" \
  TSQ(28) \
  "s_cmp_lg_u32 %5, 0\n\t" \
  "s_setprio " PL1 "\n\t" \
  "s_cbranch_scc1 3f\n\t" \
  "s_setprio " PL "\n\t" \
  "ds_write_addtid_b32 v132 offset:0\n\t" \
  "ds_write_addtid_b32 v133 offset:288\n\t" \
  "ds_write_addtid_b32 v134 offset:576\n\t" \
  "ds_write_addtid_b32 v140 offset:2304\n\t" \
  "ds_write_addtid_b32 v136 offset:1152\n\t" \
  "ds_write_addtid_b32 v148 offset:4608\n\t" \
  "ds_write_addtid_b32 v149 offset:4896\n\t" \
  "ds_write_addtid_b32 v135 offset:864\n\t" \
  "ds_write_addtid_b32 v137 offset:1440\n\t" \
  "ds_write_addtid_b32 v138 offset:1728\n\t" \
  "ds_write_addtid_b32 v141 offset:2592\n\t" \
  "ds_write_addtid_b32 v139 offset:2016\n\t" \
  "ds_write_addtid_b32 v142 offset:2880\n\t" \
  "ds_write_addtid_b32 v143 offset:3168\n\t" \
  "ds_write_addtid_b32 v144 offset:3456\n\t" \
  "ds_write_addtid_b32 v145 offset:3744\n\t" \
  "ds_write_addtid_b32 v146 offset:4032\n\t" \
  "ds_write_addtid_b32 v147 offset:4320\n\t" \
  "ds_write_addtid_b32 v150 offset:5184\n\t" \
  "ds_write_addtid_b32 v151 offset:5472\n\t" \
  "ds_write_addtid_b32 v152 offset:5760\n\t" \
  "ds_write_addtid_b32 v153 offset:6048\n\t" \
  "ds_write_addtid_b32 v154 offset:6336\n\t" \
  "ds_write_addtid_b32 v155 offset:6624\n\t" \
  "3:\n\t" \
  "ds_write_addtid_b32 v156 offset:6912\n\t" \
  "ds_write_addtid_b32 v157 offset:7200\n\t" \
  "ds_write_addtid_b32 v159 offset:7776\n\t" \
  "ds_write_addtid_b32 v158 offset:7488\n\t" \
  "ds_write_addtid_b32 v160 offset:8064\n\t" \
  "ds_write_addtid_b32 v161 offset:8352\n\t" \
  "ds_write_addtid_b32 v162 offset:8640\n\t" \
  "ds_write_addtid_b32 v163 offset:8928\n\t" \
  "s_mov_b64 exec, 1\n\t" \
  "ds_add_u32 v11, v130\n\t" \
  "s_mov_b64 exec, -1\n\t" \
  "s_cmp_lg_u32 s74, 1\n\t" \
  "s_cbranch_scc1 3f\n\t" \
  "global_load_dword v166, v165, s[72:73]" TNT "\n\t" \
  "3:\n\t" \
  TSQ(5) \
  "v_mov_b32 v112, v15\n\t" \
  "v_mov_b32 v113, 0\n\t" \
  "v_mov_b64 v[114:115], 0\n\t" \
  "v_mov_b32 v130, 1\n\t" \
  "1:\n\t" \
  "ds_read_b32 v108, v11\n\t" \
  "s_waitcnt lgkmcnt(0)\n\t" \
  "v_readfirstlane_b32 s98, v108\n\t" \
  "s_and_b32 s99, s98, 0xff\n\t" \
  "s_cmp_lt_u32 s99, %3\n\t" \
  "s_cbranch_scc1 1b\n\t" \
  "s_mov_b32 s94, s98\n\t" \
  TSQ(29) \
  "s_mov_b32 s98, s94\n\t" \
  "ds_read_b128 v[40:43], v36 offset:0\n\t" \
  "ds_read_b128 v[44:47], v10 offset:128\n\t" \
  "ds_read_b128 v[48:51], v36 offset:64\n\t" \
  "ds_read_b128 v[52:55], v10 offset:192\n\t" \
  "ds_read_b128 v[56:59], v36 offset:9216\n\t" \
  "ds_read_b128 v[60:63], v10 offset:9344\n\t" \
  "ds_read_b128 v[64:67], v36 offset:9280\n\t" \
  "ds_read_b128 v[68:71], v10 offset:9408\n\t" \
  "ds_read_b128 v[72:75], v36 offset:18432\n\t" \
  "ds_read_b128 v[76:79], v10 offset:18560\n\t" \
  "ds_read_b128 v[80:83], v36 offset:18496\n\t" \
  "ds_read_b128 v[84:87], v10 offset:18624\n\t" \
  "ds_read_b128 v[88:91], v36 offset:27648\n\t" \
  "ds_read_b128 v[92:95], v10 offset:27776\n\t" \
  "ds_read_b128 v[96:99], v36 offset:27712\n\t" \
  "ds_read_b128 v[100:103], v10 offset:27840\n\t" \
  "s_cmp_lt_u32 s98, 0x10000\n\t" \
  "s_cselect_b32 s99, 0x100, s98\n\t" \
  "s_and_b32 s99, s99, 0xff00\n\t" \
  "s_cmp_eq_u32 %3, 4\n\t" \
  "s_cselect_b32 %0, s99, 0\n\t" \
  "s_cmp_lg_u32 %0, 0\n\t" \
  "s_cbranch_scc1 8f\n\t" \
  "s_waitcnt lgkmcnt(14)\n\t" \
  "v_mfma_f32_16x16x32_f16 v[112:115], v[40:43], v[44:47], v[112:115]\n\t" \
  "s_waitcnt lgkmcnt(12)\n\t" \
  "v_mfma_f32_16x16x32_f16 v[112:115], v[48:51], v[52:55], v[112:115]\n\t" \
  "s_waitcnt lgkmcnt(10)\n\t" \
  "v_mfma_f32_16x16x32_f16 v[112:115], v[56:59], v[60:63], v[112:115]\n\t" \
  "s_waitcnt lgkmcnt(8)\n\t" \
  "v_mfma_f32_16x16x32_f16 v[112:115], v[64:67], v[68:71], v[112:115]\n\t" \
  "s_waitcnt lgkmcnt(6)\n\t" \
  "v_mfma_f32_16x16x32_f16 v[112:115], v[72:75], v[76:79], v[112:115]\n\t" \
  "s_waitcnt lgkmcnt(4)\n\t" \
  "v_mfma_f32_16x16x32_f16 v[112:115], v[80:83], v[84:87], v[112:115]\n\t" \
  "s_waitcnt lgkmcnt(2)\n\t" \
  "v_mfma_f32_16x16x32_f16 v[112:115], v[88:91], v[92:95], v[112:115]\n\t" \
  "s_waitcnt lgkmcnt(0)\n\t" \
  "v_mfma_f32_16x16x32_f16 v[112:115], v[96:99], v[100:103], v[112:115]\n\t" \
  TSQ(30) \
  "s_bitcmp1_b32 %4, 0\n\t" \
  "s_cbranch_scc0 4f\n\t" \
  "s_nop 5\n\t" \
  "v_cvt_pk_f16_f32 v104, v112, v113\n\t" \
  "v_cvt_pk_f16_f32 v105, v114, v115\n\t" \
  "s_cmp_lg_u32 %4, 1\n\t" \
  "v_readfirstlane_b32 s98, v104\n\t" \
  "v_readfirstlane_b32 s99, v105\n\t" \
  "ds_write_b16 v37, v104 offset:0\n\t" \
  "ds_write_b16_d16_hi v38, v104 offset:0\n\t" \
  "ds_write_b16 v39, v105 offset:0\n\t" \
  "ds_write_b16 v37, v104 offset:2114\n\t" \
  "ds_write_b16_d16_hi v38, v104 offset:2114\n\t" \
  "ds_write_b16 v39, v105 offset:2114\n\t" \
  "ds_write_b16 v37, v104 offset:4228\n\t" \
  "ds_write_b16_d16_hi v38, v104 offset:4228\n\t" \
  "ds_write_b16 v39, v105 offset:4228\n\t" \
  "ds_write_b16 v37, v104 offset:6342\n\t" \
  "ds_write_b16_d16_hi v38, v104 offset:6342\n\t" \
  "ds_write_b16 v39, v105 offset:6342\n\t" \
  "s_cbranch_scc1 6f\n\t" \
  "v_mov_b32 v104, s98\n\t" \
  "s_mov_b32 exec_lo, 0x80808080\n\t" \
  "v_mov_b32 v104, s99\n\t" \
  "s_mov_b32 exec_lo, 0x0f0f0f0f\n\t" \
  "s_mov_b32 exec_hi, 0\n\t" \
  "ds_write_b16 v35, v104\n\t" \
  "s_mov_b32 exec_lo, 0xf0f0f0f0\n\t" \
  "ds_write_b16_d16_hi v35, v104\n\t" \
  "s_branch 6f\n\t" \
  "4:\n\t" \
  "s_nop 5\n\t" \
  "v_cndmask_b32 v113, v113, v112, s[96:97]\n\t" \
  "v_cndmask_b32 v115, v115, v112, s[96:97]\n\t" \
  "v_cvt_pk_f16_f32 v106, v114, v113\n\t" \
  "v_cvt_pk_f16_f32 v104, v113, v112\n\t" \
  "v_cvt_f16_f32 v105, v115\n\t" \
  "ds_write_b32 v37, v106 offset:2\n\t" \
  "ds_write_b16_d16_hi v37, v104 offset:6\n\t" \
  "ds_write_b16 v38, v105 offset:0\n\t" \
  "ds_write_b32 v37, v104 offset:2118\n\t" \
  "ds_write_b16 v37, v106 offset:2116\n\t" \
  "ds_write_b16 v38, v105 offset:2114\n\t" \
  "ds_write_b32 v37, v106 offset:4230\n\t" \
  "ds_write_b16_d16_hi v37, v104 offset:4234\n\t" \
  "ds_write_b16 v38, v105 offset:4228\n\t" \
  "ds_write_b32 v37, v104 offset:6346\n\t" \
  "ds_write_b16 v37, v106 offset:6344\n\t" \
  "ds_write_b16 v38, v105 offset:6342\n\t" \
  "6:\n\t" \
  "s_mov_b64 exec, 1\n\t" \
  "ds_add_u32 v11, v130 offset:32\n\t" \
  "s_mov_b64 exec, -1\n\t" \
  "s_setprio " PE "\n\t" \
  "s_cmp_lg_u32 s74, 2\n\t" \
  "s_cbranch_scc1 10f\n\t" \
  "global_load_dword v166, v165, s[72:73]" TNT "\n\t" \
  "s_branch 10f\n\t" \
  "8:\n\t" \
  "s_waitcnt lgkmcnt(0)\n\t" \
  "10:\n\t"

#define NEAR_ASM \
  "v_and_b32 v1, 63, v0\n\t" \
  "s_mov_b32 s98, 157600\n\t" \
  "v_lshl_add_u32 v2, v1, 4, s98\n\t" \
  "ds_read_b128 v[4:7], v2 offset:0\n\t" \
  "v_lshl_add_u32 v2, v1, 4, %2\n\t" \
  "v_lshl_add_u32 v3, v1, 3, %2\n\t" \
  "v_and_b32 v8, 1, v0\n\t" \
  "v_lshlrev_b32 v8, 2, v8\n\t" \
  "v_mov_b32 v9, 1\n\t" \
  "s_waitcnt lgkmcnt(0)\n\t" \
  "v_add_u32 v4, %1, v4\n\t" \
  "v_add_u32 v5, %1, v5\n\t" \
  "v_add_u32 v6, %1, v6\n\t" \
  "ds_read_b64 v[152:153], v7 offset:256\n\t" \
  "ds_read_b64 v[154:155], v7 offset:264\n\t" \
  "ds_read_b64 v[156:157], v7 offset:384\n\t" \
  "ds_read_b64 v[158:159], v7 offset:392\n\t" \
  "1:\n\t" \
  "ds_read_b32 v160, v11 offset:32\n\t" \
  "s_waitcnt lgkmcnt(0)\n\t" \
  "v_cmp_gt_u32 vcc, 4, v160\n\t" \
  "s_cbranch_vccz 2f\n\t" \
  "s_sleep 1\n\t" \
  "s_branch 1b\n\t" \
  "2:\n\t" \
  "ds_read_b64 v[88:89], v6 offset:1880\n\t" \
  "ds_read_b64 v[90:91], v6 offset:1720\n\t" \
  "ds_read_b64 v[92:93], v6 offset:1848\n\t" \
  "ds_read_b64 v[94:95], v6 offset:1688\n\t" \
  "ds_read_b64 v[96:97], v6 offset:1816\n\t" \
  "ds_read_b64 v[98:99], v6 offset:1656\n\t" \
  "ds_read_b64 v[100:101], v6 offset:1784\n\t" \
  "ds_read_b64 v[102:103], v6 offset:1624\n\t" \
  "ds_read_b64 v[16:17], v5 offset:1112\n\t" \
  "ds_read_b64 v[18:19], v5 offset:1120\n\t" \
  "ds_read_b64 v[20:21], v5 offset:1048\n\t" \
  "ds_read_b64 v[22:23], v5 offset:1056\n\t" \
  "ds_read_b64 v[24:25], v5 offset:984\n\t" \
  "ds_read_b64 v[26:27], v5 offset:992\n\t" \
  "ds_read_b64 v[28:29], v5 offset:920\n\t" \
  "ds_read_b64 v[30:31], v5 offset:928\n\t" \
  "ds_read_b64 v[32:33], v5 offset:856\n\t" \
  "ds_read_b64 v[34:35], v5 offset:864\n\t" \
  "ds_read_b64 v[36:37], v5 offset:792\n\t" \
  "ds_read_b64 v[38:39], v5 offset:800\n\t" \
  "ds_read_b64 v[40:41], v5 offset:728\n\t" \
  "ds_read_b64 v[42:43], v5 offset:736\n\t" \
  "ds_read_b64 v[44:45], v5 offset:664\n\t" \
  "ds_read_b64 v[46:47], v5 offset:672\n\t" \
  "ds_read_b64 v[48:49], v5 offset:600\n\t" \
  "ds_read_b64 v[50:51], v5 offset:608\n\t" \
  "ds_read_b64 v[52:53], v5 offset:536\n\t" \
  "ds_read_b64 v[54:55], v5 offset:544\n\t" \
  "ds_read_b64 v[56:57], v5 offset:472\n\t" \
  "ds_read_b64 v[58:59], v5 offset:480\n\t" \
  "ds_read_b64 v[60:61], v5 offset:408\n\t" \
  "ds_read_b64 v[62:63], v5 offset:416\n\t" \
  "ds_read_b64 v[64:65], v5 offset:344\n\t" \
  "ds_read_b64 v[66:67], v5 offset:352\n\t" \
  "ds_read_b64 v[68:69], v5 offset:280\n\t" \
  "ds_read_b64 v[70:71], v5 offset:288\n\t" \
  "ds_read_b64 v[72:73], v5 offset:216\n\t" \
  "ds_read_b64 v[74:75], v5 offset:224\n\t" \
  "ds_read_b64 v[76:77], v5 offset:152\n\t" \
  "ds_read_b64 v[78:79], v5 offset:160\n\t" \
  "ds_read_b64 v[80:81], v5 offset:88\n\t" \
  "ds_read_b64 v[82:83], v5 offset:96\n\t" \
  "ds_read_b64 v[84:85], v5 offset:24\n\t" \
  "ds_read_b64 v[86:87], v5 offset:32\n\t" \
  TSQ(7) \
  "3:\n\t" \
  "ds_read_b32 v160, v8 offset:64\n\t" \
  "s_waitcnt lgkmcnt(0)\n\t" \
  "v_cmp_gt_u32 vcc, 1, v160\n\t" \
  "s_cbranch_vccz 4f\n\t" \
  "s_sleep 1\n\t" \
  "s_branch 3b\n\t" \
  "4:\n\t" \
  TSQ(8) \
  "s_setprio 3\n\t" \
  "ds_read_b128 v[120:123], v4 offset:448\n\t" \
  "ds_read_b128 v[124:127], v4 offset:432\n\t" \
  "ds_read_b128 v[128:131], v4 offset:960\n\t" \
  "ds_read_b128 v[132:135], v4 offset:944\n\t" \
  "s_waitcnt lgkmcnt(3)\n\t" \
  "v_mfma_f32_16x16x32_f16 v[120:123], v[120:123], v[152:155], 0\n\t" \
  "ds_read_b128 v[136:139], v4 offset:0\n\t" \
  "s_waitcnt lgkmcnt(3)\n\t" \
  "v_mfma_f32_16x16x32_f16 v[124:127], v[124:127], v[156:159], 0\n\t" \
  "ds_read_b128 v[140:143], v4 offset:64\n\t" \
  "s_waitcnt lgkmcnt(3)\n\t" \
  "v_mfma_f32_16x16x32_f16 v[128:131], v[128:131], v[152:155], 0\n\t" \
  "ds_read_b128 v[144:147], v4 offset:128\n\t" \
  "s_waitcnt lgkmcnt(3)\n\t" \
  "v_mfma_f32_16x16x32_f16 v[132:135], v[132:135], v[156:159], 0\n\t" \
  "ds_read_b128 v[148:151], v4 offset:192\n\t" \
  "v_cvt_pk_f16_f32 v120, v120, v121\n\t" \
  "v_cvt_pk_f16_f32 v121, v122, v123\n\t" \
  "v_cvt_pk_f16_f32 v122, v124, v125\n\t" \
  "v_cvt_pk_f16_f32 v123, v126, v127\n\t" \
  "s_nop 1\n\t" \
  "v_mfma_f32_16x16x32_f16 v[116:119], v[120:123], v[100:103], 0\n\t" \
  "v_cvt_pk_f16_f32 v128, v128, v129\n\t" \
  "v_cvt_pk_f16_f32 v129, v130, v131\n\t" \
  "v_mfma_f32_16x16x32_f16 v[112:115], v[120:123], v[96:99], 0\n\t" \
  "v_cvt_pk_f16_f32 v130, v132, v133\n\t" \
  "v_cvt_pk_f16_f32 v131, v134, v135\n\t" \
  "v_mfma_f32_16x16x32_f16 v[108:111], v[120:123], v[92:95], 0\n\t" \
  "v_mfma_f32_16x16x32_f16 v[104:107], v[120:123], v[88:91], 0\n\t" \
  "ds_read_b32 v164, v8 offset:72\n\t" \
  "v_mfma_f32_16x16x32_f16 v[116:119], v[128:131], v[96:99], v[116:119]\n\t" \
  "v_mfma_f32_16x16x32_f16 v[112:115], v[128:131], v[92:95], v[112:115]\n\t" \
  "v_mfma_f32_16x16x32_f16 v[108:111], v[128:131], v[88:91], v[108:111]\n\t" \
  "s_waitcnt lgkmcnt(3)\n\t" \
  "v_mfma_f32_16x16x32_f16 v[100:103], v[44:47], v[136:139], 0\n\t" \
  "s_waitcnt lgkmcnt(0)\n\t" \
  "v_cmp_eq_u32 vcc, 0, v164\n\t" \
  "v_mfma_f32_16x16x32_f16 v[96:99], v[76:79], v[136:139], 0\n\t" \
  "ds_read_b128 v[136:139], v4 offset:256\n\t" \
  "s_cbranch_vccnz 50f\n\t" \
  "51:\n\t" \
  "v_mfma_f32_16x16x32_f16 v[100:103], v[40:43], v[140:143], v[100:103]\n\t" \
  "ds_read_b128 v[120:123], v4 offset:1472\n\t" \
  "ds_read_b128 v[124:127], v4 offset:1456\n\t" \
  "v_mfma_f32_16x16x32_f16 v[96:99], v[72:75], v[140:143], v[96:99]\n\t" \
  "ds_read_b128 v[140:143], v4 offset:320\n\t" \
  "ds_read_b128 v[128:131], v4 offset:1984\n\t" \
  "v_mfma_f32_16x16x32_f16 v[100:103], v[36:39], v[144:147], v[100:103]\n\t" \
  "ds_read_b128 v[132:135], v4 offset:1968\n\t" \
  "v_mfma_f32_16x16x32_f16 v[96:99], v[68:71], v[144:147], v[96:99]\n\t" \
  "ds_read_b128 v[144:147], v4 offset:384\n\t" \
  "v_mfma_f32_16x16x32_f16 v[100:103], v[32:35], v[148:151], v[100:103]\n\t" \
  "v_mfma_f32_16x16x32_f16 v[96:99], v[64:67], v[148:151], v[96:99]\n\t" \
  "ds_read_b128 v[148:151], v4 offset:448\n\t" \
  "s_waitcnt lgkmcnt(7)\n\t" \
  "v_mfma_f32_16x16x32_f16 v[100:103], v[28:31], v[136:139], v[100:103]\n\t" \
  "v_mfma_f32_16x16x32_f16 v[96:99], v[60:63], v[136:139], v[96:99]\n\t" \
  "ds_read_b128 v[136:139], v4 offset:768\n\t" \
  "s_waitcnt lgkmcnt(7)\n\t" \
  "v_mfma_f32_16x16x32_f16 v[120:123], v[120:123], v[152:155], 0\n\t" \
  "s_waitcnt lgkmcnt(6)\n\t" \
  "v_mfma_f32_16x16x32_f16 v[124:127], v[124:127], v[156:159], 0\n\t" \
  "s_waitcnt lgkmcnt(4)\n\t" \
  "v_mfma_f32_16x16x32_f16 v[128:131], v[128:131], v[152:155], 0\n\t" \
  "s_nop 0\n\t" \
  "s_waitcnt lgkmcnt(3)\n\t" \
  "v_mfma_f32_16x16x32_f16 v[132:135], v[132:135], v[156:159], 0\n\t" \
  "s_nop 0\n\t" \
  "v_cvt_pk_f16_f32 v120, v120, v121\n\t" \
  "v_mfma_f32_16x16x32_f16 v[100:103], v[24:27], v[140:143], v[100:103]\n\t" \
  "v_cvt_pk_f16_f32 v121, v122, v123\n\t" \
  "v_cvt_pk_f16_f32 v122, v124, v125\n\t" \
  "v_mfma_f32_16x16x32_f16 v[96:99], v[56:59], v[140:143], v[96:99]\n\t" \
  "ds_read_b128 v[140:143], v4 offset:832\n\t" \
  "s_waitcnt lgkmcnt(3)\n\t" \
  "v_mfma_f32_16x16x32_f16 v[100:103], v[20:23], v[144:147], v[100:103]\n\t" \
  "v_cvt_pk_f16_f32 v123, v126, v127\n\t" \
  "ds_read_b128 v[124:127], v4 offset:512\n\t" \
  "v_mfma_f32_16x16x32_f16 v[96:99], v[52:55], v[144:147], v[96:99]\n\t" \
  "ds_read_b128 v[144:147], v4 offset:896\n\t" \
  "v_cvt_pk_f16_f32 v128, v128, v129\n\t" \
  "v_mfma_f32_16x16x32_f16 v[116:119], v[120:123], v[92:95], v[116:119]\n\t" \
  "v_cvt_pk_f16_f32 v129, v130, v131\n\t" \
  "v_cvt_pk_f16_f32 v130, v132, v133\n\t" \
  "v_mfma_f32_16x16x32_f16 v[112:115], v[120:123], v[88:91], v[112:115]\n\t" \
  "ds_read_b128 v[120:123], v4 offset:640\n\t" \
  "v_cvt_pk_f16_f32 v131, v134, v135\n\t" \
  "s_nop 1\n\t" \
  "v_mfma_f32_16x16x32_f16 v[116:119], v[128:131], v[88:91], v[116:119]\n\t" \
  "ds_read_b128 v[128:131], v4 offset:704\n\t" \
  "s_waitcnt lgkmcnt(5)\n\t" \
  "v_mfma_f32_16x16x32_f16 v[100:103], v[16:19], v[148:151], v[100:103]\n\t" \
  "ds_read_b128 v[132:135], v4 offset:576\n\t" \
  "v_mfma_f32_16x16x32_f16 v[96:99], v[48:51], v[148:151], v[96:99]\n\t" \
  "v_mfma_f32_16x16x32_f16 v[92:95], v[80:83], v[148:151], 0\n\t" \
  "v_mfma_f32_16x16x32_f16 v[104:107], v[84:87], v[148:151], v[104:107]\n\t" \
  "ds_read_b128 v[148:151], v4 offset:960\n\t" \
  "s_waitcnt lgkmcnt(5)\n\t" \
  "v_mfma_f32_16x16x32_f16 v[96:99], v[44:47], v[124:127], v[96:99]\n\t" \
  "v_mfma_f32_16x16x32_f16 v[92:95], v[76:79], v[124:127], v[92:95]\n\t" \
  "ds_read_b128 v[124:127], v4 offset:1024\n\t" \
  "s_waitcnt lgkmcnt(2)\n\t" \
  "v_mfma_f32_16x16x32_f16 v[96:99], v[40:43], v[132:135], v[96:99]\n\t" \
  "v_mul_f32 v156, %0, v100\n\t" \
  "v_mul_f32 v157, %0, v101\n\t" \
  "v_mfma_f32_16x16x32_f16 v[92:95], v[72:75], v[132:135], v[92:95]\n\t" \
  "ds_read_b128 v[132:135], v4 offset:1088\n\t" \
  "v_mul_f32 v158, %0, v102\n\t" \
  "v_mfma_f32_16x16x32_f16 v[96:99], v[36:39], v[120:123], v[96:99]\n\t" \
  "v_mul_f32 v159, %0, v103\n\t" \
  "v_cvt_pk_bf16_f32 v156, v156, v157\n\t" \
  "v_mfma_f32_16x16x32_f16 v[92:95], v[68:71], v[120:123], v[92:95]\n\t" \
  "ds_read_b128 v[120:123], v4 offset:1152\n\t" \
  "v_cvt_pk_bf16_f32 v157, v158, v159\n\t" \
  "v_mfma_f32_16x16x32_f16 v[96:99], v[32:35], v[128:131], v[96:99]\n\t" \
  "ds_write_b64 v3, v[156:157] offset:8192\n\t" \
  "ds_write_b32 v11, v9 offset:128\n\t" \
  "v_mfma_f32_16x16x32_f16 v[92:95], v[64:67], v[128:131], v[92:95]\n\t" \
  "ds_read_b128 v[128:131], v4 offset:1216\n\t" \
  "v_mfma_f32_16x16x32_f16 v[96:99], v[28:31], v[136:139], v[96:99]\n\t" \
  "v_mfma_f32_16x16x32_f16 v[92:95], v[60:63], v[136:139], v[92:95]\n\t" \
  "ds_read_b128 v[136:139], v4 offset:1280\n\t" \
  "ds_read_b32 v164, v8 offset:80\n\t" \
  "v_mfma_f32_16x16x32_f16 v[96:99], v[24:27], v[140:143], v[96:99]\n\t" \
  "v_mfma_f32_16x16x32_f16 v[92:95], v[56:59], v[140:143], v[92:95]\n\t" \
  "ds_read_b128 v[140:143], v4 offset:1344\n\t" \
  "v_mfma_f32_16x16x32_f16 v[96:99], v[20:23], v[144:147], v[96:99]\n\t" \
  "v_mfma_f32_16x16x32_f16 v[92:95], v[52:55], v[144:147], v[92:95]\n\t" \
  "ds_read_b128 v[144:147], v4 offset:1408\n\t" \
  "s_waitcnt lgkmcnt(8)\n\t" \
  "v_mfma_f32_16x16x32_f16 v[96:99], v[16:19], v[148:151], v[96:99]\n\t" \
  "v_mfma_f32_16x16x32_f16 v[92:95], v[48:51], v[148:151], v[92:95]\n\t" \
  "v_mfma_f32_16x16x32_f16 v[88:91], v[80:83], v[148:151], 0\n\t" \
  "v_mfma_f32_16x16x32_f16 v[108:111], v[84:87], v[148:151], v[108:111]\n\t" \
  "ds_read_b128 v[148:151], v4 offset:1472\n\t" \
  "v_mfma_f32_16x16x32_f16 v[92:95], v[44:47], v[124:127], v[92:95]\n\t" \
  "v_mfma_f32_16x16x32_f16 v[88:91], v[76:79], v[124:127], v[88:91]\n\t" \
  "ds_read_b128 v[124:127], v4 offset:1536\n\t" \
  "s_nop 0\n\t" \
  "v_mfma_f32_16x16x32_f16 v[92:95], v[40:43], v[132:135], v[92:95]\n\t" \
  "v_mul_f32 v156, %0, v96\n\t" \
  "v_mul_f32 v157, %0, v97\n\t" \
  "v_mfma_f32_16x16x32_f16 v[88:91], v[72:75], v[132:135], v[88:91]\n\t" \
  "ds_read_b128 v[132:135], v4 offset:1600\n\t" \
  "s_waitcnt lgkmcnt(4)\n\t" \
  "v_mfma_f32_16x16x32_f16 v[92:95], v[36:39], v[120:123], v[92:95]\n\t" \
  "v_mul_f32 v158, %0, v98\n\t" \
  "v_mul_f32 v159, %0, v99\n\t" \
  "v_mfma_f32_16x16x32_f16 v[88:91], v[68:71], v[120:123], v[88:91]\n\t" \
  "ds_read_b128 v[120:123], v4 offset:1664\n\t" \
  "v_cvt_pk_bf16_f32 v156, v156, v157\n\t" \
  "v_mfma_f32_16x16x32_f16 v[92:95], v[32:35], v[128:131], v[92:95]\n\t" \
  "v_cvt_pk_bf16_f32 v157, v158, v159\n\t" \
  "ds_write_b64 v3, v[156:157] offset:8704\n\t" \
  "v_mfma_f32_16x16x32_f16 v[88:91], v[64:67], v[128:131], v[88:91]\n\t" \
  "ds_read_b128 v[128:131], v4 offset:1728\n\t" \
  "ds_write_b32 v11, v9 offset:144\n\t" \
  "v_mfma_f32_16x16x32_f16 v[92:95], v[28:31], v[136:139], v[92:95]\n\t" \
  "v_mfma_f32_16x16x32_f16 v[88:91], v[60:63], v[136:139], v[88:91]\n\t" \
  "ds_read_b128 v[136:139], v4 offset:1792\n\t" \
  "v_mfma_f32_16x16x32_f16 v[92:95], v[24:27], v[140:143], v[92:95]\n\t" \
  "v_mfma_f32_16x16x32_f16 v[88:91], v[56:59], v[140:143], v[88:91]\n\t" \
  "ds_read_b128 v[140:143], v4 offset:1856\n\t" \
  "s_waitcnt lgkmcnt(7)\n\t" \
  "v_mfma_f32_16x16x32_f16 v[92:95], v[20:23], v[144:147], v[92:95]\n\t" \
  "v_cmp_eq_u32 vcc, 0, v164\n\t" \
  "s_cbranch_vccnz 52f\n\t" \
  "53:\n\t" \
  "v_mfma_f32_16x16x32_f16 v[88:91], v[52:55], v[144:147], v[88:91]\n\t" \
  "ds_read_b128 v[144:147], v4 offset:1920\n\t" \
  "ds_read_b32 v160, v11 offset:256\n\t" \
  "v_mfma_f32_16x16x32_f16 v[92:95], v[16:19], v[148:151], v[92:95]\n\t" \
  "ds_read_b128 v[152:155], v2 offset:0\n\t" \
  "v_mfma_f32_16x16x32_f16 v[88:91], v[48:51], v[148:151], v[88:91]\n\t" \
  "v_mfma_f32_16x16x32_f16 v[104:107], v[80:83], v[148:151], v[104:107]\n\t" \
  "v_mfma_f32_16x16x32_f16 v[112:115], v[84:87], v[148:151], v[112:115]\n\t" \
  "ds_read_b128 v[148:151], v4 offset:1984\n\t" \
  "v_mfma_f32_16x16x32_f16 v[88:91], v[44:47], v[124:127], v[88:91]\n\t" \
  "v_mfma_f32_16x16x32_f16 v[104:107], v[76:79], v[124:127], v[104:107]\n\t" \
  "ds_read_b128 v[124:127], v4 offset:2048\n\t" \
  "s_waitcnt lgkmcnt(6)\n\t" \
  "v_mfma_f32_16x16x32_f16 v[88:91], v[40:43], v[132:135], v[88:91]\n\t" \
  "v_mfma_f32_16x16x32_f16 v[104:107], v[72:75], v[132:135], v[104:107]\n\t" \
  "ds_read_b128 v[132:135], v4 offset:2112\n\t" \
  "v_mfma_f32_16x16x32_f16 v[88:91], v[36:39], v[120:123], v[88:91]\n\t" \
  "v_mfma_f32_16x16x32_f16 v[104:107], v[68:71], v[120:123], v[104:107]\n\t" \
  "ds_read_b128 v[120:123], v4 offset:2176\n\t" \
  "v_mfma_f32_16x16x32_f16 v[88:91], v[32:35], v[128:131], v[88:91]\n\t" \
  "v_mfma_f32_16x16x32_f16 v[104:107], v[64:67], v[128:131], v[104:107]\n\t" \
  "ds_read_b128 v[128:131], v4 offset:2240\n\t" \
  "v_mfma_f32_16x16x32_f16 v[88:91], v[28:31], v[136:139], v[88:91]\n\t" \
  "s_waitcnt lgkmcnt(5)\n\t" \
  "v_fma_f32 v156, v92, %0, v152\n\t" \
  "v_mfma_f32_16x16x32_f16 v[104:107], v[60:63], v[136:139], v[104:107]\n\t" \
  "ds_read_b128 v[136:139], v4 offset:2304\n\t" \
  "v_fma_f32 v157, v93, %0, v153\n\t" \
  "v_mfma_f32_16x16x32_f16 v[88:91], v[24:27], v[140:143], v[88:91]\n\t" \
  "v_fma_f32 v158, v94, %0, v154\n\t" \
  "v_fma_f32 v159, v95, %0, v155\n\t" \
  "v_mfma_f32_16x16x32_f16 v[104:107], v[56:59], v[140:143], v[104:107]\n\t" \
  "ds_read_b128 v[140:143], v4 offset:2368\n\t" \
  "v_cvt_pk_bf16_f32 v156, v156, v157\n\t" \
  "v_mfma_f32_16x16x32_f16 v[88:91], v[20:23], v[144:147], v[88:91]\n\t" \
  "v_cvt_pk_bf16_f32 v157, v158, v159\n\t" \
  "ds_write_b64 v3, v[156:157] offset:9216\n\t" \
  "v_mfma_f32_16x16x32_f16 v[104:107], v[52:55], v[144:147], v[104:107]\n\t" \
  "ds_read_b128 v[144:147], v4 offset:2432\n\t" \
  "s_waitcnt lgkmcnt(6)\n\t" \
  "v_mfma_f32_16x16x32_f16 v[88:91], v[16:19], v[148:151], v[88:91]\n\t" \
  "ds_write_b32 v11, v160 offset:160\n\t" \
  "ds_read_b32 v164, v8 offset:88\n\t" \
  "v_mfma_f32_16x16x32_f16 v[104:107], v[48:51], v[148:151], v[104:107]\n\t" \
  "v_mfma_f32_16x16x32_f16 v[108:111], v[80:83], v[148:151], v[108:111]\n\t" \
  "v_mfma_f32_16x16x32_f16 v[116:119], v[84:87], v[148:151], v[116:119]\n\t" \
  "ds_read_b128 v[148:151], v4 offset:2496\n\t" \
  "ds_read_b128 v[84:87], v4 offset:3008\n\t" \
  "v_mfma_f32_16x16x32_f16 v[104:107], v[44:47], v[124:127], v[104:107]\n\t" \
  "ds_read_b64 v[96:97], v7 offset:0\n\t" \
  "ds_read_b64 v[98:99], v7 offset:8\n\t" \
  "v_mfma_f32_16x16x32_f16 v[108:111], v[76:79], v[124:127], v[108:111]\n\t" \
  "ds_read_b128 v[124:127], v4 offset:2560\n\t" \
  "ds_read_b64 v[100:101], v7 offset:128\n\t" \
  "v_mfma_f32_16x16x32_f16 v[104:107], v[40:43], v[132:135], v[104:107]\n\t" \
  "ds_read_b64 v[102:103], v7 offset:136\n\t" \
  "ds_read_b32 v161, v11 offset:272\n\t" \
  "v_mfma_f32_16x16x32_f16 v[108:111], v[72:75], v[132:135], v[108:111]\n\t" \
  "ds_read_b128 v[132:135], v4 offset:2624\n\t" \
  "s_waitcnt lgkmcnt(13)\n\t" \
  "v_mfma_f32_16x16x32_f16 v[104:107], v[36:39], v[120:123], v[104:107]\n\t" \
  "ds_read_b128 v[152:155], v2 offset:1024\n\t" \
  "v_mfma_f32_16x16x32_f16 v[108:111], v[68:71], v[120:123], v[108:111]\n\t" \
  "ds_read_b128 v[120:123], v4 offset:2688\n\t" \
  "v_mfma_f32_16x16x32_f16 v[104:107], v[32:35], v[128:131], v[104:107]\n\t" \
  "v_mfma_f32_16x16x32_f16 v[108:111], v[64:67], v[128:131], v[108:111]\n\t" \
  "ds_read_b128 v[128:131], v4 offset:2752\n\t" \
  "v_mfma_f32_16x16x32_f16 v[104:107], v[28:31], v[136:139], v[104:107]\n\t" \
  "s_waitcnt lgkmcnt(12)\n\t" \
  "v_cmp_eq_u32 vcc, 0, v164\n\t" \
  "v_mfma_f32_16x16x32_f16 v[108:111], v[60:63], v[136:139], v[108:111]\n\t" \
  "ds_read_b128 v[136:139], v4 offset:2816\n\t" \
  "s_waitcnt lgkmcnt(9)\n\t" \
  "v_mfma_f32_16x16x32_f16 v[96:99], v[84:87], v[96:99], 0\n\t" \
  "s_cbranch_vccnz 54f\n\t" \
  "55:\n\t" \
  "s_waitcnt lgkmcnt(6)\n\t" \
  "v_mfma_f32_16x16x32_f16 v[100:103], v[84:87], v[100:103], 0\n\t" \
  "v_mfma_f32_16x16x32_f16 v[104:107], v[24:27], v[140:143], v[104:107]\n\t" \
  "v_mfma_f32_16x16x32_f16 v[108:111], v[56:59], v[140:143], v[108:111]\n\t" \
  "ds_read_b128 v[140:143], v4 offset:2880\n\t" \
  "s_nop 0\n\t" \
  "v_mfma_f32_16x16x32_f16 v[104:107], v[20:23], v[144:147], v[104:107]\n\t" \
  "v_cvt_pk_f16_f32 v84, v96, v97\n\t" \
  "v_cvt_pk_f16_f32 v85, v98, v99\n\t" \
  "v_mfma_f32_16x16x32_f16 v[108:111], v[52:55], v[144:147], v[108:111]\n\t" \
  "ds_read_b128 v[144:147], v4 offset:2944\n\t" \
  "v_cvt_pk_f16_f32 v86, v100, v101\n\t" \
  "v_mfma_f32_16x16x32_f16 v[104:107], v[16:19], v[148:151], v[104:107]\n\t" \
  "v_cvt_pk_f16_f32 v87, v102, v103\n\t" \
  "ds_read_b64 v[96:97], v6 offset:1560\n\t" \
  "v_mfma_f32_16x16x32_f16 v[108:111], v[48:51], v[148:151], v[108:111]\n\t" \
  "ds_read_b64 v[98:99], v6 offset:1464\n\t" \
  "v_mfma_f32_16x16x32_f16 v[112:115], v[80:83], v[148:151], v[112:115]\n\t" \
  "ds_read_b128 v[148:151], v4 offset:3008\n\t" \
  "v_mfma_f32_16x16x32_f16 v[108:111], v[44:47], v[124:127], v[108:111]\n\t" \
  "s_waitcnt lgkmcnt(8)\n\t" \
  "v_fma_f32 v156, v88, %0, v152\n\t" \
  "v_mfma_f32_16x16x32_f16 v[112:115], v[76:79], v[124:127], v[112:115]\n\t" \
  "ds_read_b128 v[124:127], v4 offset:3072\n\t" \
  "v_fma_f32 v157, v89, %0, v153\n\t" \
  "v_mfma_f32_16x16x32_f16 v[108:111], v[40:43], v[132:135], v[108:111]\n\t" \
  "v_fma_f32 v158, v90, %0, v154\n\t" \
  "v_fma_f32 v159, v91, %0, v155\n\t" \
  "v_mfma_f32_16x16x32_f16 v[112:115], v[72:75], v[132:135], v[112:115]\n\t" \
  "ds_read_b128 v[132:135], v4 offset:3136\n\t" \
  "s_waitcnt lgkmcnt(6)\n\t" \
  "v_mfma_f32_16x16x32_f16 v[108:111], v[36:39], v[120:123], v[108:111]\n\t" \
  "v_cvt_pk_bf16_f32 v156, v156, v157\n\t" \
  "v_cvt_pk_bf16_f32 v157, v158, v159\n\t" \
  "v_mfma_f32_16x16x32_f16 v[112:115], v[68:71], v[120:123], v[112:115]\n\t" \
  "ds_read_b128 v[120:123], v4 offset:3200\n\t" \
  "ds_write_b64 v3, v[156:157] offset:9728\n\t" \
  "v_mfma_f32_16x16x32_f16 v[108:111], v[32:35], v[128:131], v[108:111]\n\t" \
  "ds_write_b32 v11, v161 offset:176\n\t" \
  "v_mfma_f32_16x16x32_f16 v[112:115], v[64:67], v[128:131], v[112:115]\n\t" \
  "ds_read_b128 v[128:131], v4 offset:3264\n\t" \
  "s_waitcnt lgkmcnt(7)\n\t" \
  "v_mfma_f32_16x16x32_f16 v[116:119], v[84:87], v[96:99], v[116:119]\n\t" \
  "ds_read_b128 v[84:87], v4 offset:2992\n\t" \
  "ds_read_b64 v[96:97], v7 offset:0\n\t" \
  "v_mfma_f32_16x16x32_f16 v[108:111], v[28:31], v[136:139], v[108:111]\n\t" \
  "ds_read_b64 v[98:99], v7 offset:8\n\t" \
  "ds_read_b64 v[100:101], v7 offset:128\n\t" \
  "v_mfma_f32_16x16x32_f16 v[112:115], v[60:63], v[136:139], v[112:115]\n\t" \
  "ds_read_b128 v[136:139], v4 offset:3328\n\t" \
  "ds_read_b64 v[102:103], v7 offset:136\n\t" \
  "v_mfma_f32_16x16x32_f16 v[108:111], v[24:27], v[140:143], v[108:111]\n\t" \
  "ds_read_b32 v162, v11 offset:288\n\t" \
  "ds_read_b128 v[152:155], v2 offset:2048\n\t" \
  "v_mfma_f32_16x16x32_f16 v[112:115], v[56:59], v[140:143], v[112:115]\n\t" \
  "ds_read_b128 v[140:143], v4 offset:3392\n\t" \
  "v_mfma_f32_16x16x32_f16 v[108:111], v[20:23], v[144:147], v[108:111]\n\t" \
  "v_mfma_f32_16x16x32_f16 v[112:115], v[52:55], v[144:147], v[112:115]\n\t" \
  "ds_read_b128 v[144:147], v4 offset:3456\n\t" \
  "s_waitcnt lgkmcnt(13)\n\t" \
  "v_mfma_f32_16x16x32_f16 v[108:111], v[16:19], v[148:151], v[108:111]\n\t" \
  "v_mfma_f32_16x16x32_f16 v[112:115], v[48:51], v[148:151], v[112:115]\n\t" \
  "v_mfma_f32_16x16x32_f16 v[116:119], v[80:83], v[148:151], v[116:119]\n\t" \
  "ds_read_b128 v[148:151], v4 offset:3520\n\t" \
  "v_mfma_f32_16x16x32_f16 v[112:115], v[44:47], v[124:127], v[112:115]\n\t" \
  "v_mfma_f32_16x16x32_f16 v[116:119], v[76:79], v[124:127], v[116:119]\n\t" \
  "ds_read_b128 v[124:127], v4 offset:3584\n\t" \
  "s_waitcnt lgkmcnt(6)\n\t" \
  "v_mfma_f32_16x16x32_f16 v[96:99], v[84:87], v[96:99], 0\n\t" \
  "v_mfma_f32_16x16x32_f16 v[100:103], v[84:87], v[100:103], 0\n\t" \
  "s_waitcnt lgkmcnt(4)\n\t" \
  "v_fma_f32 v156, v104, %0, v152\n\t" \
  "v_mfma_f32_16x16x32_f16 v[112:115], v[40:43], v[132:135], v[112:115]\n\t" \
  "v_fma_f32 v157, v105, %0, v153\n\t" \
  "v_fma_f32 v158, v106, %0, v154\n\t" \
  "v_mfma_f32_16x16x32_f16 v[116:119], v[72:75], v[132:135], v[116:119]\n\t" \
  "ds_read_b128 v[132:135], v4 offset:3648\n\t" \
  "v_fma_f32 v159, v107, %0, v155\n\t" \
  "v_mfma_f32_16x16x32_f16 v[112:115], v[36:39], v[120:123], v[112:115]\n\t" \
  "v_cvt_pk_bf16_f32 v156, v156, v157\n\t" \
  "v_cvt_pk_bf16_f32 v157, v158, v159\n\t" \
  "v_mfma_f32_16x16x32_f16 v[116:119], v[68:71], v[120:123], v[116:119]\n\t" \
  "ds_read_b128 v[120:123], v4 offset:3712\n\t" \
  "ds_write_b64 v3, v[156:157] offset:10240\n\t" \
  "v_mfma_f32_16x16x32_f16 v[112:115], v[32:35], v[128:131], v[112:115]\n\t" \
  "ds_write_b32 v11, v162 offset:192\n\t" \
  "v_cvt_pk_f16_f32 v84, v96, v97\n\t" \
  "v_mfma_f32_16x16x32_f16 v[116:119], v[64:67], v[128:131], v[116:119]\n\t" \
  "ds_read_b128 v[128:131], v4 offset:3776\n\t" \
  "v_cvt_pk_f16_f32 v85, v98, v99\n\t" \
  "v_mfma_f32_16x16x32_f16 v[112:115], v[28:31], v[136:139], v[112:115]\n\t" \
  "v_cvt_pk_f16_f32 v86, v100, v101\n\t" \
  "v_cvt_pk_f16_f32 v87, v102, v103\n\t" \
  "v_mfma_f32_16x16x32_f16 v[116:119], v[60:63], v[136:139], v[116:119]\n\t" \
  "ds_read_b128 v[136:139], v4 offset:3840\n\t" \
  "s_waitcnt lgkmcnt(6)\n\t" \
  "v_mfma_f32_16x16x32_f16 v[112:115], v[24:27], v[140:143], v[112:115]\n\t" \
  "ds_read_b64 v[96:97], v6 offset:1368\n\t" \
  "ds_read_b64 v[98:99], v6 offset:1272\n\t" \
  "v_mfma_f32_16x16x32_f16 v[116:119], v[56:59], v[140:143], v[116:119]\n\t" \
  "ds_read_b128 v[140:143], v4 offset:3904\n\t" \
  "ds_read_b32 v163, v11 offset:304\n\t" \
  "v_mfma_f32_16x16x32_f16 v[112:115], v[20:23], v[144:147], v[112:115]\n\t" \
  "ds_read_b128 v[152:155], v2 offset:3072\n\t" \
  "v_mfma_f32_16x16x32_f16 v[116:119], v[52:55], v[144:147], v[116:119]\n\t" \
  "ds_read_b128 v[144:147], v4 offset:3968\n\t" \
  "v_mfma_f32_16x16x32_f16 v[112:115], v[16:19], v[148:151], v[112:115]\n\t" \
  "v_mfma_f32_16x16x32_f16 v[116:119], v[48:51], v[148:151], v[116:119]\n\t" \
  "ds_read_b128 v[148:151], v4 offset:4032\n\t" \
  "s_waitcnt lgkmcnt(5)\n\t" \
  "v_mfma_f32_16x16x32_f16 v[116:119], v[84:87], v[96:99], v[116:119]\n\t" \
  "v_mfma_f32_16x16x32_f16 v[116:119], v[44:47], v[124:127], v[116:119]\n\t" \
  "v_mfma_f32_16x16x32_f16 v[116:119], v[40:43], v[132:135], v[116:119]\n\t" \
  "v_mfma_f32_16x16x32_f16 v[116:119], v[36:39], v[120:123], v[116:119]\n\t" \
  "s_waitcnt lgkmcnt(2)\n\t" \
  "v_fma_f32 v156, v108, %0, v152\n\t" \
  "v_mfma_f32_16x16x32_f16 v[116:119], v[32:35], v[128:131], v[116:119]\n\t" \
  "v_fma_f32 v157, v109, %0, v153\n\t" \
  "v_fma_f32 v158, v110, %0, v154\n\t" \
  "v_mfma_f32_16x16x32_f16 v[116:119], v[28:31], v[136:139], v[116:119]\n\t" \
  "v_fma_f32 v159, v111, %0, v155\n\t" \
  "v_cvt_pk_bf16_f32 v156, v156, v157\n\t" \
  "v_mfma_f32_16x16x32_f16 v[116:119], v[24:27], v[140:143], v[116:119]\n\t" \
  "v_cvt_pk_bf16_f32 v157, v158, v159\n\t" \
  "s_waitcnt lgkmcnt(1)\n\t" \
  "v_mfma_f32_16x16x32_f16 v[116:119], v[20:23], v[144:147], v[116:119]\n\t" \
  "ds_write_b64 v3, v[156:157] offset:10752\n\t" \
  "s_waitcnt lgkmcnt(1)\n\t" \
  "v_mfma_f32_16x16x32_f16 v[116:119], v[16:19], v[148:151], v[116:119]\n\t" \
  "ds_write_b32 v11, v163 offset:208\n\t" \
  "ds_read_b32 v164, v11 offset:320\n\t" \
  "ds_read_b128 v[152:155], v2 offset:4096\n\t" \
  "ds_read_b128 v[100:103], v2 offset:5120\n\t" \
  "s_waitcnt lgkmcnt(1)\n\t" \
  "v_fma_f32 v156, v112, %0, v152\n\t" \
  "v_fma_f32 v157, v113, %0, v153\n\t" \
  "v_fma_f32 v158, v114, %0, v154\n\t" \
  "v_fma_f32 v159, v115, %0, v155\n\t" \
  "v_cvt_pk_bf16_f32 v156, v156, v157\n\t" \
  "v_cvt_pk_bf16_f32 v157, v158, v159\n\t" \
  "ds_write_b64 v3, v[156:157] offset:11264\n\t" \
  "s_waitcnt lgkmcnt(1)\n\t" \
  "v_fma_f32 v96, v116, %0, v100\n\t" \
  "v_fma_f32 v97, v117, %0, v101\n\t" \
  "v_fma_f32 v98, v118, %0, v102\n\t" \
  "v_fma_f32 v99, v119, %0, v103\n\t" \
  "v_cvt_pk_bf16_f32 v96, v96, v97\n\t" \
  "v_cvt_pk_bf16_f32 v97, v98, v99\n\t" \
  "ds_write_b64 v3, v[96:97] offset:11776\n\t" \
  "ds_write_b32 v11, v164 offset:224\n\t" \
  TSQ(11) \
  "v_and_b32 v160, v160, v161\n\t" \
  "v_and_b32 v162, v162, v163\n\t" \
  "v_bitop3_b32 v160, v160, v162, v164 bitop3:0x80\n\t" \
  "v_cmp_eq_u32 vcc, 0, v160\n\t" \
  "s_cbranch_vccz 40f\n\t" \
  "5:\n\t" \
  "ds_read_b32 v160, v11 offset:320\n\t" \
  "s_waitcnt lgkmcnt(0)\n\t" \
  "v_cmp_gt_u32 vcc, 1, v160\n\t" \
  "s_cbranch_vccz 6f\n\t" \
  "s_sleep 1\n\t" \
  "s_branch 5b\n\t" \
  "6:\n\t" \
  "ds_read_b128 v[152:155], v2 offset:0\n\t" \
  "v_mov_b32 v160, 1\n\t" \
  "s_waitcnt lgkmcnt(0)\n\t" \
  "v_fma_f32 v156, v92, %0, v152\n\t" \
  "v_fma_f32 v157, v93, %0, v153\n\t" \
  "v_fma_f32 v158, v94, %0, v154\n\t" \
  "v_fma_f32 v159, v95, %0, v155\n\t" \
  "v_cvt_pk_bf16_f32 v156, v156, v157\n\t" \
  "v_cvt_pk_bf16_f32 v157, v158, v159\n\t" \
  "ds_write_b64 v3, v[156:157] offset:9216\n\t" \
  "ds_write_b32 v11, v160 offset:160\n\t" \
  "ds_read_b128 v[152:155], v2 offset:1024\n\t" \
  "v_mov_b32 v161, 1\n\t" \
  "s_waitcnt lgkmcnt(0)\n\t" \
  "v_fma_f32 v156, v88, %0, v152\n\t" \
  "v_fma_f32 v157, v89, %0, v153\n\t" \
  "v_fma_f32 v158, v90, %0, v154\n\t" \
  "v_fma_f32 v159, v91, %0, v155\n\t" \
  "v_cvt_pk_bf16_f32 v156, v156, v157\n\t" \
  "v_cvt_pk_bf16_f32 v157, v158, v159\n\t" \
  "ds_write_b64 v3, v[156:157] offset:9728\n\t" \
  "ds_write_b32 v11, v161 offset:176\n\t" \
  "ds_read_b128 v[152:155], v2 offset:2048\n\t" \
  "v_mov_b32 v162, 1\n\t" \
  "s_waitcnt lgkmcnt(0)\n\t" \
  "v_fma_f32 v156, v104, %0, v152\n\t" \
  "v_fma_f32 v157, v105, %0, v153\n\t" \
  "v_fma_f32 v158, v106, %0, v154\n\t" \
  "v_fma_f32 v159, v107, %0, v155\n\t" \
  "v_cvt_pk_bf16_f32 v156, v156, v157\n\t" \
  "v_cvt_pk_bf16_f32 v157, v158, v159\n\t" \
  "ds_write_b64 v3, v[156:157] offset:10240\n\t" \
  "ds_write_b32 v11, v162 offset:192\n\t" \
  "ds_read_b128 v[152:155], v2 offset:3072\n\t" \
  "v_mov_b32 v163, 1\n\t" \
  "s_waitcnt lgkmcnt(0)\n\t" \
  "v_fma_f32 v156, v108, %0, v152\n\t" \
  "v_fma_f32 v157, v109, %0, v153\n\t" \
  "v_fma_f32 v158, v110, %0, v154\n\t" \
  "v_fma_f32 v159, v111, %0, v155\n\t" \
  "v_cvt_pk_bf16_f32 v156, v156, v157\n\t" \
  "v_cvt_pk_bf16_f32 v157, v158, v159\n\t" \
  "ds_write_b64 v3, v[156:157] offset:10752\n\t" \
  "ds_write_b32 v11, v163 offset:208\n\t" \
  "ds_read_b128 v[152:155], v2 offset:4096\n\t" \
  "v_mov_b32 v164, 1\n\t" \
  "s_waitcnt lgkmcnt(0)\n\t" \
  "v_fma_f32 v156, v112, %0, v152\n\t" \
  "v_fma_f32 v157, v113, %0, v153\n\t" \
  "v_fma_f32 v158, v114, %0, v154\n\t" \
  "v_fma_f32 v159, v115, %0, v155\n\t" \
  "v_cvt_pk_bf16_f32 v156, v156, v157\n\t" \
  "v_cvt_pk_bf16_f32 v157, v158, v159\n\t" \
  "ds_write_b64 v3, v[156:157] offset:11264\n\t" \
  "ds_read_b128 v[100:103], v2 offset:5120\n\t" \
  "v_mov_b32 v164, 1\n\t" \
  "s_waitcnt lgkmcnt(0)\n\t" \
  "v_fma_f32 v96, v116, %0, v100\n\t" \
  "v_fma_f32 v97, v117, %0, v101\n\t" \
  "v_fma_f32 v98, v118, %0, v102\n\t" \
  "v_fma_f32 v99, v119, %0, v103\n\t" \
  "v_cvt_pk_bf16_f32 v96, v96, v97\n\t" \
  "v_cvt_pk_bf16_f32 v97, v98, v99\n\t" \
  "ds_write_b64 v3, v[96:97] offset:11776\n\t" \
  "ds_write_b32 v11, v164 offset:224\n\t" \
  "s_branch 40f\n\t" \
  "50:\n\t" \
  "s_sleep 1\n\t" \
  "ds_read_b32 v164, v8 offset:72\n\t" \
  "s_waitcnt lgkmcnt(0)\n\t" \
  "v_cmp_eq_u32 vcc, 0, v164\n\t" \
  "s_cbranch_vccnz 50b\n\t" \
  "s_branch 51b\n\t" \
  "52:\n\t" \
  "s_sleep 1\n\t" \
  "ds_read_b32 v164, v8 offset:80\n\t" \
  "s_waitcnt lgkmcnt(0)\n\t" \
  "v_cmp_eq_u32 vcc, 0, v164\n\t" \
  "s_cbranch_vccnz 52b\n\t" \
  "s_branch 53b\n\t" \
  "54:\n\t" \
  "s_sleep 1\n\t" \
  "ds_read_b32 v164, v8 offset:88\n\t" \
  "s_waitcnt lgkmcnt(0)\n\t" \
  "v_cmp_eq_u32 vcc, 0, v164\n\t" \
  "s_cbranch_vccnz 54b\n\t" \
  "s_branch 55b\n\t" \
  "40:\n\t"

#define FAR_ASM \
  "v_and_b32 v1, 63, v0\n\t" \
  "s_mov_b32 s98, 157600\n\t" \
  "v_lshl_add_u32 v2, v1, 4, s98\n\t" \
  "ds_read_b128 v[4:7], v2 offset:0\n\t" \
  "v_lshl_add_u32 v2, v1, 4, %2\n\t" \
  "v_and_b32 v8, 1, v0\n\t" \
  "v_lshlrev_b32 v8, 2, v8\n\t" \
  "v_mov_b32 v9, 1\n\t" \
  "s_waitcnt lgkmcnt(0)\n\t" \
  "v_add_u32 v4, %1, v4\n\t" \
  "v_add_u32 v6, %1, v6\n\t" \
  "ds_read_b64 v[40:41], v7 offset:0\n\t" \
  "ds_read_b64 v[42:43], v7 offset:8\n\t" \
  "ds_read_b64 v[44:45], v7 offset:128\n\t" \
  "ds_read_b64 v[46:47], v7 offset:136\n\t" \
  "1:\n\t" \
  "ds_read_b32 v144, v11 offset:32\n\t" \
  "s_waitcnt lgkmcnt(0)\n\t" \
  "v_cmp_gt_u32 vcc, 4, v144\n\t" \
  "s_cbranch_vccz 2f\n\t" \
  "s_sleep 1\n\t" \
  "s_branch 1b\n\t" \
  "2:\n\t" \
  "ds_read_b64 v[16:17], v6 offset:1560\n\t" \
  "ds_read_b64 v[18:19], v6 offset:1464\n\t" \
  "ds_read_b64 v[20:21], v6 offset:1368\n\t" \
  "ds_read_b64 v[22:23], v6 offset:1272\n\t" \
  "ds_read_b64 v[24:25], v6 offset:1528\n\t" \
  "ds_read_b64 v[26:27], v6 offset:1432\n\t" \
  "ds_read_b64 v[28:29], v6 offset:1336\n\t" \
  "ds_read_b64 v[30:31], v6 offset:1240\n\t" \
  "ds_read_b64 v[32:33], v6 offset:1496\n\t" \
  "ds_read_b64 v[34:35], v6 offset:1400\n\t" \
  "ds_read_b64 v[36:37], v6 offset:1304\n\t" \
  "ds_read_b64 v[38:39], v6 offset:1208\n\t" \
  TSQ(7) \
  "3:\n\t" \
  "ds_read_b32 v144, v8 offset:64\n\t" \
  "s_waitcnt lgkmcnt(0)\n\t" \
  "v_cmp_gt_u32 vcc, 1, v144\n\t" \
  "s_cbranch_vccz 4f\n\t" \
  "s_sleep 1\n\t" \
  "s_branch 3b\n\t" \
  "4:\n\t" \
  TSQ(8) \
  "s_setprio 3\n\t" \
  "ds_read_b128 v[48:51], v4 offset:448\n\t" \
  "ds_read_b128 v[52:55], v4 offset:432\n\t" \
  "ds_read_b128 v[56:59], v4 offset:960\n\t" \
  "ds_read_b128 v[60:63], v4 offset:944\n\t" \
  "s_waitcnt lgkmcnt(3)\n\t" \
  "v_mfma_f32_16x16x32_f16 v[72:75], v[48:51], v[40:43], 0\n\t" \
  "v_mfma_f32_16x16x32_f16 v[76:79], v[48:51], v[44:47], 0\n\t" \
  "s_waitcnt lgkmcnt(2)\n\t" \
  "v_mfma_f32_16x16x32_f16 v[80:83], v[52:55], v[40:43], 0\n\t" \
  "v_mfma_f32_16x16x32_f16 v[84:87], v[52:55], v[44:47], 0\n\t" \
  "ds_read_b32 v144, v8 offset:72\n\t" \
  "s_waitcnt lgkmcnt(2)\n\t" \
  "v_mfma_f32_16x16x32_f16 v[88:91], v[56:59], v[40:43], 0\n\t" \
  "s_nop 0\n\t" \
  "v_cvt_pk_f16_f32 v104, v72, v73\n\t" \
  "v_mfma_f32_16x16x32_f16 v[92:95], v[56:59], v[44:47], 0\n\t" \
  "v_cvt_pk_f16_f32 v105, v74, v75\n\t" \
  "s_waitcnt lgkmcnt(1)\n\t" \
  "v_mfma_f32_16x16x32_f16 v[96:99], v[60:63], v[40:43], 0\n\t" \
  "v_cvt_pk_f16_f32 v106, v76, v77\n\t" \
  "v_cvt_pk_f16_f32 v107, v78, v79\n\t" \
  "v_mfma_f32_16x16x32_f16 v[100:103], v[60:63], v[44:47], 0\n\t" \
  "v_cvt_pk_f16_f32 v108, v80, v81\n\t" \
  "v_cvt_pk_f16_f32 v109, v82, v83\n\t" \
  "v_mfma_f32_16x16x32_f16 v[128:131], v[104:107], v[16:19], 0\n\t" \
  "v_cvt_pk_f16_f32 v110, v84, v85\n\t" \
  "v_cvt_pk_f16_f32 v111, v86, v87\n\t" \
  "s_nop 1\n\t" \
  "v_mfma_f32_16x16x32_f16 v[128:131], v[108:111], v[20:23], v[128:131]\n\t" \
  "v_cvt_pk_f16_f32 v112, v88, v89\n\t" \
  "v_cvt_pk_f16_f32 v113, v90, v91\n\t" \
  "v_mfma_f32_16x16x32_f16 v[132:135], v[104:107], v[24:27], 0\n\t" \
  "s_waitcnt lgkmcnt(0)\n\t" \
  "v_cmp_eq_u32 vcc, 0, v144\n\t" \
  "v_mfma_f32_16x16x32_f16 v[132:135], v[108:111], v[28:31], v[132:135]\n\t" \
  "s_cbranch_vccnz 50f\n\t" \
  "51:\n\t" \
  "ds_read_b128 v[64:67], v4 offset:1472\n\t" \
  "v_mfma_f32_16x16x32_f16 v[120:123], v[104:107], v[32:35], 0\n\t" \
  "ds_read_b128 v[68:71], v4 offset:1456\n\t" \
  "ds_read_b128 v[48:51], v4 offset:1984\n\t" \
  "v_mfma_f32_16x16x32_f16 v[120:123], v[108:111], v[36:39], v[120:123]\n\t" \
  "ds_read_b128 v[52:55], v4 offset:1968\n\t" \
  "s_waitcnt lgkmcnt(3)\n\t" \
  "v_mfma_f32_16x16x32_f16 v[72:75], v[64:67], v[40:43], 0\n\t" \
  "v_cvt_pk_f16_f32 v114, v92, v93\n\t" \
  "v_cvt_pk_f16_f32 v115, v94, v95\n\t" \
  "v_mfma_f32_16x16x32_f16 v[76:79], v[64:67], v[44:47], 0\n\t" \
  "v_cvt_pk_f16_f32 v116, v96, v97\n\t" \
  "s_waitcnt lgkmcnt(2)\n\t" \
  "v_mfma_f32_16x16x32_f16 v[80:83], v[68:71], v[40:43], 0\n\t" \
  "v_cvt_pk_f16_f32 v117, v98, v99\n\t" \
  "v_cvt_pk_f16_f32 v118, v100, v101\n\t" \
  "v_mfma_f32_16x16x32_f16 v[84:87], v[68:71], v[44:47], 0\n\t" \
  "v_cvt_pk_f16_f32 v119, v102, v103\n\t" \
  "v_mul_f32 v136, %0, v128\n\t" \
  "v_mfma_f32_16x16x32_f16 v[132:135], v[112:115], v[16:19], v[132:135]\n\t" \
  "v_mul_f32 v137, %0, v129\n\t" \
  "v_mul_f32 v138, %0, v130\n\t" \
  "v_mfma_f32_16x16x32_f16 v[132:135], v[116:119], v[20:23], v[132:135]\n\t" \
  "v_mul_f32 v139, %0, v131\n\t" \
  "ds_write_b128 v2, v[136:139] offset:0\n\t" \
  "v_mfma_f32_16x16x32_f16 v[120:123], v[112:115], v[24:27], v[120:123]\n\t" \
  "ds_write_b32 v11, v9 offset:256\n\t" \
  "s_setprio 2\n\t" \
  "v_mfma_f32_16x16x32_f16 v[120:123], v[116:119], v[28:31], v[120:123]\n\t" \
  "ds_read_b32 v144, v8 offset:80\n\t" \
  "v_cvt_pk_f16_f32 v104, v72, v73\n\t" \
  "v_mfma_f32_16x16x32_f16 v[124:127], v[112:115], v[32:35], 0\n\t" \
  "v_cvt_pk_f16_f32 v105, v74, v75\n\t" \
  "v_cvt_pk_f16_f32 v106, v76, v77\n\t" \
  "v_mfma_f32_16x16x32_f16 v[124:127], v[116:119], v[36:39], v[124:127]\n\t" \
  "v_cvt_pk_f16_f32 v107, v78, v79\n\t" \
  "s_waitcnt lgkmcnt(3)\n\t" \
  "v_mfma_f32_16x16x32_f16 v[88:91], v[48:51], v[40:43], 0\n\t" \
  "v_cvt_pk_f16_f32 v108, v80, v81\n\t" \
  "v_cvt_pk_f16_f32 v109, v82, v83\n\t" \
  "v_mfma_f32_16x16x32_f16 v[92:95], v[48:51], v[44:47], 0\n\t" \
  "v_cvt_pk_f16_f32 v110, v84, v85\n\t" \
  "v_cvt_pk_f16_f32 v111, v86, v87\n\t" \
  "v_mfma_f32_16x16x32_f16 v[96:99], v[52:55], v[40:43], 0\n\t" \
  "s_waitcnt lgkmcnt(0)\n\t" \
  "v_cmp_eq_u32 vcc, 0, v144\n\t" \
  "v_mfma_f32_16x16x32_f16 v[100:103], v[52:55], v[44:47], 0\n\t" \
  "s_cbranch_vccnz 52f\n\t" \
  "53:\n\t" \
  "ds_read_b128 v[56:59], v4 offset:2496\n\t" \
  "v_mfma_f32_16x16x32_f16 v[120:123], v[104:107], v[16:19], v[120:123]\n\t" \
  "ds_read_b128 v[60:63], v4 offset:2480\n\t" \
  "v_mul_f32 v140, %0, v132\n\t" \
  "v_mfma_f32_16x16x32_f16 v[120:123], v[108:111], v[20:23], v[120:123]\n\t" \
  "v_mul_f32 v141, %0, v133\n\t" \
  "v_mul_f32 v142, %0, v134\n\t" \
  "v_mfma_f32_16x16x32_f16 v[124:127], v[104:107], v[24:27], v[124:127]\n\t" \
  "v_mul_f32 v143, %0, v135\n\t" \
  "ds_write_b128 v2, v[140:143] offset:1024\n\t" \
  "v_mfma_f32_16x16x32_f16 v[124:127], v[108:111], v[28:31], v[124:127]\n\t" \
  "ds_write_b32 v11, v9 offset:272\n\t" \
  "s_setprio 1\n\t" \
  "v_mfma_f32_16x16x32_f16 v[128:131], v[104:107], v[32:35], 0\n\t" \
  "v_cvt_pk_f16_f32 v112, v88, v89\n\t" \
  "v_cvt_pk_f16_f32 v113, v90, v91\n\t" \
  "v_mfma_f32_16x16x32_f16 v[128:131], v[108:111], v[36:39], v[128:131]\n\t" \
  "v_cvt_pk_f16_f32 v114, v92, v93\n\t" \
  "s_waitcnt lgkmcnt(3)\n\t" \
  "v_mfma_f32_16x16x32_f16 v[72:75], v[56:59], v[40:43], 0\n\t" \
  "v_cvt_pk_f16_f32 v115, v94, v95\n\t" \
  "v_cvt_pk_f16_f32 v116, v96, v97\n\t" \
  "v_mfma_f32_16x16x32_f16 v[76:79], v[56:59], v[44:47], 0\n\t" \
  "v_cvt_pk_f16_f32 v117, v98, v99\n\t" \
  "s_waitcnt lgkmcnt(2)\n\t" \
  "v_mfma_f32_16x16x32_f16 v[80:83], v[60:63], v[40:43], 0\n\t" \
  "v_cvt_pk_f16_f32 v118, v100, v101\n\t" \
  "v_cvt_pk_f16_f32 v119, v102, v103\n\t" \
  "v_mfma_f32_16x16x32_f16 v[84:87], v[60:63], v[44:47], 0\n\t" \
  "v_mul_f32 v136, %0, v120\n\t" \
  "v_mul_f32 v137, %0, v121\n\t" \
  "v_mfma_f32_16x16x32_f16 v[124:127], v[112:115], v[16:19], v[124:127]\n\t" \
  "v_mul_f32 v138, %0, v122\n\t" \
  "v_mul_f32 v139, %0, v123\n\t" \
  "v_mfma_f32_16x16x32_f16 v[124:127], v[116:119], v[20:23], v[124:127]\n\t" \
  "ds_write_b128 v2, v[136:139] offset:2048\n\t" \
  "ds_write_b32 v11, v9 offset:288\n\t" \
  "v_mfma_f32_16x16x32_f16 v[128:131], v[112:115], v[24:27], v[128:131]\n\t" \
  "s_setprio 0\n\t" \
  "v_cvt_pk_f16_f32 v104, v72, v73\n\t" \
  "v_mfma_f32_16x16x32_f16 v[128:131], v[116:119], v[28:31], v[128:131]\n\t" \
  "v_cvt_pk_f16_f32 v105, v74, v75\n\t" \
  "v_cvt_pk_f16_f32 v106, v76, v77\n\t" \
  "v_mfma_f32_16x16x32_f16 v[132:135], v[112:115], v[32:35], 0\n\t" \
  "v_cvt_pk_f16_f32 v107, v78, v79\n\t" \
  "v_cvt_pk_f16_f32 v108, v80, v81\n\t" \
  "v_mfma_f32_16x16x32_f16 v[132:135], v[116:119], v[36:39], v[132:135]\n\t" \
  "v_cvt_pk_f16_f32 v109, v82, v83\n\t" \
  "v_cvt_pk_f16_f32 v110, v84, v85\n\t" \
  "v_mfma_f32_16x16x32_f16 v[128:131], v[104:107], v[16:19], v[128:131]\n\t" \
  "v_cvt_pk_f16_f32 v111, v86, v87\n\t" \
  "v_mul_f32 v140, %0, v124\n\t" \
  "s_nop 0\n\t" \
  "v_mfma_f32_16x16x32_f16 v[128:131], v[108:111], v[20:23], v[128:131]\n\t" \
  "v_mul_f32 v141, %0, v125\n\t" \
  "v_mul_f32 v142, %0, v126\n\t" \
  "v_mfma_f32_16x16x32_f16 v[132:135], v[104:107], v[24:27], v[132:135]\n\t" \
  "v_mul_f32 v143, %0, v127\n\t" \
  "ds_write_b128 v2, v[140:143] offset:3072\n\t" \
  "v_mfma_f32_16x16x32_f16 v[132:135], v[108:111], v[28:31], v[132:135]\n\t" \
  "ds_write_b32 v11, v9 offset:304\n\t" \
  "s_nop 0\n\t" \
  "v_mul_f32 v136, %0, v128\n\t" \
  "v_mul_f32 v137, %0, v129\n\t" \
  "v_mul_f32 v138, %0, v130\n\t" \
  "v_mul_f32 v139, %0, v131\n\t" \
  "ds_write_b128 v2, v[136:139] offset:4096\n\t" \
  "s_nop 0\n\t" \
  "v_mul_f32 v140, %0, v132\n\t" \
  "v_mul_f32 v141, %0, v133\n\t" \
  "v_mul_f32 v142, %0, v134\n\t" \
  "v_mul_f32 v143, %0, v135\n\t" \
  "ds_write_b128 v2, v[140:143] offset:5120\n\t" \
  "ds_write_b32 v11, v9 offset:320\n\t" \
  "s_branch 40f\n\t" \
  "50:\n\t" \
  "s_sleep 1\n\t" \
  "ds_read_b32 v144, v8 offset:72\n\t" \
  "s_waitcnt lgkmcnt(0)\n\t" \
  "v_cmp_eq_u32 vcc, 0, v144\n\t" \
  "s_cbranch_vccnz 50b\n\t" \
  "s_branch 51b\n\t" \
  "52:\n\t" \
  "s_sleep 1\n\t" \
  "ds_read_b32 v144, v8 offset:80\n\t" \
  "s_waitcnt lgkmcnt(0)\n\t" \
  "v_cmp_eq_u32 vcc, 0, v144\n\t" \
  "s_cbranch_vccnz 52b\n\t" \
  "s_branch 53b\n\t" \
  "40:\n\t"

#define INTER 0
#define FFN_ASM \
  "s_and_b32 s88, %[tag], 1023\n\t" \
  "s_bfe_u32 s89, %[tag], 0xa0010\n\t" \
  "s_bfe_u32 s90, %[tag], 0xa0008\n\t" \
  "s_lshr_b32 s91, %[tag], 22\n\t" \
  "s_mov_b32 s87, 0x40000\n\t" \
  "s_cmp_lt_u32 %[w], 8\n\t" \
  "s_cbranch_scc1 30f\n\t" \
  "s_mov_b32 s92, 0x05040100\n\t" \
  "s_mov_b32 s93, 0x07060302\n\t" \
  "s_and_b32 s86, %[w], 3\n\t" \
  "s_lshl_b32 s85, s86, 5\n\t" \
  "s_add_u32 s85, s85, 46720\n\t" \
  "s_lshl_b32 s86, s86, 1\n\t" \
  "s_mul_i32 s84, s86, 1568\n\t" \
  "s_add_u32 s84, s84, 48768\n\t" \
  "s_add_u32 s82, s70, 9435648\n\t" \
  "s_addc_u32 s83, s71, 0\n\t" \
  "11:\n\t" \
  "s_memrealtime s[96:97]\n\t" \
  "s_waitcnt lgkmcnt(0)\n\t" \
  "s_sub_u32 s96, s96, %[tg]\n\t" \
  "s_cmp_lt_i32 s96, 0\n\t" \
  "s_cbranch_scc1 11b\n\t" \
  "global_load_dwordx3 v[110:112], v91, %[wsb] sc1\n\t" \
  "global_load_dwordx3 v[114:116], v92, %[wsb] sc1\n\t" \
  "1:\n\t" \
  "s_waitcnt vmcnt(0)\n\t" \
  TSQ(22) \
  "v_perm_b32 v118, v110, v93, s92\n\t" \
  "v_perm_b32 v119, v110, v93, s93\n\t" \
  "v_perm_b32 v120, v111, v94, s92\n\t" \
  "v_perm_b32 v121, v111, v94, s93\n\t" \
  "v_perm_b32 v122, v114, v95, s92\n\t" \
  "v_perm_b32 v123, v114, v95, s93\n\t" \
  "v_perm_b32 v124, v115, v96, s92\n\t" \
  "v_perm_b32 v125, v115, v96, s93\n\t" \
  "v_max3_u32 v130, v118, v119, v120\n\t" \
  "v_max3_u32 v131, v121, v122, v123\n\t" \
  "v_max3_u32 v126, v124, v125, v130\n\t" \
  "v_xor_b32 v132, %[tag], v112\n\t" \
  "v_max_u32 v126, v126, v131\n\t" \
  "v_bitop3_b32 v132, v132, v116, %[tag] bitop3:0xf6\n\t" \
  "v_med3_u32 v127, v118, v119, v120\n\t" \
  "v_max_u32_dpp v129, v126, v126 quad_perm:[1,0,3,2] row_mask:0xf bank_mask:0xf bound_ctrl:1\n\t" \
  "v_cmp_ne_u32 vcc, 0, v132\n\t" \
  "v_min3_u32 v128, v118, v119, v120\n\t" \
  "v_max_u32_dpp v129, v129, v129 quad_perm:[2,3,0,1] row_mask:0xf bank_mask:0xf bound_ctrl:1\n\t" \
  "v_med3_u32 v128, v127, v128, v121\n\t" \
  "v_med3_u32 v127, v130, v127, v121\n\t" \
  "v_max_u32_dpp v129, v129, v129 row_half_mirror row_mask:0xf bank_mask:0xf bound_ctrl:1\n\t" \
  "v_max_u32 v130, v130, v121\n\t" \
  "v_med3_u32 v128, v127, v128, v122\n\t" \
  "v_max_u32_dpp v129, v129, v129 row_mirror row_mask:0xf bank_mask:0xf bound_ctrl:1\n\t" \
  "v_lshrrev_b32 v129, 1, v129\n\t" \
  "s_cbranch_vccnz 20f\n\t" \
  "ds_write_b32 v97, v129\n\t" \
  "v_med3_u32 v127, v130, v127, v122\n\t" \
  "v_max_u32 v130, v130, v122\n\t" \
  "v_med3_u32 v128, v127, v128, v123\n\t" \
  "v_med3_u32 v127, v130, v127, v123\n\t" \
  "v_max_u32 v130, v130, v123\n\t" \
  "v_med3_u32 v128, v127, v128, v124\n\t" \
  "v_med3_u32 v127, v130, v127, v124\n\t" \
  "v_max_u32 v130, v130, v124\n\t" \
  "v_med3_u32 v128, v127, v128, v125\n\t" \
  "v_med3_u32 v127, v130, v127, v125\n\t" \
  "s_waitcnt lgkmcnt(0)\n\t" \
  "s_barrier\n\t" \
  "ds_read_b32 v133, v98 offset:0\n\t" \
  "ds_read_b128 v[134:137], v99 offset:0\n\t" \
  "v_lshrrev_b32 v130, 1, v126\n\t" \
  "v_lshrrev_b32 v131, 1, v127\n\t" \
  "s_waitcnt lgkmcnt(0)\n\t" \
  "v_sub_u32 v134, v133, v134\n\t" \
  "v_sub_u32 v135, v133, v135\n\t" \
  "v_sub_u32 v136, v133, v136\n\t" \
  "v_sub_u32 v137, v133, v137\n\t" \
  "v_lshrrev_b32 v138, 31, v134\n\t" \
  "v_alignbit_b32 v138, v138, v135, 31\n\t" \
  "v_alignbit_b32 v138, v138, v136, 31\n\t" \
  "v_alignbit_b32 v138, v138, v137, 31\n\t" \
  "v_bcnt_u32_b32 v138, v138, 0\n\t" \
  "s_nop 1\n\t" \
  "v_add_u32_dpp v138, v138, v138 quad_perm:[1,0,3,2] row_mask:0xf bank_mask:0xf bound_ctrl:1\n\t" \
  "s_nop 1\n\t" \
  "v_add_u32_dpp v138, v138, v138 quad_perm:[2,3,0,1] row_mask:0xf bank_mask:0xf bound_ctrl:1\n\t" \
  "v_cmp_eq_u32 vcc, 7, v138\n\t" \
  "s_ff1_i32_b64 s96, vcc\n\t" \
  "v_readlane_b32 s96, v133, s96\n\t" \
  "s_lshl_b32 s96, s96, 1\n\t" \
  "v_cmp_le_u32 s[94:95], s96, v126\n\t" \
  "v_cmp_le_u32 s[98:99], s96, v127\n\t" \
  "v_cmp_le_u32 vcc, s96, v128\n\t" \
  "v_mbcnt_lo_u32_b32 v134, s94, 0\n\t" \
  "v_mbcnt_lo_u32_b32 v135, s98, 0\n\t" \
  "v_mbcnt_hi_u32_b32 v134, s95, v134\n\t" \
  "v_mbcnt_hi_u32_b32 v135, s99, v135\n\t" \
  "s_bcnt1_i32_b64 s96, s[94:95]\n\t" \
  "s_bcnt1_i32_b64 s97, s[98:99]\n\t" \
  "v_lshl_add_u32 v134, v134, 2, s85\n\t" \
  "v_mad_i32_i24 v135, v135, -4, s85\n\t" \
  "s_add_u32 s96, s96, s97\n\t" \
  "s_cmp_gt_u32 s96, " TOPCAP "\n\t" \
  "s_cbranch_scc1 22f\n\t" \
  "s_cbranch_vccnz 22f\n\t" \
  "s_mov_b64 exec, s[94:95]\n\t" \
  "ds_write_b32 v134, v130\n\t" \
  "s_mov_b64 exec, s[98:99]\n\t" \
  "ds_write_b32 v135, v131 offset:28\n\t" \
  "s_mov_b64 exec, -1\n\t" \
  "2:\n\t" \
  "s_waitcnt lgkmcnt(0)\n\t" \
  "s_barrier\n\t" \
  "ds_read_b32 v133, v100 offset:0\n\t" \
  "ds_read_b128 v[140:143], v101 offset:0\n\t" \
  "ds_read_b128 v[144:147], v101 offset:16\n\t" \
  "ds_read_b128 v[148:151], v101 offset:32\n\t" \
  "ds_read_b128 v[152:155], v101 offset:48\n\t" \
  "s_mov_b32 m0, s84\n\t" \
  "s_waitcnt lgkmcnt(3)\n\t" \
  "v_sub_u32 v140, v133, v140\n\t" \
  "v_sub_u32 v141, v133, v141\n\t" \
  "v_lshrrev_b32 v138, 31, v140\n\t" \
  "v_sub_u32 v142, v133, v142\n\t" \
  "v_lshrrev_b32 v139, 31, v141\n\t" \
  "v_sub_u32 v143, v133, v143\n\t" \
  "v_alignbit_b32 v138, v138, v142, 31\n\t" \
  "s_waitcnt lgkmcnt(2)\n\t" \
  "v_sub_u32 v144, v133, v144\n\t" \
  "v_alignbit_b32 v139, v139, v143, 31\n\t" \
  "v_sub_u32 v145, v133, v145\n\t" \
  "v_alignbit_b32 v138, v138, v144, 31\n\t" \
  "v_sub_u32 v146, v133, v146\n\t" \
  "v_alignbit_b32 v139, v139, v145, 31\n\t" \
  "v_sub_u32 v147, v133, v147\n\t" \
  "v_alignbit_b32 v138, v138, v146, 31\n\t" \
  "s_waitcnt lgkmcnt(1)\n\t" \
  "v_sub_u32 v148, v133, v148\n\t" \
  "v_alignbit_b32 v139, v139, v147, 31\n\t" \
  "v_sub_u32 v149, v133, v149\n\t" \
  "v_alignbit_b32 v138, v138, v148, 31\n\t" \
  "v_sub_u32 v150, v133, v150\n\t" \
  "v_alignbit_b32 v139, v139, v149, 31\n\t" \
  "v_sub_u32 v151, v133, v151\n\t" \
  "v_alignbit_b32 v138, v138, v150, 31\n\t" \
  "s_waitcnt lgkmcnt(0)\n\t" \
  "v_sub_u32 v152, v133, v152\n\t" \
  "v_alignbit_b32 v139, v139, v151, 31\n\t" \
  "v_sub_u32 v153, v133, v153\n\t" \
  "v_alignbit_b32 v138, v138, v152, 31\n\t" \
  "v_sub_u32 v154, v133, v154\n\t" \
  "v_alignbit_b32 v139, v139, v153, 31\n\t" \
  "v_sub_u32 v155, v133, v155\n\t" \
  "v_alignbit_b32 v138, v138, v154, 31\n\t" \
  "v_bcnt_u32_b32 v138, v138, 0\n\t" \
  "v_alignbit_b32 v139, v139, v155, 31\n\t" \
  "v_bfe_u32 v130, v133, 4, 11\n\t" \
  "v_bcnt_u32_b32 v138, v139, v138\n\t" \
  "v_mul_u32_u24 v130, 1536, v130\n\t" \
  "v_mov_b32 v139, v138\n\t" \
  "s_add_u32 s97, s86, 1\n\t" \
  "s_nop 0\n\t" \
  "v_permlane32_swap_b32 v138, v139\n\t" \
  "v_add_u32 v138, v138, v139\n\t" \
  "v_cmp_eq_u32 vcc, s86, v138\n\t" \
  "v_cmp_eq_u32 s[94:95], s97, v138\n\t" \
  "s_ff1_i32_b32 s96, vcc_lo\n\t" \
  "s_ff1_i32_b32 s97, s94\n\t" \
  "v_readlane_b32 s96, v130, s96\n\t" \
  "v_readlane_b32 s97, v130, s97\n\t" \
  "s_sub_u32 s94, s82, s96\n\t" \
  "s_subb_u32 s95, s83, 0\n\t" \
  "global_load_lds_dwordx4 v102, s[94:95] sc1\n\t" \
  "s_mov_b32 exec_hi, 0\n\t" \
  "global_load_lds_dwordx4 v102, s[94:95] offset:1024 sc1\n\t" \
  "s_mov_b32 exec_hi, -1\n\t" \
  "s_add_u32 m0, s84, 1568\n\t" \
  "s_sub_u32 s94, s82, s97\n\t" \
  "s_subb_u32 s95, s83, 0\n\t" \
  "global_load_lds_dwordx4 v102, s[94:95] sc1\n\t" \
  "s_mov_b32 exec_hi, 0\n\t" \
  "global_load_lds_dwordx4 v102, s[94:95] offset:1024 sc1\n\t" \
  "s_mov_b32 exec_hi, -1\n\t" \
  TSQ(23) \
  "v_min_u32 v138, 8, v138\n\t" \
  "v_lshlrev_b32 v138, 2, v138\n\t" \
  "ds_write_b32 v138, v133 offset:46976\n\t" \
  "v_mov_b64 v[140:141], 0\n\t" \
  "v_mov_b64 v[142:143], 0\n\t" \
  "v_mov_b64 v[144:145], 0\n\t" \
  "v_mov_b64 v[146:147], 0\n\t" \
  "v_mov_b64 v[148:149], 0\n\t" \
  "v_mov_b64 v[150:151], 0\n\t" \
  "v_mov_b64 v[152:153], 0\n\t" \
  "v_mov_b64 v[154:155], 0\n\t" \
  "v_mov_b32 v91, 0xfffffc00\n\t" \
  "s_waitcnt vmcnt(0)\n\t" \
  "s_waitcnt lgkmcnt(0)\n\t" \
  "s_barrier\n\t" \
  TSQ(24) \
  "s_cmp_lg_u32 %[w], 11\n\t" \
  "s_cbranch_scc1 3f\n\t" \
  "ds_read_b32 v110, v106 offset:0\n\t" \
  "s_waitcnt lgkmcnt(0)\n\t" \
  "v_bfe_u32 v130, v110, 4, 11\n\t" \
  "v_xor_b32 v130, 0x7ff, v130\n\t" \
  "s_movk_i32 s96, 1152\n\t" \
  "v_mad_u32_u24 v111, v130, s96, v107\n\t" \
  "global_load_short_d16_hi v113, v111, %[x]\n\t" \
  "v_lshrrev_b32 v131, 6, v130\n\t" \
  "v_bfe_u32 v138, v130, 1, 3\n\t" \
  "v_lshl_or_b32 v131, v131, 3, v138\n\t" \
  "v_lshrrev_b32 v130, 4, v130\n\t" \
  "v_xor_b32 v130, v130, v108\n\t" \
  "v_and_b32 v130, 3, v130\n\t" \
  "v_and_b32 v138, -4, v108\n\t" \
  "v_lshl_add_u32 v130, v130, 4, v138\n\t" \
  "s_movk_i32 s96, 12288\n\t" \
  "v_mad_u32_u24 v112, v131, s96, v130\n\t" \
  "global_load_dwordx4 v[114:117], v112, %[wsb] sc1\n\t" \
  "global_store_short v111, v140, %[out] sc1\n\t" \
  "v_lshlrev_b32 v130, 1, v110\n\t" \
  "v_ashrrev_i32 v131, 31, v130\n\t" \
  "s_brev_b32 s96, 1\n\t" \
  "v_bitop3_b32 v130, v130, v131, s96 bitop3:0x4b\n\t" \
  "v_and_b32 v130, 0xffff0000, v130\n\t" \
  "v_mul_f32 v130, 0xbfb8aa3b, v130\n\t" \
  "v_exp_f32 v130, v130\n\t" \
  "s_nop 0\n\t" \
  "v_add_f32 v130, 1.0, v130\n\t" \
  "v_rcp_f32 v130, v130\n\t" \
  "s_nop 0\n\t" \
  "v_cvt_pk_bf16_f32 v119, 0, v130\n\t" \
  "3:\n\t" \
  "s_barrier\n\t" \
  "s_cmp_lg_u32 %[w], 11\n\t" \
  "s_cbranch_scc1 3f\n\t" \
  "ds_read_u16_d16_hi v118, v109\n\t" \
  "3:\n\t" \
  "s_barrier\n\t" \
  TSQ(25) \
  "ds_read_b128 v[124:127], v103 offset:0\n\t" \
  "ds_read_b128 v[128:131], v103 offset:16\n\t" \
  "s_waitcnt lgkmcnt(1)\n\t" \
  "v_mfma_f32_16x16x32_bf16 v[156:159], v[124:127], v[80:83], 0\n\t" \
  "v_smfmac_f32_16x16x64_bf16 v[140:143], v[124:127], v[16:23], v90\n\t" \
  "v_smfmac_f32_16x16x64_bf16 v[144:147], v[124:127], v[24:31], v90\n\t" \
  "v_smfmac_f32_16x16x64_bf16 v[148:151], v[124:127], v[32:39], v90\n\t" \
  "v_smfmac_f32_16x16x64_bf16 v[152:155], v[124:127], v[40:47], v90\n\t" \
  "s_waitcnt lgkmcnt(0)\n\t" \
  "v_mfma_f32_16x16x32_bf16 v[156:159], v[128:131], v[84:87], v[156:159]\n\t" \
  "v_smfmac_f32_16x16x64_bf16 v[140:143], v[128:131], v[48:55], v90\n\t" \
  "v_smfmac_f32_16x16x64_bf16 v[144:147], v[128:131], v[56:63], v90\n\t" \
  "v_smfmac_f32_16x16x64_bf16 v[148:151], v[128:131], v[64:71], v90\n\t" \
  "v_smfmac_f32_16x16x64_bf16 v[152:155], v[128:131], v[72:79], v90\n\t" \
  "v_add_u32 v92, 4096, v104\n\t" \
  "s_nop 2\n\t" \
  "v_bitop3_b32 v32, v156, v91, s88 bitop3:0xea\n\t" \
  "v_bitop3_b32 v33, v157, v91, s89 bitop3:0xea\n\t" \
  "v_bitop3_b32 v34, v158, v91, s90 bitop3:0xea\n\t" \
  "v_bitop3_b32 v35, v159, v91, s91 bitop3:0xea\n\t" \
  "s_mov_b32 exec_hi, 0\n\t" \
  "global_store_dwordx4 v105, v[32:35], %[wsb] sc1\n\t" \
  "s_mov_b32 exec_hi, -1\n\t" \
  "v_bitop3_b32 v16, v140, v91, s88 bitop3:0xea\n\t" \
  "v_bitop3_b32 v17, v144, v91, s89 bitop3:0xea\n\t" \
  "v_bitop3_b32 v18, v148, v91, s90 bitop3:0xea\n\t" \
  "v_bitop3_b32 v19, v152, v91, s91 bitop3:0xea\n\t" \
  "global_store_dwordx4 v104, v[16:19], %[wsb] offset:-2048 sc1\n\t" \
  "v_bitop3_b32 v20, v141, v91, s88 bitop3:0xea\n\t" \
  "v_bitop3_b32 v21, v145, v91, s89 bitop3:0xea\n\t" \
  "v_bitop3_b32 v22, v149, v91, s90 bitop3:0xea\n\t" \
  "v_bitop3_b32 v23, v153, v91, s91 bitop3:0xea\n\t" \
  "global_store_dwordx4 v104, v[20:23], %[wsb] offset:0 sc1\n\t" \
  "v_bitop3_b32 v24, v142, v91, s88 bitop3:0xea\n\t" \
  "v_bitop3_b32 v25, v146, v91, s89 bitop3:0xea\n\t" \
  "v_bitop3_b32 v26, v150, v91, s90 bitop3:0xea\n\t" \
  "v_bitop3_b32 v27, v154, v91, s91 bitop3:0xea\n\t" \
  "global_store_dwordx4 v104, v[24:27], %[wsb] offset:2048 sc1\n\t" \
  "v_bitop3_b32 v28, v143, v91, s88 bitop3:0xea\n\t" \
  "v_bitop3_b32 v29, v147, v91, s89 bitop3:0xea\n\t" \
  "v_bitop3_b32 v30, v151, v91, s90 bitop3:0xea\n\t" \
  "v_bitop3_b32 v31, v155, v91, s91 bitop3:0xea\n\t" \
  "global_store_dwordx4 v92, v[28:31], %[wsb] offset:0 sc1\n\t" \
  TSQ(26) \
  "s_cmp_lg_u32 %[w], 11\n\t" \
  "s_cbranch_scc1 50f\n\t" \
  "s_waitcnt vmcnt(6)\n\t" \
  "4:\n\t" \
  "v_cmp_ne_u32 vcc, %[tag], v117\n\t" \
  "s_cbranch_vccnz 24f\n\t" \
  "5:\n\t" \
  "v_cmp_gt_u32 vcc, 384, v107\n\t" \
  "v_mov_b32 v130, 768\n\t" \
  "v_cmp_gt_u32 s[94:95], v130, v107\n\t" \
  "v_and_b32 v131, 16, v110\n\t" \
  "v_cndmask_b32 v115, v115, v114, vcc\n\t" \
  "v_cndmask_b32 v114, v116, v115, s[94:95]\n\t" \
  "v_lshlrev_b32 v114, v131, v114\n\t" \
  "v_and_b32 v114, 0xffff0000, v114\n\t" \
  "v_and_b32 v113, 0xffff0000, v113\n\t" \
  "v_and_b32 v118, 0xffff0000, v118\n\t" \
  "v_add_f32 v120, v113, v114\n\t" \
  "v_cvt_pk_bf16_f32 v120, 0, v120\n\t" \
  "s_barrier\n\t" \
  "ds_read2st64_b32 v[124:125], v89 offset0:0 offset1:1\n\t" \
  "ds_read2st64_b32 v[126:127], v89 offset0:2 offset1:3\n\t" \
  "ds_read2st64_b32 v[128:129], v89 offset0:4 offset1:5\n\t" \
  "s_waitcnt lgkmcnt(1)\n\t" \
  "v_pk_add_f32 v[124:125], v[124:125], v[126:127]\n\t" \
  "s_waitcnt lgkmcnt(0)\n\t" \
  "v_pk_add_f32 v[124:125], v[124:125], v[128:129]\n\t" \
  "v_add_f32 v124, v124, v125\n\t" \
  "v_cvt_pk_bf16_f32 v124, 0, v124\n\t" \
  "v_mul_f32 v124, v124, v119\n\t" \
  "v_cvt_pk_bf16_f32 v124, 0, v124\n\t" \
  "v_add_f32 v124, v124, v118\n\t" \
  "v_cvt_pk_bf16_f32 v124, 0, v124\n\t" \
  "v_add_f32 v124, v124, v120\n\t" \
  "v_cvt_pk_bf16_f32 v124, v124, v124\n\t" \
  "global_store_short v111, v124, %[out] sc1\n\t" \
  "s_branch 50f\n\t" \
  "20:\n\t" \
  "s_sub_u32 s87, s87, 1\n\t" \
  "s_cbranch_scc1 21f\n\t" \
  "s_sleep 1\n\t" \
  "v_add_u32 v130, 81920, v91\n\t" \
  "v_add_u32 v131, 81920, v92\n\t" \
  "s_mov_b64 exec, vcc\n\t" \
  "global_load_dwordx3 v[110:112], v130, %[wsb] sc1\n\t" \
  "global_load_dwordx3 v[114:116], v131, %[wsb] sc1\n\t" \
  "s_mov_b64 exec, -1\n\t" \
  "s_branch 1b\n\t" \
  "21:\n\t" \
  "v_mov_b32 v112, %[tag]\n\t" \
  "v_mov_b32 v116, %[tag]\n\t" \
  "s_branch 1b\n\t" \
  "22:\n\t" \
  "v_mov_b32 v134, s85\n\t" \
  "s_mov_b32 s97, 8\n\t" \
  "23:\n\t" \
  "v_max3_u32 v130, v118, v119, v120\n\t" \
  "v_max3_u32 v130, v130, v121, v122\n\t" \
  "v_max3_u32 v130, v130, v123, v124\n\t" \
  "v_max_u32 v130, v130, v125\n\t" \
  "s_nop 1\n\t" \
  "v_max_u32_dpp v130, v130, v130 quad_perm:[1,0,3,2] row_mask:0xf bank_mask:0xf bound_ctrl:1\n\t" \
  "s_nop 1\n\t" \
  "v_max_u32_dpp v130, v130, v130 quad_perm:[2,3,0,1] row_mask:0xf bank_mask:0xf bound_ctrl:1\n\t" \
  "s_nop 1\n\t" \
  "v_max_u32_dpp v130, v130, v130 row_half_mirror row_mask:0xf bank_mask:0xf bound_ctrl:1\n\t" \
  "s_nop 1\n\t" \
  "v_max_u32_dpp v130, v130, v130 row_mirror row_mask:0xf bank_mask:0xf bound_ctrl:1\n\t" \
  "v_mov_b32 v131, v130\n\t" \
  "s_nop 1\n\t" \
  "v_permlane16_swap_b32 v130, v131\n\t" \
  "v_max_u32 v130, v130, v131\n\t" \
  "v_mov_b32 v131, v130\n\t" \
  "s_nop 1\n\t" \
  "v_permlane32_swap_b32 v130, v131\n\t" \
  "v_max_u32 v130, v130, v131\n\t" \
  "v_cmp_ne_u32 vcc, v130, v118\n\t" \
  "s_nop 1\n\t" \
  "v_cndmask_b32 v118, 0, v118, vcc\n\t" \
  "v_cmp_ne_u32 vcc, v130, v119\n\t" \
  "s_nop 1\n\t" \
  "v_cndmask_b32 v119, 0, v119, vcc\n\t" \
  "v_cmp_ne_u32 vcc, v130, v120\n\t" \
  "s_nop 1\n\t" \
  "v_cndmask_b32 v120, 0, v120, vcc\n\t" \
  "v_cmp_ne_u32 vcc, v130, v121\n\t" \
  "s_nop 1\n\t" \
  "v_cndmask_b32 v121, 0, v121, vcc\n\t" \
  "v_cmp_ne_u32 vcc, v130, v122\n\t" \
  "s_nop 1\n\t" \
  "v_cndmask_b32 v122, 0, v122, vcc\n\t" \
  "v_cmp_ne_u32 vcc, v130, v123\n\t" \
  "s_nop 1\n\t" \
  "v_cndmask_b32 v123, 0, v123, vcc\n\t" \
  "v_cmp_ne_u32 vcc, v130, v124\n\t" \
  "s_nop 1\n\t" \
  "v_cndmask_b32 v124, 0, v124, vcc\n\t" \
  "v_cmp_ne_u32 vcc, v130, v125\n\t" \
  "s_nop 1\n\t" \
  "v_cndmask_b32 v125, 0, v125, vcc\n\t" \
  "v_lshrrev_b32 v130, 1, v130\n\t" \
  "ds_write_b32 v134, v130\n\t" \
  "v_add_u32 v134, 4, v134\n\t" \
  "s_sub_u32 s97, s97, 1\n\t" \
  "s_cmp_lg_u32 s97, 0\n\t" \
  "s_cbranch_scc1 23b\n\t" \
  "s_branch 2b\n\t" \
  "24:\n\t" \
  "s_sub_u32 s87, s87, 1\n\t" \
  "s_cbranch_scc1 5b\n\t" \
  "global_load_dwordx4 v[114:117], v112, %[wsb] sc1\n\t" \
  "s_waitcnt vmcnt(0)\n\t" \
  "s_branch 4b\n\t" \
  "30:\n\t" \
  "s_mul_i32 s96, %[w], 4608\n\t" \
  "s_add_u32 m0, s96, 8704\n\t" \
  "s_barrier\n\t" \
  "s_barrier\n\t" \
  "v_mov_b64 v[124:125], 0\n\t" \
  "v_mov_b64 v[126:127], 0\n\t" \
  "v_mov_b64 v[128:129], 0\n\t" \
  "v_mov_b64 v[130:131], 0\n\t" \
  "v_mov_b64 v[132:133], 0\n\t" \
  "v_mov_b64 v[134:135], 0\n\t" \
  "v_mov_b64 v[136:137], 0\n\t" \
  "v_mov_b64 v[138:139], 0\n\t" \
  "s_barrier\n\t" \
  TSQ(24) \
  "ds_read_b128 v[118:121], v91 offset:1024\n\t" \
  "ds_read_b128 v[110:113], v91 offset:0\n\t" \
  "ds_read_b128 v[114:117], v91 offset:512\n\t" \
  "31:\n\t" \
  "s_waitcnt lgkmcnt(2)\n\t" \
  "v_xor_b32 v121, %[tag], v121\n\t" \
  "s_waitcnt lgkmcnt(1)\n\t" \
  "v_bitop3_b32 v122, v121, v113, %[tag] bitop3:0xf6\n\t" \
  "v_mov_b32 v113, v118\n\t" \
  "v_smfmac_f32_16x16x32_bf16 v[124:127], v[120:121], v[80:83], v90\n\t" \
  "s_waitcnt lgkmcnt(0)\n\t" \
  "v_bitop3_b32 v122, v122, v117, %[tag] bitop3:0xf6\n\t" \
  "v_mov_b32 v117, v119\n\t" \
  "v_smfmac_f32_16x16x32_bf16 v[128:131], v[120:121], v[82:85], v90\n\t" \
  "v_cmp_ne_u32 vcc, 0, v122\n\t" \
  "v_smfmac_f32_16x16x32_bf16 v[132:135], v[120:121], v[84:87], v90\n\t" \
  "v_smfmac_f32_16x16x32_bf16 v[136:139], v[120:121], v[86:89], v90\n\t" \
  "s_cbranch_vccnz 40f\n\t" \
  "32:\n\t" \
  "s_nop 3\n\t" \
  "v_smfmac_f32_16x16x64_bf16 v[124:127], v[110:113], v[16:23], v90\n\t" \
  "v_smfmac_f32_16x16x64_bf16 v[128:131], v[110:113], v[24:31], v90\n\t" \
  "v_smfmac_f32_16x16x64_bf16 v[132:135], v[110:113], v[32:39], v90\n\t" \
  "v_smfmac_f32_16x16x64_bf16 v[136:139], v[110:113], v[40:47], v90\n\t" \
  "v_smfmac_f32_16x16x64_bf16 v[124:127], v[114:117], v[48:55], v90\n\t" \
  "v_smfmac_f32_16x16x64_bf16 v[128:131], v[114:117], v[56:63], v90\n\t" \
  "v_smfmac_f32_16x16x64_bf16 v[132:135], v[114:117], v[64:71], v90\n\t" \
  "v_smfmac_f32_16x16x64_bf16 v[136:139], v[114:117], v[72:79], v90\n\t" \
  "s_nop 4\n\t" \
  "ds_write_addtid_b32 v124 offset:0\n\t" \
  "ds_write_addtid_b32 v125 offset:288\n\t" \
  "ds_write_addtid_b32 v126 offset:576\n\t" \
  "ds_write_addtid_b32 v127 offset:864\n\t" \
  "ds_write_addtid_b32 v128 offset:1152\n\t" \
  "ds_write_addtid_b32 v129 offset:1440\n\t" \
  "ds_write_addtid_b32 v130 offset:1728\n\t" \
  "ds_write_addtid_b32 v131 offset:2016\n\t" \
  "ds_write_addtid_b32 v132 offset:2304\n\t" \
  "ds_write_addtid_b32 v133 offset:2592\n\t" \
  "ds_write_addtid_b32 v134 offset:2880\n\t" \
  "ds_write_addtid_b32 v135 offset:3168\n\t" \
  "ds_write_addtid_b32 v136 offset:3456\n\t" \
  "ds_write_addtid_b32 v137 offset:3744\n\t" \
  "ds_write_addtid_b32 v138 offset:4032\n\t" \
  "ds_write_addtid_b32 v139 offset:4320\n\t" \
  "s_waitcnt lgkmcnt(0)\n\t" \
  "s_barrier\n\t" \
  "ds_read_b32 v110, v92 offset:0\n\t" \
  "ds_read_b32 v111, v92 offset:32\n\t" \
  "ds_read_b32 v112, v92 offset:4608\n\t" \
  "ds_read_b32 v113, v92 offset:4640\n\t" \
  "ds_read_b32 v114, v92 offset:9216\n\t" \
  "ds_read_b32 v115, v92 offset:9248\n\t" \
  "ds_read_b32 v116, v92 offset:13824\n\t" \
  "ds_read_b32 v117, v92 offset:13856\n\t" \
  "ds_read_b32 v118, v92 offset:18432\n\t" \
  "ds_read_b32 v119, v92 offset:18464\n\t" \
  "ds_read_b32 v120, v92 offset:23040\n\t" \
  "ds_read_b32 v121, v92 offset:23072\n\t" \
  "ds_read_b32 v122, v92 offset:27648\n\t" \
  "ds_read_b32 v123, v92 offset:27680\n\t" \
  "ds_read_b32 v124, v92 offset:32256\n\t" \
  "ds_read_b32 v125, v92 offset:32288\n\t" \
  "s_waitcnt lgkmcnt(12)\n\t" \
  "v_pk_add_f32 v[110:111], v[110:111], v[112:113]\n\t" \
  "s_waitcnt lgkmcnt(8)\n\t" \
  "v_pk_add_f32 v[114:115], v[114:115], v[116:117]\n\t" \
  "v_pk_add_f32 v[110:111], v[110:111], v[114:115]\n\t" \
  "s_waitcnt lgkmcnt(4)\n\t" \
  "v_pk_add_f32 v[118:119], v[118:119], v[120:121]\n\t" \
  "s_waitcnt lgkmcnt(0)\n\t" \
  "v_pk_add_f32 v[122:123], v[122:123], v[124:125]\n\t" \
  "v_pk_add_f32 v[118:119], v[118:119], v[122:123]\n\t" \
  "v_pk_add_f32 v[110:111], v[110:111], v[118:119]\n\t" \
  "v_cvt_pk_bf16_f32 v110, 0, v110\n\t" \
  "v_mul_f32 v112, 0xbfb8aa3b, v110\n\t" \
  "v_exp_f32 v112, v112\n\t" \
  "v_cvt_pk_bf16_f32 v111, 0, v111\n\t" \
  "v_add_f32 v112, 1.0, v112\n\t" \
  "v_rcp_f32 v112, v112\n\t" \
  "s_nop 0\n\t" \
  "v_mul_f32 v110, v110, v112\n\t" \
  "v_cvt_pk_bf16_f32 v110, 0, v110\n\t" \
  "v_mul_f32 v110, v110, v111\n\t" \
  "v_cvt_pk_bf16_f32 v110, v110, v110\n\t" \
  "ds_write_b16 v93, v110\n\t" \
  "s_waitcnt lgkmcnt(0)\n\t" \
  "s_barrier\n\t" \
  TSQ(25) \
  "s_cmp_gt_u32 %[w], 5\n\t" \
  "s_cbranch_scc1 50f\n\t" \
  "s_memrealtime s[94:95]\n\t" \
  "s_lshl_b32 s96, %[w], 8\n\t" \
  "s_add_u32 m0, s96, 47232\n\t" \
  "v_add_u32 v95, 73728, v94\n\t" \
  "v_add_u32 v96, 147456, v94\n\t" \
  "s_waitcnt lgkmcnt(0)\n\t" \
  "s_add_u32 s94, s94, " STR(FW) "\n\t" \
  "11:\n\t" \
  "s_memrealtime s[96:97]\n\t" \
  "s_waitcnt lgkmcnt(0)\n\t" \
  "s_sub_u32 s96, s96, s94\n\t" \
  "s_cmp_lt_i32 s96, 0\n\t" \
  "s_cbranch_scc1 11b\n\t" \
  "33:\n\t" \
  "global_load_dwordx4 v[110:113], v94, %[wsb] sc1\n\t" \
  "global_load_dwordx4 v[114:117], v95, %[wsb] sc1\n\t" \
  "global_load_dwordx4 v[118:121], v96, %[wsb] sc1\n\t" \
  "s_waitcnt vmcnt(2)\n\t" \
  "v_xor_b32 v110, s88, v110\n\t" \
  "v_xor_b32 v111, s89, v111\n\t" \
  "v_xor_b32 v112, s90, v112\n\t" \
  "v_xor_b32 v113, s91, v113\n\t" \
  "v_or3_b32 v122, v110, v111, v112\n\t" \
  "v_or_b32 v122, v122, v113\n\t" \
  "s_waitcnt vmcnt(1)\n\t" \
  "v_xor_b32 v114, s88, v114\n\t" \
  "v_xor_b32 v115, s89, v115\n\t" \
  "v_xor_b32 v116, s90, v116\n\t" \
  "v_xor_b32 v117, s91, v117\n\t" \
  "v_or3_b32 v122, v122, v114, v115\n\t" \
  "v_or3_b32 v122, v122, v116, v117\n\t" \
  "v_pk_add_f32 v[110:111], v[110:111], v[114:115]\n\t" \
  "v_pk_add_f32 v[112:113], v[112:113], v[116:117]\n\t" \
  "s_waitcnt vmcnt(0)\n\t" \
  TSQ(26) \
  "v_xor_b32 v118, s88, v118\n\t" \
  "v_xor_b32 v119, s89, v119\n\t" \
  "v_xor_b32 v120, s90, v120\n\t" \
  "v_xor_b32 v121, s91, v121\n\t" \
  "v_or3_b32 v122, v122, v118, v119\n\t" \
  "v_or3_b32 v122, v122, v120, v121\n\t" \
  "v_pk_add_f32 v[110:111], v[110:111], v[118:119]\n\t" \
  "v_pk_add_f32 v[112:113], v[112:113], v[120:121]\n\t" \
  "v_and_b32 v122, 1023, v122\n\t" \
  "v_permlane32_swap_b32 v110, v111\n\t" \
  "v_cmp_ne_u32 vcc, 0, v122\n\t" \
  "v_permlane32_swap_b32 v112, v113\n\t" \
  "v_add_f32 v110, v110, v111\n\t" \
  "v_add_f32 v112, v112, v113\n\t" \
  "s_cbranch_vccnz 42f\n\t" \
  "34:\n\t" \
  "s_nop 0\n\t" \
  "v_permlane16_swap_b32 v110, v112\n\t" \
  "v_add_f32 v110, v110, v112\n\t" \
  "ds_write_addtid_b32 v110\n\t" \
  "s_waitcnt lgkmcnt(0)\n\t" \
  "s_barrier\n\t" \
  "s_branch 50f\n\t" \
  "40:\n\t" \
  "s_sub_u32 s87, s87, 1\n\t" \
  "s_cbranch_scc1 32b\n\t" \
  "s_sleep 2\n\t" \
  "ds_read_b32 v122, v97\n\t" \
  "s_waitcnt lgkmcnt(0)\n\t" \
  "v_bfe_u32 v122, v122, 4, 11\n\t" \
  "s_movk_i32 s96, -1536\n\t" \
  "v_mad_i32_i24 v122, v122, s96, v98\n\t" \
  "global_load_dwordx4 v[110:113], v122, %[wsb] offset:0 sc1\n\t" \
  "global_load_dwordx4 v[114:117], v122, %[wsb] offset:512 sc1\n\t" \
  "global_load_dwordx4 v[118:121], v122, %[wsb] offset:1024 sc1\n\t" \
  "v_mov_b64 v[124:125], 0\n\t" \
  "v_mov_b64 v[126:127], 0\n\t" \
  "v_mov_b64 v[128:129], 0\n\t" \
  "v_mov_b64 v[130:131], 0\n\t" \
  "v_mov_b64 v[132:133], 0\n\t" \
  "v_mov_b64 v[134:135], 0\n\t" \
  "v_mov_b64 v[136:137], 0\n\t" \
  "v_mov_b64 v[138:139], 0\n\t" \
  "s_waitcnt vmcnt(0)\n\t" \
  "ds_write_b128 v91, v[110:113] offset:0\n\t" \
  "ds_write_b128 v91, v[114:117] offset:512\n\t" \
  "ds_write_b128 v91, v[118:121] offset:1024\n\t" \
  "s_waitcnt lgkmcnt(0)\n\t" \
  "s_branch 31b\n\t" \
  "42:\n\t" \
  "s_sub_u32 s87, s87, 1\n\t" \
  "s_cbranch_scc1 34b\n\t" \
  "s_sleep 1\n\t" \
  "s_branch 33b\n\t" \
  "50:\n\t"

constexpr uint32_t KREP = 160 * 64;
#define KEY_ADR(tid) uint32_t k0 = WS_KEYS + (blockIdx.x & 7) * KREP + (tid) * 16, k1 = k0 + 4096u + max((int)((tid) & ~1) - 128, 0) * 16
__device__ __forceinline__ void key_sweep(char* wsb, uint32_t tag, uint32_t k0, uint32_t k1, uint32_t klo, uint32_t* q) {
  u32x3 r0, r1;
#define KLD(io, o0, o1) asm volatile("global_load_dwordx3 %0, %2, %4 sc1\n\tglobal_load_dwordx3 %1, %3, %4 sc1\n\t" \
      "s_waitcnt vmcnt(0)" : io(r0), io(r1) : "v"(o0), "v"(o1), "s"(wsb) : "memory")
  KLD("=&v", k0, k1);
#pragma unroll 1
  for (int spin = 1; spin < SPIN_CAP; ++spin) {
    uint32_t bad = r0[2] ^ tag;
    XOR_OR(bad, r1[2], tag);
    if (LIKELY(!__any(bad != 0u))) break;
    __builtin_amdgcn_s_sleep(PSLEEP2);
    const uint32_t kr = (uint32_t)min(spin, NCOPY - 1) * (8u * KREP);
    if (bad) KLD("+v", k0 + kr, k1 + kr);
  }
  const uint32_t s[4] = {r0[0], r0[1], r1[0], r1[1]};
#pragma unroll
  for (int k = 0; k < 4; ++k) {
    const uint32_t lo = klo - (uint32_t)((k & 1) * 1024 + (k >> 1) * 32768) * 0x10001u;
    q[2 * k] = perm_lo(lo, s[k]);
    q[2 * k + 1] = perm_hi(lo, s[k]);
  }
}
__device__ __forceinline__ uint32_t key_lows(int tid) {
  const uint32_t l = (uint32_t)(0x7ff - tok0(tid >> 2) - (tid & 3)) << 5;
  return l | ((l - 512u) << 16);
}

#define FFN_REGS "v112", "v113", "v114", "v115", "v116", "v117", "v118", "v119", "v120", "v121", "v122", "v123", "v124", "v125", "v126", "v127", "v128", "v129", "v130", "v131", "v132", "v133", "v134", "v135", "v136", "v137", "v138", "v139", "v140", "v141", "v142", "v143", "v144", "v145", "v146", "v147", "v148", "v149", "v150", "v151", "v152", "v153", "v154", "v155", "v156", "v157", "v158", "v159"
#define LN2_REGS "v44", "v45", "v46", "v47", "v48", "v49", "v50", "v51", "v52", "v53", "v54", "v55", "v56", "v57", "v58", "v59", "v60", "v61", "v62", "v63", "v64", "v65", "v66", "v67", "v68", "v69", "v70", "v71", "v72", "v73", "v74", "v75", "v76", "v77", "v78", "v79", "v80", "v81", "v82", "v83", "v84", "v85", "v86", "v87", "v88", "v89", "v90", "v91", "v92", "v93", "v94", "v95", "v96", "v97", "v98", "v99", "v100", "v101", "v102", "v103", "v107", "v108"
#define RING_REGS "v120", "v121", "v122", "v123", "v124", "v125", "v126", "v127", "v128", "v129", "v130", "v131", "v132", "v133", "v134", "v135", "v136", "v137", "v138", "v139", "v140", "v141", "v142", "v143", "v144", "v145", "v146", "v147", "v148", "v149", "v150", "v151"
#define UDONE() LWAITS(4u * (tidx() & 1), 88, 1, 4)
struct VOut { uint32_t a[9]; };
__device__ __forceinline__ VOut v_prep(int k) {
  const int b = blockIdx.x, lane = tidx() & 63;
  const uint32_t vo = WS_VG + vrun(b) + ((lane & 12) + (lane >> 4)) * 24576 + (((lane ^ (b >> 3)) & 3) << 4);
  const uint32_t xo = G_OP + 8192 + 8 * lane, co = 128 + 4 * min(lane & 3, 2);
  const uint32_t x1 = xo + 512 * k, x2 = xo + 512 * (6 + (k >> 1)) + 4 * (k & 1);
  VOut p = {{co, co + 16 * k, x1, x1 + 8 * OPW, x2, x2 + 8 * OPW, vo, vo + 12288u, vo + 12288u * (k & 1)}};
  PIN9(p.a);
  return p;
}
__device__ __forceinline__ void v_out(const VOut& p, char* wsb, uint32_t tag, int k) {
#define VO_RD(r, a, b_, o) "ds_read_b32 v[" #r "], %" #a " offset:" #o "\n\tds_read_b32 v[" #r "+1], %" #a " offset:%13+" #o "\n\tds_read_b32 v[" #r "+2], %" #b_ " offset:" #o "\n\t"
#define VO_POLL(a, o, more) "1:\n\tds_read_b32 v136, %" #a " offset:" #o "\n\t" more "s_waitcnt lgkmcnt(0)\n\tv_cmp_eq_u32 vcc, 0, v136\n\ts_cbranch_vccz 2f\n\ts_sleep " ESL "\n\ts_branch 1b\n2:\n\t"
#define VO_TILE(r, j, sb) VO_POLL(1, 48*j, "") VO_RD(r, 2, 3, 1536*j) VO_RD(r+4, 2, 3, 4+1536*j) "s_waitcnt lgkmcnt(0)\n\t" \
      "global_store_dwordx4 %6, v[" #r ":" #r "+3], %" #sb " sc1\n\tglobal_store_dwordx4 %7, v[" #r "+4:" #r "+7], %" #sb " sc1\n\t"
  asm volatile("v_mov_b32 v123, %12\n\tv_mov_b32 v127, %12\n\tv_mov_b32 v131, %12\n\tv_mov_b32 v135, %12\n\ts_cmp_eq_u32 %14, 3\n\ts_cbranch_scc1 3f\n\t"
               VO_TILE(120, 0, 9)
               VO_TILE(128, 1, 10) "3:\n\t" VO_POLL(0, 80, "")
               "s_setprio 3\n\t" VO_POLL(0, 96, VO_RD(120, 4, 5, 0)) "global_store_dwordx4 %8, v[120:123], %11 sc1\n\ts_nop 0"
               : : "v"(p.a[0]), "v"(p.a[1]), "v"(p.a[2]), "v"(p.a[3]), "v"(p.a[4]), "v"(p.a[5]), "v"(p.a[6]), "v"(p.a[7]), "v"(p.a[8]),
                   "s"(wsb + k * (16 * 24576)), "s"(wsb + (k + 3) * (16 * 24576)), "s"(wsb + (6 + (k >> 1)) * (16 * 24576)), "s"(tag), "i"(4 * OPW), "s"(k)
               : "vcc", "scc", "memory", "v120", "v121", "v122", "v123", "v124", "v125", "v126", "v127", "v128", "v129", "v130", "v131", "v132", "v133", "v134", "v135", "v136");
  TS(11);
}
struct Touch { uint64_t b; uint32_t when; };
#define TOUCH(t, pol) asm volatile("global_load_dword v166, v165, %0" pol : : "s"(t.b) : "v166")
#define LTOUCH() do { if (th.when - 4u < 8u) { for (uint32_t i_ = 4; i_ < th.when; ++i_) __builtin_amdgcn_s_sleep(9); TOUCH(th, TNT); } } while (0)
__device__ __forceinline__ uint32_t hint_max(char* wsb, uint32_t tag) {
  const uint32_t base = NOW() - 2048u;
#pragma unroll 1
  for (int spin = 0; spin < 3; ++spin) {
    uint32_t m;
    asm volatile("s_load_dword s72, %1, 0\n\t" "s_load_dword s73, %1, 64\n\t" "s_load_dword s74, %1, 128\n\t" "s_load_dword s75, %1, 192\n\t" "s_load_dword s76, %1, 256\n\t" "s_load_dword s77, %1, 320\n\t" "s_load_dword s78, %1, 384\n\t" "s_load_dword s79, %1, 448\n\t" "s_load_dword s80, %1, 512\n\t" "s_load_dword s81, %1, 576\n\t" "s_load_dword s82, %1, 640\n\t" "s_load_dword s83, %1, 704\n\t" "s_load_dword s84, %1, 768\n\t" "s_load_dword s85, %1, 832\n\t" "s_load_dword s86, %1, 896\n\t" "s_load_dword s87, %1, 960\n\t" "s_load_dword s88, %1, 1024\n\t" "s_load_dword s89, %1, 1088\n\t" "s_load_dword s90, %1, 1152\n\t" "s_load_dword s91, %1, 1216\n\t" "s_load_dword s92, %1, 1280\n\t" "s_load_dword s93, %1, 1344\n\t" "s_load_dword s94, %1, 1408\n\t" "s_load_dword s95, %1, 1472\n\t"
                 "s_mov_b32 %0, 0\n\ts_waitcnt lgkmcnt(0)\n\t"
                 "s_sub_u32 s72, s72, %2\n\ts_max_u32 %0, %0, s72\n\t" "s_sub_u32 s73, s73, %2\n\ts_max_u32 %0, %0, s73\n\t" "s_sub_u32 s74, s74, %2\n\ts_max_u32 %0, %0, s74\n\t" "s_sub_u32 s75, s75, %2\n\ts_max_u32 %0, %0, s75\n\t" "s_sub_u32 s76, s76, %2\n\ts_max_u32 %0, %0, s76\n\t" "s_sub_u32 s77, s77, %2\n\ts_max_u32 %0, %0, s77\n\t" "s_sub_u32 s78, s78, %2\n\ts_max_u32 %0, %0, s78\n\t" "s_sub_u32 s79, s79, %2\n\ts_max_u32 %0, %0, s79\n\t" "s_sub_u32 s80, s80, %2\n\ts_max_u32 %0, %0, s80\n\t" "s_sub_u32 s81, s81, %2\n\ts_max_u32 %0, %0, s81\n\t" "s_sub_u32 s82, s82, %2\n\ts_max_u32 %0, %0, s82\n\t" "s_sub_u32 s83, s83, %2\n\ts_max_u32 %0, %0, s83\n\t" "s_sub_u32 s84, s84, %2\n\ts_max_u32 %0, %0, s84\n\t" "s_sub_u32 s85, s85, %2\n\ts_max_u32 %0, %0, s85\n\t" "s_sub_u32 s86, s86, %2\n\ts_max_u32 %0, %0, s86\n\t" "s_sub_u32 s87, s87, %2\n\ts_max_u32 %0, %0, s87\n\t" "s_sub_u32 s88, s88, %2\n\ts_max_u32 %0, %0, s88\n\t" "s_sub_u32 s89, s89, %2\n\ts_max_u32 %0, %0, s89\n\t" "s_sub_u32 s90, s90, %2\n\ts_max_u32 %0, %0, s90\n\t" "s_sub_u32 s91, s91, %2\n\ts_max_u32 %0, %0, s91\n\t" "s_sub_u32 s92, s92, %2\n\ts_max_u32 %0, %0, s92\n\t" "s_sub_u32 s93, s93, %2\n\ts_max_u32 %0, %0, s93\n\t" "s_sub_u32 s94, s94, %2\n\ts_max_u32 %0, %0, s94\n\t" "s_sub_u32 s95, s95, %2\n\ts_max_u32 %0, %0, s95\n\t"
                 : "=&s"(m) : "s"(wsb + WS_HINT + spin * 2048), "s"(base + tag) : "scc", "memory", "s72", "s73", "s74", "s75", "s76", "s77", "s78", "s79", "s80", "s81", "s82", "s83", "s84", "s85", "s86", "s87", "s88", "s89", "s90", "s91", "s92", "s93", "s94", "s95");
    if (LIKELY(m < 4096u)) return base + m;
  }
  return NOW() - 60u;
}
__device__ __forceinline__ void ln2_block(const KaArgs& A, char* smem, char* wsb, uint32_t tag, const Touch& th) {
  typedef __attribute__((ext_vector_type(16))) float f32x16;
  typedef __attribute__((ext_vector_type(8))) uint32_t u32x8;
  const int tid = tidx() - 256;
  const int w = __builtin_amdgcn_readfirstlane(tid >> 6), lane = tid & 63, l5 = tid & 31;
  const int b = blockIdx.x, two = (b < 96), gs = two ? w >> 2 : 0, G = two ? 2 * b + gs : 96 + b;
  const int e = (tid >> 5) & 3, su = 2 * gs + ((w >> 1) & 1), row = 4 * su + e;
  const int tokw = tok0(G >> 1) + 2 * (G & 1) + ((w >> 1) & 1), tok = tokw + 16 * e;
  TS(12);
  uint32_t xv[9], wv[9], nv[9];
  volatile LDS_AS uint32_t* hw = (volatile LDS_AS uint32_t*)(smem + 52);
  uint32_t xo = (uint32_t)(tok * D_ * 2 + 4 * l5), wo = 4u * l5, vo = WS_VG + (uint32_t)(G * 12288 + (w & 3) * 3072 + lane * 16);
  asm volatile("" : "+v"(xo), "+v"(wo), "+v"(vo));
  VOut vp;
  if (w < 3 || w == 4) vp = v_prep(min(w, 3));
  UDONE();
  if (two || w >= 4) {
    LD9(xv, xo, A.x);
    LD9(wv, wo, A.wr);
  }
  LTOUCH();
  SB0();
  if (w < 3 || w == 4) {
    v_out(vp, wsb, tag, min(w, 3));
    if (w == 4) hw[2] = NOW();
  } else if (w == 5) {
    const uint32_t t0 = __builtin_amdgcn_readfirstlane(hw[0]);
    WAIT_T(t0, HT);
    hw[1] = hint_max(wsb, tag);
  }
  asm volatile("s_mov_b32 m0, %0" : : "s"(G_IMG + gs * 12288 + (w & 3) * 3072));
  TS(31);
  RAWBAR();
  if (!two && w < 4) { TSE(); __builtin_amdgcn_endpgm(); }
  SETPRIO(3);
  const uint32_t tg = __builtin_amdgcn_readfirstlane(hw[2] - hw[0] + hw[1]);
  TS(13);
  TSV(19, tg);
  while ((int)(NOW() - tg) < (two ? ((b < 80) ? VT4 : VT5) : VT)) {}
  TS(14);
  asm volatile("global_load_lds_dwordx4 %0, %1 sc1\n\tglobal_load_lds_dwordx4 %0, %1 offset:1024 sc1\n\t"
               "global_load_lds_dwordx4 %0, %1 offset:2048 sc1\n\ts_waitcnt vmcnt(3)" : : "v"(vo), "s"(wsb) : "memory");
  SB0();
  PIN9(xv); PIN9(wv);
  char* rec = smem + G_CH;
  uint32_t* cand = (uint32_t*)(rec + 64);
  uint32_t* so = (uint32_t*)(rec + 1024) + w * 576;
  if (w == 4) {
    if (lane < 4) *(u32x2*)(rec + 16 * lane + 8) = (u32x2){tag, tag};
    if (lane < 16) cand[lane] = 0u;
  }
  float ws = 0.f;
#pragma unroll
  for (int k = 0; k < 9; ++k) ws = DOT2(wv[k], 0x3f803f80u, ws);
  ws = half_sum(ws);
  f32x16 xa;
#pragma unroll
  for (int k = 0; k < 8; ++k) { xa[2 * k] = bflo(xv[k]); xa[2 * k + 1] = bfhi(xv[k]); }
  f32x2 xb = bf2x(xv[8]);
  u32x8 wa = {wv[0], wv[1], wv[2], wv[3], wv[4], wv[5], wv[6], wv[7]};
  uint32_t kl = (uint32_t)(0x7ff - tok) << 4;
  uint32_t ko = (uint32_t)(WS_KEYS + (lane >> 2) * KREP + b * 64 + (lane & 3) * 16);
  uint32_t ur = (uint32_t)(l5 * 64 + ((e ^ (l5 >> 2)) & 3) * 16), ga = WS_VG + G * 12288 + 2048 + ur;
  ur += G_IMG + gs * 12288;
  uint32_t ra = G_CH + 16 * su + 2 * e, ca = G_CH + 64 + 4 * row, ru = G_CH + 16 * (lane & 3), gb = ga + 6144u;
  asm volatile("s_waitcnt vmcnt(0) lgkmcnt(0)\n\ts_barrier" : "+{v[16:31]}"(xa), "+{v[32:33]}"(xb), "+{v[34:41]}"(wa), "+{v42}"(wv[8]), "+v"(ws),
               "+v"(kl), "+v"(ko), "+v"(ur), "+v"(ga), "+v"(ra), "+v"(ca), "+v"(ru), "+v"(gb) : : "memory");
  TS(15);
  float mb, rc;
  uint32_t me;
  asm volatile(LN2_ASM : "+{v[16:31]}"(xa), "+{v[32:33]}"(xb), "={v104}"(mb), "={v105}"(rc), "={v106}"(me)
               : "v"(ur), "v"(ga), "v"(gb), "v"(ws), "v"(ra), "v"(ca), "v"(kl), "v"(ru), "v"(ko), "s"((w & 2) ? 0x03020c0cu : 0x01000c0cu), "s"(tag), "s"(wsb),
                 "s"(two ? 7 : 3), "s"(~0ull >> (64 - 32 * NCOPY)), "{v[34:41]}"(wa), "{v42}"(wv[8])
               : "memory", "vcc", "scc", "s96", "s97", "s98", "s99", LN2_REGS);
  TS(16);
  bool first;
  {
    f32x2 h[9];
#pragma unroll
    for (int k = 0; k < 8; ++k) h[k] = (f32x2){xa[2 * k], xa[2 * k + 1]};
    h[8] = xb;
    mb *= -rc;
#pragma unroll
    for (int k = 0; k < 9; ++k) { const f32x2 c = h[k] * rc + mb; nv[k] = pack2(c[0], c[1]); }
    PIN9(nv);
    RAWBAR();
    {
      uint32_t c = 0;
#pragma unroll
      for (int k = 0; k < 4; ++k) {
        const u32x4 cq = *(const u32x4*)(cand + 4 * k);
#pragma unroll
        for (int j = 0; j < 4; ++j) c = __builtin_amdgcn_alignbit(c, me - cq[j], 31);
      }
      first = __builtin_popcount(c) < XR;
    }
#define XSTORE() _Pragma("unroll") for (int j = 0; j < 3; ++j) \
      st16s(wsb, WS_XN + (uint32_t)(tok * 1536 + j * 512 + l5 * 16), ((j < 2) ? (u32x4){nv[4 * j], nv[4 * j + 1], nv[4 * j + 2], tag} : (u32x4){nv[3], nv[7], nv[8], tag}))
    if (first) { XSTORE(); }
    SETPRIO(0);
    TS(17);
    uint32_t* sp = so + (lane >> 5) * 288 + l5;
#pragma unroll
    for (int k = 0; k < 9; ++k) {
      const f32x2 o = h[k] + bf2x(nv[k]);
      sp[32 * k] = pack2(o[0], o[1]);
    }
  }
  if (w >= 4) {
    uint32_t q[8];
    KEY_ADR(tid - 256);
    WAIT_T(tg, OT);
    key_sweep(wsb, tag, k0, k1, key_lows(tid - 256), q);
    const uint32_t m1 = max(max(max(q[0], q[1]), max(q[2], q[3])), max(max(q[4], q[5]), max(q[6], q[7]))), mine = cand[tid & 15] << 1;
    uint32_t cr = 0;
#pragma unroll 1
    for (int r = 0; r < 8 + 8 * two; ++r) {
      const uint32_t me = __builtin_amdgcn_readlane(mine, r);
      uint32_t c = __builtin_popcountll(__builtin_amdgcn_ballot_w64(m1 > me));
      if (c < 8u) {
        c = 0;
#pragma unroll
        for (int j = 0; j < 8; ++j) c += __builtin_popcountll(__builtin_amdgcn_ballot_w64(q[j] > me));
      }
      cr = ((tid & 15) == r) ? c : cr;
    }
    cand[16 + 4 * (tid & 15) + (w & 3)] = cr;
  }
  RAWBAR();
  const u32x4 c4 = *(const u32x4*)&cand[16 + 4 * row];
  const bool routed = (c4[0] + c4[1]) + (c4[2] + c4[3]) < 8u;
  const uint64_t rm = __builtin_amdgcn_ballot_w64(routed);
  if (!first && routed) { XSTORE(); }
  const int tokA = tokw + 32 * (w & 1);
#pragma unroll
  for (int k = 0; k < 3; ++k) {
    const int e = lane + 64 * k, r = (e >= 72);
    if (e < 144 && !((rm >> (32 * r)) & 1u)) st16s(A.out, (uint32_t)((tokA + 16 * r) * (D_ * 2) + (e - 72 * r) * 16), *(const u32x4*)(so + 4 * e));
  }
  TS(18);
  TSE();
}

__device__ __forceinline__ void ffn_block(const KaArgs& A, char* smem, char* wsb, uint32_t ep, uint32_t tag, uint32_t tstart, int pw) {
  typedef __attribute__((ext_vector_type(32))) uint32_t u32x32;
  typedef __attribute__((ext_vector_type(16))) uint32_t u32x16;
  const int lane = tidx() & 63, w3 = (pw * 11) >> 5, w = (pw - 3 * w3 == 2) ? 8 + w3 : pw - w3, tid = 64 * w + lane;
  const int u = (blockIdx.x >= 192) ? blockIdx.x - 192 : blockIdx.x - 120, f0 = 64 * u;
  const int n = lane & 15, kq = lane >> 4;
  const bool dn = w >= 8;
  const int wd = w & 3;
  TS(12);
  constexpr int F_WDL = 65536, F_PART = 8704, F_HID = F_PART + 8 * 4608, F_CAND = F_HID + 8 * 144, F_RED = F_CAND + 512, F_XN = F_RED + 6 * 256, XS = 1568;
  uint32_t* cand = (uint32_t*)(smem + F_CAND);
  u32x32 B0, B1;
  typedef __attribute__((ext_vector_type(8))) uint32_t u32x8;
  u32x8 QA, QH;
  u32x16 QB;
  uint32_t tm;
  const int l5 = 4 * w + kq;
  {
    const char *W, *W2;
    int64_t d0, d1, d2, d3;
    asm volatile("v_bfe_u32 v6, %[t], 4, 2\n\tv_and_b32 v7, 7, %[t]\n\ts_cmp_gt_u32 %[w], 7\n\ts_cbranch_scc1 1f\n\t"
                 "v_mov_b64 v[2:3], %[pg]\n\ts_mov_b32 exec_lo, 0xff00ff00\n\ts_mov_b32 exec_hi, 0xff00ff00\n\tv_mov_b64 v[2:3], %[pu]\n\ts_mov_b64 exec, -1\n\t"
                 "s_mul_i32 s98, %[w], 16*4608\n\ts_lshl_b32 s99, %[u], 7\n\ts_add_u32 s98, s98, s99\n\tv_lshl_add_u32 v7, v7, 4, s98\n\t"
                 "v_mul_u32_u24 v4, 129, v6\n\tv_and_b32 v4, 0x82, v4\n\ts_movk_i32 s98, 9216\n\tv_mad_u32_u24 v8, v4, s98, v7\n\t"
                 "v_or_b32 v6, 256, v6\n\ts_movk_i32 s98, 18432\n\tv_mad_u32_u24 v6, v6, s98, v7\n\t"
                 "s_mov_b64 %[d0], 9216\n\ts_mov_b64 %[d1], 63*9216\n\ts_mov_b32 s92, -61*9216\n\ts_mov_b32 s93, -1\n\ts_mov_b64 %[d3], 187*9216\n\ts_branch 2f\n1:\n\t"
                 "v_mov_b64 v[2:3], %[pd]\n\ts_and_b32 s97, %[w], 3\n\ts_mul_i32 s96, %[u], 64*1152\n\ts_lshl_b32 s98, s97, 8\n\ts_add_u32 s98, s98, s96\n\t"
                 "v_lshlrev_b32 v4, 2, v6\n\tv_lshl_or_b32 v4, v6, 3, v4\n\tv_and_b32 v4, 20, v4\n\tv_and_b32 v8, 15, %[t]\n\tv_lshl_add_u32 v8, v8, 4, s98\n\ts_movk_i32 s98, 1152\n\tv_mad_u32_u24 v8, v4, s98, v8\n\t"
                 "s_mulk_i32 s97, 16*1152\n\ts_add_u32 s97, s97, s96\n\ts_addk_i32 s97, 1024\n\tv_lshl_add_u32 v7, v7, 4, s97\n\tv_bfe_u32 v6, %[t], 3, 3\n\t"
                 "s_movk_i32 s98, 2304\n\tv_mad_u32_u24 v6, v6, s98, v7\n\t"
                 "s_mov_b64 %[d0], 1152\n\ts_mov_b64 %[d1], 1152\n\ts_mov_b64 %[d2], 29*1152\n\ts_mov_b32 s94, -27*1152\n\ts_mov_b32 s95, -1\n2:\n\t"
                 "v_mov_b32 v7, 0\n\tv_mov_b32 v9, 0\n\tv_lshl_add_u64 v[4:5], v[2:3], 0, v[6:7]\n\tv_lshl_add_u64 v[2:3], v[2:3], 0, v[8:9]"
                 : "=&{v[2:3]}"(W), "=&{v[4:5]}"(W2), [d0] "=&s"(d0), [d1] "=&s"(d1), [d2] "=&{s[92:93]}"(d2), [d3] "=&{s[94:95]}"(d3)
                 : [t] "v"(lane), [w] "s"(w), [u] "s"(u), [pg] "s"(A.wg), [pu] "s"(A.wu), [pd] "s"(A.wd) : "scc", "s96", "s97", "s98", "s99", "v6", "v7", "v8", "v9");
    asm volatile("" : "+s"(d0), "+s"(d1), "+s"(d2), "+s"(d3));
    u32x4 wa[8], wb[8], w8[2];
    const bool hlp = (u >> 6) & (0x170 >> pw);
    VOut vp;
    if (hlp) vp = v_prep(min(pw - 4, 3));
    TS(13);
    if (u >= 64) {
      if ((LE >> pw) & 1) UDONE(); else LWAIT(0u, 112, 4);
    } else if (WD && ((0xdb6 >> pw) & 1)) asm volatile("s_barrier");
    TS(14);
#define WL1(o, d) "global_load_dwordx4 %" #o ", %8, off" WNT "\n\tv_lshl_add_u64 %8, %8, 0, %" #d "\n\t"
#define WLD(r_) asm volatile(WL1(0, 9) WL1(1, 10) WL1(2, 9) WL1(3, 11) WL1(4, 9) WL1(5, 10) WL1(6, 9) WL1(7, 12) \
      : "=&v"(r_[0]), "=&v"(r_[1]), "=&v"(r_[2]), "=&v"(r_[3]), "=&v"(r_[4]), "=&v"(r_[5]), "=&v"(r_[6]), "=&v"(r_[7]), "+v"(W) : "s"(d0), "s"(d1), "s"(d2), "s"(d3))
#define PIN8(r_) asm volatile("" : "+v"(r_[0]), "+v"(r_[1]), "+v"(r_[2]), "+v"(r_[3]), "+v"(r_[4]), "+v"(r_[5]), "+v"(r_[6]), "+v"(r_[7]))
#define PRM(a_, b_, hi) ((hi) ? perm_hi(a_, b_) : perm_lo(a_, b_))
#define WTR(r_, B_) do { PIN8(r_); \
      _Pragma("unroll") for (int e = 0; e < 32; ++e) B_[e] = PRM(r_[e & 6][(e >> 4) + 2 * (e & 1)], r_[(e & 6) + 1][(e >> 4) + 2 * (e & 1)], (e >> 3) & 1); } while (0)
    WLD(wa);
    WLD(wb);
    asm volatile("global_load_dwordx4 %0, %2, off" WNT "\n\tv_lshl_add_u64 %2, %2, 0, %3\n\tglobal_load_dwordx4 %1, %2, off" WNT : "=&v"(w8[0]), "=&v"(w8[1]), "+v"(W2) : "s"(d0));
    if (hlp) {
      v_out(vp, wsb, tag, min(pw - 4, 3));
      LARRIVE(0u, 112);
      SETPRIO(0);
      VMWAIT(5);
    }
    SETPRIO(0);
    SB0();
    TS(15);
  QH[0] = 0u;
  QH[2] = (lane & 8) ? 0xeeeeeeeeu : 0x44444444u;
  if (dn) {
    KEY_ADR(tid - 512);
    const uint32_t klo = key_lows(tid - 512);
    const uint32_t dd = (u < 64) ? 64 * (u & 7) + lane : 512 + 8 * (u & 6) + n, fc = (u < 64) ? u >> 3 : 4 * (u & 1) + kq;
    const uint32_t q = (u < 64) ? 2 * (lane & 4) + (lane >> 3) : n, i = (u < 64) ? lane & 3 : kq;
    const uint32_t i3 = ((dd >> 6) * 86u) >> 8, p = dd - 192u * i3;
    QH[1] = F_RED + 4 * (32 * (i & 1) + 16 * (i >> 1) + q);
    QH[3] = k0; QH[4] = k1; QH[5] = klo; QH[6] = klo - 1024u * 0x10001u; QH[7] = klo - 32768u * 0x10001u; QB[0] = klo - 33792u * 0x10001u;
    QB[1] = F_CAND + 128 + 4 * (4 * wd + kq); QB[2] = F_CAND + 128 + (lane & 60); QB[3] = F_CAND + 128 + 16 * (lane & 3);
    QB[4] = F_CAND + 4 * (lane & 31); QB[5] = F_CAND + 2 * (lane & 32); QB[6] = 16 * lane; QB[7] = F_HID + (n & 7) * 144 + 32 * kq;
    QB[8] = WS_SLAB + u * SLABB + (uint32_t)(4 * (kq & 1) * 512 + 128 * wd + 64 * (kq >> 1) + 4 * n) * 4u + 2048u;
    QB[9] = WS_SLAB + u * SLABB + (uint32_t)(4096 + 128 * wd + 4 * lane) * 4u;
    QB[10] = F_CAND + 256 + 4 * fc; QB[11] = 2 * dd; QB[12] = WS_VG + vrun(p) + ((p >> 3) & 3);
    const uint32_t jj = dd >> 6, l2 = (jj & 3) == 3 || jj == 8;
    QB[13] = F_XN + fc * XS + (l2 ? 1024 + (jj >> 2) * 4 : (jj >> 2) * 512 + (jj & 3) * 4) + ((dd >> 1) & 31) * 16 + 2 * (dd & 1);
  } else {
    const uint32_t g = INTER ? tid >> 5 : 2 * (tid >> 6) + ((tid >> 4) & 1), k = INTER ? (tid >> 3) & 3 : 2 * ((tid >> 5) & 1) + ((tid >> 3) & 1), c = tid & 7;
    const uint32_t fpos = (u < 64) ? (u >> 1) * 128 + (u & 1) * 32 : 64 * u;
    QH[1] = 0u;
    QH[3] = F_XN + (n & 7) * XS + l5 * 16;
    QH[4] = F_PART + g * 288 + k * 64 + c * (INTER ? 8 : 4);
    QH[5] = F_HID + (4 * (k & 1) + (g & 3)) * 144 + 2 * (8 * c + 4 * (k >> 1) + (g >> 2));
    QH[6] = WS_SLAB + (uint32_t)(12 * w + kq) * SLABB + (fpos + (n >> 3) * ((u < 64) ? 64 : 32) + 4 * (n & 7)) * 4u;
    QB[1] = F_CAND + 256 + 4 * (n & 7);
    QB[2] = WS_XN + (uint32_t)((L_ - 1) * 1536 + l5 * 16);
  }
    asm volatile("" : "+{v[88:95]}"(QH), "+{v[96:111]}"(QB));
    TS(16);
    TS(17);
    VMWAIT(10);
    WTR(wa, B0);
    asm volatile("" : "+{v[16:47]}"(B0));
    asm volatile("s_cmp_lg_u32 %0, 0\n\ts_cbranch_scc1 1f\n\ts_waitcnt vmcnt(0)\n1:\n\ts_waitcnt vmcnt(5)" : : "s"((u >> 6) & (0x70 >> pw)) : "scc", "memory");
    SB0();
    TS(18);
    WTR(wb, B1);
    asm volatile("" : "+{v[48:79]}"(B1));
    asm volatile("" : "+v"(w8[0]), "+v"(w8[1]));
#pragma unroll
    for (int e = 0; e < 8; ++e) QA[e] = PRM(w8[0][(e >> 2) + 2 * (e & 1)], w8[1][(e >> 2) + 2 * (e & 1)], (e >> 1) & 1);
    TS(19);
    if (pw == 9 * (u >> 6)) { WAIT_T(tstart, HF); *(u32x4*)(smem + 96) = (u32x4){hint_max(wsb, tag), ep, tag, 0u}; }
    if (u >= 64) LWAIT(0u, 36, 4);
    if (tid < 32) cand[tid] = 0u;
    if (dn) {
      uint32_t* o2 = (uint32_t*)(smem + F_WDL + ((lane >> 1) & 3) * 2048 + (lane >> 5) * 1024 + (16 * wd + 8 * (lane & 1)) * 16) + ((lane >> 3) & 3);
#pragma unroll
      for (int e = 0; e < 8; ++e) o2[4 * ((e >> 1) + 4 * (e & 1))] = QA[e];
    }
    RAWBAR();
    const u32x4 et = *(const u32x4*)(smem + 96);
    tm = __builtin_amdgcn_readfirstlane(et[0]); ep = __builtin_amdgcn_readfirstlane(et[1]); tag = __builtin_amdgcn_readfirstlane(et[2]);
  }
  if (dn) {
    const u32x4 e0 = *(frag_p)(smem + F_WDL + wd * 2048 + lane * 16), e1 = *(frag_p)(smem + F_WDL + wd * 2048 + 1024 + lane * 16);
#pragma unroll
    for (int e = 0; e < 4; ++e) { QA[e] = e0[e]; QA[4 + e] = e1[e]; }
  }
  asm volatile("" : "+{v[80:87]}"(QA));
  TS(21);
  TSV(20, tm);
  if (dn) WAIT_T(tm, KT - 12);
  asm volatile(FFN_ASM : "+{v[16:47]}"(B0), "+{v[88:95]}"(QH), "+{v[96:111]}"(QB)
               : [w] "s"(w), [tag] "s"(tag), [wsb] "{s[70:71]}"(wsb), [tg] "s"(tm + KT), [x] "s"(A.x), [out] "s"(A.out), "{v[48:79]}"(B1), "{v[80:87]}"(QA)
               : "memory", "vcc", "scc", "s82", "s83", "s84", "s85", "s86", "s87", "s88", "s89", "s90", "s91", "s92", "s93", "s94", "s95", "s96", "s97", "s98", "s99",
                 FFN_REGS);
  if (u == 0 && w == 6 && (tidx() & 63) < 4) st16s(A.ws, (tidx() & 3) * 16u, (u32x4){ep + 0x9e3779b1u, 0u, 0u, 0u});
  TS(27);
  TSE();
}

#define LN1_REGS "v40", "v41", "v42", "v43", "v44", "v45", "v46", "v47", "v48", "v49", "v50", "v51", "v52", "v53", "v54", "v55", "v56", "v57", "v58", "v59", "v60", "v61", "v62", "v63", "v64", "v65", "v66", "v67", "v68", "v69", "v70", "v71", "v72", "v73", "v74", "v75", "v76", "v77", "v78", "v79", "v80", "v81", "v82", "v83", "v84", "v85", "v86", "v87", "v88", "v89", "v90", "v91"
#define KGEN_REGS LN1_REGS, "v92", "v93", "v94", "v95", "v96", "v97", "v98", "v99", "v100", "v101", "v102", "v103", "v104", "v105", "v106", "v107", "v108", "v109", "v110", "v111", "v112", "v113", "v114", "v115", "v116", "v117", "v118", "v119", "v120", "v121", "v122", "v123", "v124", "v125", "v126", "v127", "v128", "v129", "v130", "v131", "v132", "v133", "v134", "v135", "v136", "v137", "v138", "v139", "v140", "v141", "v142", "v143", "v144", "v145", "v146", "v147", "v148", "v149", "v150", "v151", "v152", "v153", "v154", "v155", "v156", "v157", "v158", "v159", "v160", "v161", "v162", "v163"
#define NEAR_REGS "v16", "v17", "v18", "v19", "v20", "v21", "v22", "v23", "v24", "v25", "v26", "v27", "v28", "v29", "v30", "v31", "v32", "v33", "v34", "v35", "v36", "v37", "v38", "v39", "v40", "v41", "v42", "v43", "v44", "v45", "v46", "v47", "v48", "v49", "v50", "v51", "v52", "v53", "v54", "v55", "v56", "v57", "v58", "v59", "v60", "v61", "v62", "v63", "v64", "v65", "v66", "v67", "v68", "v69", "v70", "v71", "v72", "v73", "v74", "v75", "v76", "v77", "v78", "v79", "v80", "v81", "v82", "v83", "v84", "v85", "v86", "v87", "v88", "v89", "v90", "v91", "v92", "v93", "v94", "v95", "v96", "v97", "v98", "v99", "v100", "v101", "v102", "v103", "v104", "v105", "v106", "v107", "v108", "v109", "v110", "v111", "v112", "v113", "v114", "v115", "v116", "v117", "v118", "v119", RING_REGS, "v152", "v153", "v154", "v155", "v156", "v157", "v158", "v159", "v160", "v161", "v162", "v163", "v164"
extern "C" __global__ __launch_bounds__(768, 3) __attribute__((amdgpu_num_vgpr(79))) void k_all(const KaArgs) {
  __shared__ __attribute__((aligned(128))) char smem[GRP];
  typedef __attribute__((ext_vector_type(8))) uint64_t u64x8;
  typedef __attribute__((ext_vector_type(8))) uint32_t u32x8;
  u64x8 a0, a1;
  Touch th;
  uint64_t t64;
  char* wsb;
  int w, c, wk, d;
  uint32_t ep, tag, dtb, sD, m0v, av, chb, opc;
  u32x8 mx, px, kx;
  const int b = blockIdx.x;
  asm volatile(ENTRY_ASM : [w] "=&s"(w), [c] "=&s"(c), [wk] "=&s"(wk), [d] "=&s"(d), [ep] "=&s"(ep), [sdt] "=&s"(dtb), [sd] "=&s"(sD), [tag] "=&s"(tag),
               [m0v] "=&s"(m0v), [av] "=&s"(av), [chb] "=&s"(chb), [opc] "=&s"(opc),
               "=&{s[36:51]}"(a0), "=&{s[52:67]}"(a1), "=&{s[68:69]}"(t64), "=&{s[70:71]}"(wsb), "=&{s[72:73]}"(th.b), "=&{s74}"(th.when), "=&{v[8:15]}"(mx), "=&{v[24:31]}"(px), "=&{v[32:39]}"(kx)
               : [ka] "s"(__builtin_amdgcn_kernarg_segment_ptr()), [b] "s"(b), [id] "s"((uint32_t)amdgcn_dispatch_id()), [rot] "i"(NROT - 1), "{v0}"(threadIdx.x)
               : "memory", "vcc", "scc", "s92", "s93", "s94", "s95", "s96", "s97", "s98", "s99", "v1", "v2", "v3", "v4", "v5", "v6", "v7", "v16", "v17", "v18", "v19", "v20", "v21", "v22", "v23", "v165", "v166", LN1_REGS TSCL);
  asm volatile("" : "+s"(a0), "+s"(a1), "+s"(t64), "+s"(wsb), "+s"(w), "+s"(c), "+s"(wk), "+s"(d), "+s"(ep), "+s"(dtb), "+s"(sD), "+s"(tag), "+s"(m0v), "+s"(av), "+s"(chb), "+s"(opc), "+s"(th.b), "+s"(th.when));
  const KaArgs A = {(const u16*)a0[0], (const u16*)a0[1], (const u16*)a0[2], (const u16*)a0[3], (const u16*)a0[4], (const u16*)a0[5], (const u16*)a0[6],
                    (const u16*)a0[7], (const u16*)a1[0], (char*)a1[1], (const u16*)a1[2], (const u16*)a1[3], (const u16*)a1[4], (const u16*)a1[5], (u16*)a1[6], nullptr};
  const uint32_t tstart = (uint32_t)t64;
  TSV(0, tstart);
  const int wq = w & 3;
  if (LIKELY(b < 192)) {
  TS(4);
  uint32_t ysb = dtb - (13u << 23);
#pragma unroll 1
  for (uint32_t tgt = 4;; tgt = 8) {
    uint32_t st;
    asm volatile(KGEN_ASM : "=&s"(st) : "s"(m0v), "s"(av), "s"(tgt), "s"(wk), "s"(EM & (1 << c)), "{v[24:31]}"(px), "{v[8:15]}"(mx), "{v[32:39]}"(kx), "{s[72:73]}"(th.b), "{s74}"(th.when)
                 : "memory", "vcc", "scc", "s94", "s95", "s96", "s97", "s98", "s99", "v166", KGEN_REGS);
    if (LIKELY(!st)) break;
    asm volatile("" : "+s"(dtb), "+s"(sD), "+s"(d));
    const float dt = __uint_as_float(dtb);
    float c200 = 200.f;
    int lo = -100, hi = 128;
    asm volatile("" : "+v"(c200), "+s"(lo), "+s"(hi));
    const int tid = tidx();
    const uint32_t o = d * (N_ * 2) + 4 * (tid & 63);
    uint32_t q[5];
    asm volatile("global_load_dword %0, %5, %6\n\tglobal_load_dword %1, %5, %7\n\tglobal_load_dword %2, %5, %8\n\tglobal_load_dword %3, %5, %9\n\t"
                 "global_load_dword %4, %5, %10\n\ts_waitcnt vmcnt(0)" : "=&v"(q[0]), "=&v"(q[1]), "=&v"(q[2]), "=&v"(q[3]), "=&v"(q[4])
                 : "v"(o), "s"(A.Are), "s"(A.Bre), "s"(A.Bim), "s"(A.Cre), "s"(A.Cim));
    const u16x2 mB = __builtin_elementwise_max(__builtin_bit_cast(u16x2, q[1] & 0x7fff7fffu), __builtin_bit_cast(u16x2, q[2] & 0x7fff7fffu));
    const u16x2 mC = __builtin_elementwise_max(__builtin_bit_cast(u16x2, q[3] & 0x7fff7fffu), __builtin_bit_cast(u16x2, q[4] & 0x7fff7fffu));
    const uint32_t mS = __builtin_bit_cast(uint32_t, mB + mC);
    const float gl = dt * (1.4426950408889634f * 2304.f);
    const int gi = (int)__builtin_ceilf(__builtin_amdgcn_fmed3f(__uint_as_float(px[2]) * gl, 0.f, c200));
    const int gm = (int)wave_maxu((uint32_t)(int)__builtin_ceilf(__builtin_amdgcn_fmed3f(__builtin_fmaxf(bflo(q[0]), bfhi(q[0])) * gl, 0.f, c200)));
    int ek = (int)(wave_maxu(max(mS, mS << 16)) >> 23) + (int)(dtb >> 23) - 3 * 127 + 12 + gm + ((gm + 7) >> 3);
    ek = min(max(max(ek, (int)((sD >> 23) & 255u) - 125), lo), hi);
    const bool h = tid & 32;
    const int sh = h ? gi + 13 - ek : 2 - gi;
    px[4] = __float_as_uint(__builtin_ldexpf(h ? __uint_as_float(px[4]) * dt : 1.f, sh));
    px[5] = __float_as_uint(__builtin_ldexpf(h ? __uint_as_float(px[5]) * dt : 0.f, sh));
    mx[7] = ((tid & 63) + wk == 0) ? __float_as_uint(__builtin_ldexpf(__uint_as_float(sD), 15 - ek)) : 0u;
    ysb = (uint32_t)__builtin_amdgcn_readfirstlane(ek + 105) << 23;
  }
  TS(6);
  {
#define CONV_ARGS : : "s"(ysb), "s"(chb), "s"(opc), "{v11}"(mx[3]), "{v0}"(threadIdx.x) \
                 : "memory", "vcc", "scc", "s98", "s99", "v1", "v2", "v3", "v4", "v5", "v6", "v7", "v8", "v9", NEAR_REGS
  if (w < 3) {
    asm volatile(NEAR_ASM CONV_ARGS);
    TS(9);
  } else if (wq == 3) {
    asm volatile(FAR_ASM CONV_ARGS);
    TS(9);
  } else if (c == 1) {
    const char* ub = wsb + WS_UG + (uint32_t)(b * 16384 + (w >> 3) * 1024 + 4096);
    asm volatile("s_mov_b32 m0, %0" : : "s"(G_C0 + OFF_U + (w >> 3) * 256));
    const uint32_t lane = tidx() & 63, l0 = 16u * lane, l1 = l0 + 8192u;
    uint32_t part = 0, to;
    asm volatile("v_mov_b32 %0, v165" : "=v"(to));
    to = (th.when == 3) ? to : 0u;
    const uint64_t tb = (th.when == 3) ? th.b : (uint64_t)A.wr;
    WAIT_T(tstart, UT);
    SETPRIO(3);
    TS(7);
#pragma unroll 1
    for (int spin = 0; spin < SPIN_CAP; ++spin) {
      uint32_t bad;
#define UL(k, a, o) "global_load_dwordx4 v[" #k ":" #k "+3], %" #a ", %4 offset:" #o " sc1\n\t"
#define US(k, n, o) "s_waitcnt vmcnt(" #n ")\n\tds_write_addtid_b32 v[" #k "] offset:" #o "\n\tds_write_addtid_b32 v[" #k "+1] offset:%6+" #o "\n\t" \
                    "ds_write_addtid_b32 v[" #k "+2] offset:%7+" #o "\n\tv_bitop3_b32 %0, %0, v[" #k "+3], %5 bitop3:0xf6\n\t"
#define UF(q) "v_cmp_ne_u32 vcc, 0, %0\n\ts_cbranch_vccnz 1f\n\tds_write_b32 %8, %9 offset:64+8*" #q "\n\t"
#define UG "s_setprio 0\n2:\n\ts_sleep 1\n\ts_memrealtime s[98:99]\n\ts_waitcnt lgkmcnt(0)\n\ts_sub_u32 s98, s98, %10\n\ts_cmp_lt_i32 s98, 0\n\ts_cbranch_scc1 2b\n\t"
      asm volatile("v_mov_b32 %0, 0\n\t"
                   UL(120, 2, -4096) UL(124, 2, -2048) UL(128, 2, 0) UL(132, 2, 2048) UL(136, 3, -4096) UL(140, 3, -2048) UL(144, 3, 0) UL(148, 3, 2048)
                   US(120, 7, 0) US(124, 6, 512) UF(0) TSQ(1) "global_load_dword v166, %12, %11" TNT "\n\t" US(128, 6, 1024) US(132, 5, 1536) UF(1) US(136, 4, 2048) US(140, 3, 2560) UF(2)
                   US(144, 2, 3072) US(148, 1, 3584) UF(3) "s_branch 4f\n1:\n\ts_waitcnt vmcnt(0)\n4:"
                   : "=&v"(bad), "+s"(part) : "v"(l0), "v"(l1), "s"(ub), "s"(tag), "i"(CHS), "i"(2 * CHS), "v"((uint32_t)(w >> 3) * 4u), "v"(1u), "s"(tstart + UD), "s"(tb), "v"(to)
                   : "memory", "vcc", "scc", "s98", "s99", "v166", RING_REGS);
      part = __builtin_amdgcn_readfirstlane(part);
      if (LIKELY(!__any(bad != 0u))) break;
      __builtin_amdgcn_s_sleep(USLEEP);
      if (spin == SPIN_CAP - 1 && lane < 4) *(volatile LDS_AS uint32_t*)(smem + 64 + 8 * lane + 4 * (w >> 3)) = 1u;
    }
    TS(8);
  }
  }
  if (w == 9 && b < 160) {
    const int lane = tidx() & 63;
    LWAIT(4u * (lane & 1), 64, 1);
    const uint32_t t = NOW();
    if (lane < 12 && (b & 56) == 8) st16s(wsb, WS_HINT + ((b >> 6) * 8 + (b & 7)) * 64 + 16 * (lane & 3) + 512 * (lane & 12), (u32x4){t + tag, 0u, 0u, 0u});
    *(volatile LDS_AS uint32_t*)(smem + 52) = t;
  }
  SETPRIO(0);
  TS(10);
  if (b < 160) {
    if (w < 4) { asm volatile("s_waitcnt lgkmcnt(0)\n\ts_barrier"); TSE(); __builtin_amdgcn_endpgm(); }
    ln2_block(A, smem, wsb, tag, th);
    __builtin_amdgcn_endpgm();
  }
  SETPRIO(0);
  if (b < 184) {
    if (th.when - 4u < 8u) { UDONE(); LTOUCH(); }
    if ((0x170 >> w) & 1) v_out(v_prep(min(w - 4, 3)), wsb, tag, min(w - 4, 3));
    TSE();
    __builtin_amdgcn_endpgm();
  }
    SETPRIO(2);
  } else {
    SETPRIO(0);
    if (WD && c == 0) asm volatile("s_barrier");
  }
  ffn_block(A, smem, wsb, ep, tag, tstart, w);
}

extern "C" void kernel_launch(void* const* d_in, const int* in_sizes, int n_in,
                              void* d_out, int out_size, void* d_ws, size_t ws_size,
                              hipStream_t stream) {
  const u16* x = (const u16*)d_in[0];
  const u16* Are = (const u16*)d_in[1];
  const u16* Aim = (const u16*)d_in[2];
  const u16* Bre = (const u16*)d_in[3];
  const u16* Bim = (const u16*)d_in[4];
  const u16* Cre = (const u16*)d_in[5];
  const u16* Cim = (const u16*)d_in[6];
  const u16* logdt = (const u16*)d_in[7];
  const u16* Dskip = (const u16*)d_in[8];
  const u16* wr = (const u16*)d_in[10];
  const u16* wg = (const u16*)d_in[11];
  const u16* wu = (const u16*)d_in[12];
  const u16* wd = (const u16*)d_in[13];
  u16* out = (u16*)d_out;
  const KaArgs ka = {x, Are, Aim, Bre, Bim, Cre, Cim, logdt, Dskip, (char*)d_ws, wr, wg, wu, wd, out, nullptr};
  k_all<<<256, 768, 0, stream>>>(ka);
}
```

```cpp
#define XR 3
#define KT 185
#define FW 56
#include <hip/hip_runtime.h>
#include <hip/hip_bf16.h>
#include <stdint.h>

typedef __attribute__((ext_vector_type(8))) short bf16x8;
typedef __attribute__((ext_vector_type(4))) float f32x4;
typedef uint16_t u16;
typedef __attribute__((ext_vector_type(4))) uint32_t u32x4;
typedef __attribute__((ext_vector_type(3))) uint32_t u32x3;
typedef __attribute__((ext_vector_type(2))) uint32_t u32x2;
#define STR2(x) #x
#define STR(x) STR2(x)
__device__ __forceinline__ int tidx() { int t = threadIdx.x; asm volatile("" : "+v"(t)); return t; }
#define TSD0
#define TSCL
#define TS(i)
#define TSV(i, v)
#define TSQ(i)
#define TSE()

#define LIKELY(x) __builtin_expect(!!(x), 1)
constexpr int L_ = 2048, D_ = 576, N_ = 128, F_ = 4608, C_ = 8;

__device__ __forceinline__ float bf2f(u16 v) { return __uint_as_float((uint32_t)v << 16); }
__device__ __forceinline__ float bflo(uint32_t v) { return __uint_as_float(v << 16); }
__device__ __forceinline__ float bfhi(uint32_t v) { return __uint_as_float(v & 0xffff0000u); }
__device__ __forceinline__ u16 f2bf(float f) { return __builtin_bit_cast(u16, (__bf16)f); }
__device__ __forceinline__ float rbf(float f) { return bf2f(f2bf(f)); }
typedef __attribute__((ext_vector_type(2))) float f32x2;
typedef __attribute__((ext_vector_type(2))) __bf16 bf16x2v;
__device__ __forceinline__ uint32_t pack2(float a, float b) {
  return __builtin_bit_cast(uint32_t, __builtin_convertvector((f32x2){a, b}, bf16x2v));
}

#define DPP_I(v, ctrl, rmask) __builtin_amdgcn_update_dpp(0, (v), (ctrl), (rmask), 0xf, true)
__device__ __forceinline__ uint32_t wave_maxu(uint32_t v) {
  v = max(v, (uint32_t)DPP_I((int)v, 0x111, 0xf));
  v = max(v, (uint32_t)DPP_I((int)v, 0x112, 0xf));
  v = max(v, (uint32_t)DPP_I((int)v, 0x114, 0xf));
  v = max(v, (uint32_t)DPP_I((int)v, 0x118, 0xf));
  v = max(v, (uint32_t)DPP_I((int)v, 0x142, 0xa));
  v = max(v, (uint32_t)DPP_I((int)v, 0x143, 0xc));
  return (uint32_t)__builtin_amdgcn_readlane((int)v, 63);
}
__device__ __forceinline__ void wave_lds_sync() {
  __builtin_amdgcn_s_waitcnt(0xc07f);
  __builtin_amdgcn_wave_barrier();
}
__device__ __forceinline__ void st2(void* p, uint32_t v) {
  asm volatile("global_store_short %0, %1, off sc1" : : "v"(p), "v"(v) : "memory");
}

typedef __attribute__((ext_vector_type(8))) _Float16 f16x8;
typedef __attribute__((ext_vector_type(2))) _Float16 f16x2;
typedef __attribute__((ext_vector_type(2))) unsigned short u16x2;
#define LDS_AS __attribute__((address_space(3)))
constexpr int PS = 288;
constexpr int OPW = 32 * PS;
constexpr int CS = 2112;
constexpr int OFF_F3 = 1184;
constexpr int OFF_F4 = OFF_F3 + 416;
constexpr int OFF_X = 2064;
constexpr int OFF_U = CS + 480;
constexpr int CHS = 4 * CS + 480 + 4096;
constexpr int G_CNT = 0;
constexpr int G_BAND = 512;
constexpr int G_OP = G_BAND + 2304;
constexpr int G_C0 = G_OP + 12 * OPW + 3 * CS;
constexpr int GRP = G_OP + 12 * OPW + 3 * CHS + 7168;
constexpr int G_IMG = G_OP + 2 * OPW, G_CH = G_OP + 6 * OPW;
static_assert(G_OP % 128 == 0 && G_C0 % 16 == 0 && CHS % 16 == 0 && GRP <= 160 * 1024, "lds map");
__device__ __forceinline__ uint32_t packh2(float a, float b) {
  return __builtin_bit_cast(uint32_t, __builtin_convertvector((f32x2){a, b}, f16x2));
}
__device__ __forceinline__ f32x2 cmul(f32x2 a, f32x2 b) {
  f32x2 t, r;
  asm("v_pk_mul_f32 %0, %1, %2 op_sel_hi:[0,1]" : "=v"(t) : "v"(a), "v"(b));
  asm("v_pk_fma_f32 %0, %1, %2, %3 op_sel:[1,1,0] op_sel_hi:[1,0,1] neg_lo:[1,0,0]" : "=v"(r) : "v"(a), "v"(b), "v"(t));
  return r;
}
__device__ __forceinline__ void cmul2(f32x2& r0, f32x2& r1, f32x2 a0, f32x2 b0, f32x2 a1, f32x2 b1) {
  asm("v_pk_mul_f32 %0, %2, %3 op_sel_hi:[0,1]\n\tv_pk_mul_f32 %1, %4, %5 op_sel_hi:[0,1]\n\t"
      "v_pk_fma_f32 %0, %2, %3, %0 op_sel:[1,1,0] op_sel_hi:[1,0,1] neg_lo:[1,0,0]\n\t"
      "v_pk_fma_f32 %1, %4, %5, %1 op_sel:[1,1,0] op_sel_hi:[1,0,1] neg_lo:[1,0,0]"
      : "=&v"(r0), "=&v"(r1) : "v"(a0), "v"(b0), "v"(a1), "v"(b1));
}
__device__ __forceinline__ f32x2 cexp2(f32x2 a, float l) {
  const f32x2 t = a * l;
  const float f = __builtin_amdgcn_fractf(t[1]);
  return (f32x2){__builtin_amdgcn_cosf(f), __builtin_amdgcn_sinf(f)} * __builtin_amdgcn_exp2f(t[0]);
}
#define MFMA16(a, b, c) __builtin_amdgcn_mfma_f32_16x16x32_f16(__builtin_bit_cast(f16x8, a), __builtin_bit_cast(f16x8, b), c, 0, 0, 0)
__device__ __forceinline__ f32x2 bf2x(uint32_t v) { return (f32x2){bflo(v), bfhi(v)}; }
__device__ __forceinline__ float half_sum(float v) {
  v += __int_as_float(DPP_I(__float_as_int(v), 0xb1, 0xf));
  v += __int_as_float(DPP_I(__float_as_int(v), 0x4e, 0xf));
  v += __int_as_float(DPP_I(__float_as_int(v), 0x141, 0xf));
  v += __int_as_float(DPP_I(__float_as_int(v), 0x140, 0xf));
  const auto r = __builtin_amdgcn_permlane16_swap(__float_as_uint(v), __float_as_uint(v), false, false);
  return __uint_as_float(r[0]) + __uint_as_float(r[1]);
}

#define DADD(v, ctrl) v += __int_as_float(DPP_I(__float_as_int(v), ctrl, 0xf))
#define DOT2(a, b, c) __builtin_amdgcn_fdot2_f32_bf16(__builtin_bit_cast(bf16x2v, a), __builtin_bit_cast(bf16x2v, b), c, false)
typedef const volatile LDS_AS u32x2* afrag_p;
typedef const volatile LDS_AS u32x4* frag_p;
__device__ __forceinline__ u32x4 ld_af(afrag_p p, int off) {
  const u32x2 lo = p[off / 8], hi = p[off / 8 + 1];
  return (u32x4){lo[0], lo[1], hi[0], hi[1]};
}
__device__ __forceinline__ u32x4 pack_w(f32x4 a, f32x4 b) {
  return (u32x4){packh2(a[0], a[1]), packh2(a[2], a[3]), packh2(b[0], b[1]), packh2(b[2], b[3])};
}
#define SB0() __builtin_amdgcn_sched_barrier(0)
__device__ __forceinline__ uint32_t perm_lo(uint32_t a, uint32_t b) { return __builtin_amdgcn_perm(b, a, 0x05040100u); }
__device__ __forceinline__ uint32_t perm_hi(uint32_t a, uint32_t b) { return __builtin_amdgcn_perm(b, a, 0x07060302u); }
__device__ __forceinline__ void st16s(void* sbase, uint32_t voff, u32x4 v) {
  asm volatile("global_store_dwordx4 %0, %1, %2 sc1\n\ts_nop 1" : : "v"(voff), "v"(v), "s"(sbase) : "memory");
}
constexpr int SPIN_CAP = 1 << 18;
extern "C" __device__ uint64_t amdgcn_dispatch_id() __asm("llvm.amdgcn.dispatch.id");
#define UT 205
#define VT 40
#define OT 170
#define PF 1
#define P2 "1"
#define P0 "0"
#define PT "3"
#define P1 "2"
#define PM "3"
#define PW "3"
#define WD 1
#define VT5 20
#define VT4 5
#define MK 0
#define HT 75
#define HF 540
#define PE "0"
#define PL "2"
#define PL1 "3"
#define EM 2
#define UD 0
#define PN "3"
#define TOPCAP "8"
#define LE 0xf70
#define WNT NT_
#define TNT
#define TSLOT_LO 0x90a00
#define TSLOT_HI 0x0
#define TWHEN_LO 0xfff3f0ff
#define TWHEN_HI 0xffff
#define KOFF 10
#define WSOFS 4096
#define DPRI 1
#define EB 0
#define MIDBR "s_cbranch_vccnz"
#define H2 125
#define D2 130
#define PC2 "3"
#define NS 16
#define D1 190
#define NT_ " nt"
#define XNT
#define PNT
#define PORD 0x136c479a
#define AV0 "1"
#define ESL "1"
#define FW2 20
#define USLEEP 4
#define PSLEEP 2
#define PSLEEP2 1
#define NCOPY 2
#define FORCE1 0
#define NROT 8
#define VMWAIT(n) do { asm volatile("s_waitcnt vmcnt(" #n ")" ::: "memory"); __builtin_amdgcn_sched_barrier(0); } while (0)
struct KaArgs {
  const u16 *x, *Are, *Aim, *Bre, *Bim, *Cre, *Cim, *logdt, *Dskip;
  char* ws;
  const u16 *wr, *wg, *wu, *wd;
  u16* out;
  void* spare;
};
constexpr uint32_t WS_UG = 0, WS_VG = 3u << 20, WS_XN = 6u << 20, WS_SLAB = 9u << 20, WS_KEYS = 11u << 20, WS_HINT = 12u << 20, WS_SET = 16u << 20, WSOFF = 4096;
constexpr uint32_t SLABB = C_ * D_ * 4;
#define CEX(a, b) { const uint32_t hi_ = max(a, b), lo_ = min(a, b); a = hi_; b = lo_; }
#define MFMAB(a, b, c) __builtin_amdgcn_mfma_f32_16x16x32_bf16(__builtin_bit_cast(bf16x8, a), __builtin_bit_cast(bf16x8, b), c, 0, 0, 0)

__device__ __forceinline__ u32x4 tag_bits(uint32_t t) {
  return (u32x4){t & 0x70007u, (t >> 3) & 0x70007u, (t >> 6) & 0x70007u, (t >> 9) & 0x70007u};
}
__device__ __forceinline__ uint32_t spat(uint32_t t, int j) { return ((j == 0) ? t : (j == 1) ? t >> 16 : (j == 2) ? t >> 8 : t >> 22) & 1023u; }
#define XOR_OR(acc, a, b) acc = __builtin_amdgcn_bitop3_b32(acc, a, b, 0xf6)
#define NOW() ((uint32_t)__builtin_amdgcn_s_memrealtime())
#define WAIT_T(t0, dt) do { while ((int)(NOW() - (t0)) < (dt) - 30) __builtin_amdgcn_s_sleep(8); \
    while ((int)(NOW() - (t0)) < (dt)) __builtin_amdgcn_s_sleep(1); } while (0)
#define SETPRIO(n) __builtin_amdgcn_s_setprio(n)
typedef __attribute__((ext_vector_type(4))) short s16x4;
__device__ __forceinline__ u32x2 ld_tr(const char* p) {
  return __builtin_bit_cast(u32x2, __builtin_amdgcn_ds_read_tr16_b64_v4i16((LDS_AS s16x4*)p));
}
#define LARRIVE(adr, off) asm volatile("s_mov_b64 exec, 1\n\tds_add_u32 %0, %1 offset:%2\n\ts_mov_b64 exec, -1" : : "v"(adr), "v"(1u), "i"(off) : "memory")
#define LWAIT(adr, off, n) LWAITS(adr, off, n, 1)
#define LWAITS(adr, off, n, sl) do { uint32_t t_; asm volatile("1:\n\tds_read_b32 %0, %1 offset:%2\n\ts_waitcnt lgkmcnt(0)\n\tv_cmp_gt_u32 vcc, %3, %0\n\t" \
      "s_cbranch_vccz 2f\n\ts_sleep " #sl "\n\ts_branch 1b\n2:" : "=&v"(t_) : "v"(adr), "i"(off), "i"(n) : "vcc", "memory"); SB0(); } while (0)
#define RAWBAR() do { asm volatile("s_waitcnt lgkmcnt(0)\n\ts_barrier" ::: "memory"); SB0(); } while (0)
#define PIN9(v) asm volatile("" : "+v"(v[0]), "+v"(v[1]), "+v"(v[2]), "+v"(v[3]), "+v"(v[4]), "+v"(v[5]), "+v"(v[6]), "+v"(v[7]), "+v"(v[8]))
#define LD9(v, o, base) asm volatile("global_load_dword %0, %9, %10\n\tglobal_load_dword %1, %9, %10 offset:128\n\t" \
                 "global_load_dword %2, %9, %10 offset:256\n\tglobal_load_dword %3, %9, %10 offset:384\n\t" \
                 "global_load_dword %4, %9, %10 offset:512\n\tglobal_load_dword %5, %9, %10 offset:640\n\t" \
                 "global_load_dword %6, %9, %10 offset:768\n\tglobal_load_dword %7, %9, %10 offset:896\n\t" \
                 "global_load_dword %8, %9, %10 offset:1024" \
                 : "=&v"(v[0]), "=&v"(v[1]), "=&v"(v[2]), "=&v"(v[3]), "=&v"(v[4]), "=&v"(v[5]), "=&v"(v[6]), "=&v"(v[7]), "=&v"(v[8]) \
                 : "v"(o), "s"(base))
#define KSZ "28736"
__device__ __forceinline__ uint32_t vrun(int p) { return (uint32_t)(((p & 1) * 96 + (p >> 1)) * 64); }
__device__ __forceinline__ int tok0(int q) { return 256 * (q >> 4) + 16 * (q & 12) + 4 * (q & 3); }

#define ENTRY_ASM \
  "s_load_dwordx16 s[36:51], %[ka], 0x0\n\t" \
  "s_load_dwordx16 s[52:67], %[ka], 0x40\n\t" \
  "s_memrealtime s[68:69]\n\t" \
  "v_readfirstlane_b32 %[w], v0\n\t" \
  "v_and_b32 v1, 31, v0\n\t" \
  "v_and_b32 v2, 63, v0\n\t" \
  "s_lshl2_add_u32 s99, %[w], %[b]\n\t" \
  "s_sub_u32 s99, s99, 448\n\t" \
  "s_cmp_ge_u32 s99, 64\n\t" \
  "s_cbranch_scc1 1f\n\t" \
  "s_lshl_b32 s99, s99, 4\n\t" \
  "v_lshl_or_b32 v6, v1, 10, s99\n\t" \
  "v_min_u32 v6, " KSZ ", v6\n\t" \
  "s_getpc_b64 s[98:99]\n\t" \
  "global_load_dword v166, v6, s[98:99]\n\t" \
  "1:\n\t" \
  "s_lshr_b32 s98, %[w], 5\n\t" \
  "s_lshr_b32 %[w], %[w], 6\n\t" \
  "s_lshr_b32 %[c], 0x924924, s98\n\t" \
  "s_and_b32 %[c], %[c], 3\n\t" \
  TSD0 \
  "s_add_u32 %[wk], %[w], %[c]\n\t" \
  "s_and_b32 %[wk], %[wk], 3\n\t" \
  "s_mul_i32 %[d], %[c], 192\n\t" \
  "s_add_u32 %[d], %[d], %[b]\n\t" \
  "s_lshl2_add_u32 s98, %[d], %[wk]\n\t" \
  "s_lshl_b32 s99, s98, 6\n\t" \
  "s_mulk_i32 s98, 2304\n\t" \
  "v_lshl_add_u32 v3, v1, 1, s99\n\t" \
  "v_lshl_add_u32 v4, v1, 2, s98\n\t" \
  "v_and_b32 v6, 32, v0\n\t" \
  "v_mad_u32_u24 v4, v6, 36, v4\n\t" \
  "v_mov_b64 v[26:27], 0\n\t" \
  "v_mov_b32 v28, 0x42800000\n\t" \
  "v_mov_b32 v29, 0\n\t" \
  "v_mov_b32 v30, 1.0\n\t" \
  "v_mov_b32 v31, 0\n\t" \
  "s_lshl_b32 %[av], %[d], 1\n\t" \
  "s_and_b32 %[av], %[av], -4\n\t" \
  "s_lshl_b32 s97, %[wk], 10\n\t" \
  "s_add_u32 s96, s97, 152480\n\t" \
  "v_lshl_add_u32 v5, v2, 4, s96\n\t" \
  "v_mov_b64 v[40:41], 0\n\t" \
  "v_mov_b64 v[42:43], 0\n\t" \
  "s_cmp_ge_u32 %[d], 384\n\t" \
  "s_cbranch_scc1 50f\n\t" \
  "s_cmp_lg_u32 %[c], 0\n\t" \
  "s_cbranch_scc1 7f\n\t" \
  "s_setprio " PN "\n\t" \
  "s_waitcnt lgkmcnt(0)\n\t" \
  "global_load_dword v16, v4, s[36:37] offset:0" XNT "\n\t" \
  "global_load_dword v17, v4, s[36:37] offset:128" XNT "\n\t" \
  "global_load_dword v18, v4, s[36:37] offset:256" XNT "\n\t" \
  "global_load_dword v19, v4, s[36:37] offset:384" XNT "\n\t" \
  "global_load_dword v20, v4, s[36:37] offset:512" XNT "\n\t" \
  "global_load_dword v21, v4, s[36:37] offset:640" XNT "\n\t" \
  "global_load_dword v22, v4, s[36:37] offset:768" XNT "\n\t" \
  "global_load_dword v23, v4, s[36:37] offset:896" XNT "\n\t" \
  "global_load_dword v24, v4, s[36:37] offset:1024" XNT "\n\t" \
  "s_cmp_gt_u32 %[b], 191\n\t" \
  "s_cbranch_scc1 2f\n\t" \
  "7:\n\t" \
  "s_waitcnt lgkmcnt(0)\n\t" \
  "global_load_short_d16_hi v26, v3, s[38:39]" PNT "\n\t" \
  "global_load_short_d16_hi v27, v3, s[40:41]" PNT "\n\t" \
  "s_mov_b32 exec_lo, 0\n\t" \
  "global_load_short_d16_hi v28, v3, s[42:43]" PNT "\n\t" \
  "global_load_short_d16_hi v29, v3, s[44:45]" PNT "\n\t" \
  "global_load_short_d16_hi v30, v3, s[46:47]" PNT "\n\t" \
  "global_load_short_d16_hi v31, v3, s[48:49]" PNT "\n\t" \
  "s_mov_b32 exec_lo, -1\n\t" \
  "2:\n\t" \
  "s_load_dword %[sdt], s[50:51], %[av]\n\t" \
  "s_load_dword %[sd], s[52:53], %[av]\n\t" \
  "s_load_dword %[ep], s[54:55], 0x0\n\t" \
  "8:\n\t" \
  "s_mul_i32 %[chb], %[c], 13024\n\t" \
  "s_add_u32 %[chb], %[chb], 119744\n\t" \
  "s_add_u32 s98, s97, %[chb]\n\t" \
  "s_addk_i32 s98, -9696\n\t" \
  "v_lshl_add_u32 v6, v2, 4, s98\n\t" \
  "ds_write_b128 v6, v[40:43] offset:4096\n\t" \
  "ds_write_b128 v6, v[40:43] offset:8192\n\t" \
  "s_cmp_lg_u32 %[wk], 3\n\t" \
  "s_cbranch_scc1 1f\n\t" \
  "s_mov_b32 exec_lo, 0xfffc0000\n\t" \
  "ds_write_b128 v6, v[40:43]\n\t" \
  "s_mov_b32 exec_lo, -1\n\t" \
  "1:\n\t" \
  "v_mov_b32 v32, 0x3fb8aa3b\n\t" \
  "v_mov_b32 v33, 0.15915494\n\t" \
  "v_mov_b32 v34, 0x43800000\n\t" \
  "s_mov_b32 exec_lo, 0\n\t" \
  "v_mov_b32 v32, 0x4138aa3b\n\t" \
  "v_mov_b32 v33, 0xbfa2f983\n\t" \
  "v_mov_b32 v34, 0x41000000\n\t" \
  "s_mov_b32 exec_lo, -1\n\t" \
  "s_and_b32 s70, %[id], %[rot]\n\t" \
  "s_lshl_b32 s70, s70, 24\n\t" \
  "s_add_u32 s70, s70, " STR(WSOFS) "\n\t" \
  "s_add_u32 s70, s70, s54\n\t" \
  "s_addc_u32 s71, s55, 0\n\t" \
  "s_mul_i32 %[opc], %[c], 36864\n\t" \
  "s_add_u32 %[opc], %[opc], 2816\n\t" \
  "s_mul_i32 %[tag], %[id], 0x5bd1e996\n\t" \
  "v_lshlrev_b32 v11, 2, %[c]\n\t" \
  "s_mul_i32 s96, %[wk], 9216\n\t" \
  "s_add_u32 m0, s96, %[opc]\n\t" \
  "s_mov_b32 s75, 4\n\t" \
  "s_lshr_b32 s76, 0x41000, %[w]\n\t" \
  "s_and_b32 s76, s76, 256\n\t" \
  "s_bitcmp1_b32 " STR(EM) ", %[c]\n\t" \
  "s_addc_u32 s76, s76, 0\n\t" \
  "s_lshr_b32 s96, %[wk], 1\n\t" \
  "s_and_b32 s97, %[wk], 1\n\t" \
  "s_sub_u32 s96, s96, s97\n\t" \
  "s_mulk_i32 s96, 4608\n\t" \
  "s_add_u32 s78, s96, %[opc]\n\t" \
  "s_lshl_b32 s98, %[w], 2\n\t" \
  "s_mov_b32 s72, " STR(TWHEN_LO) "\n\t" \
  "s_mov_b32 s73, " STR(TWHEN_HI) "\n\t" \
  "s_lshr_b64 s[72:73], s[72:73], s98\n\t" \
  "s_and_b32 s74, s72, 15\n\t" \
  "s_cbranch_scc1 1f\n\t" \
  "s_call_b64 s[90:91], 65f\n\t" \
  "global_load_dword v166, v165, s[72:73]" TNT "\n\t" \
  "1:\n\t" \
  "s_lshl_b32 s97, %[d], 4\n\t" \
  "s_mov_b32 %[av], " AV0 "\n\t" \
  "s_mov_b32 s96, 158624\n\t" \
  "v_and_b32 v25, 3, v0\n\t" \
  "v_lshl_add_u32 v25, v25, 2, s96\n\t" \
  "v_mov_b32 v15, 0\n\t" \
  "v_mov_b32 v6, 0x3fb8aa3b\n\t" \
  "s_waitcnt lgkmcnt(0)\n\t" \
  "s_lshr_b32 %[sdt], %[sdt], s97\n\t" \
  "s_lshl_b32 %[sdt], %[sdt], 16\n\t" \
  "v_mul_f32 v6, %[sdt], v6\n\t" \
  "s_add_u32 %[tag], %[tag], %[ep]\n\t" \
  "v_exp_f32 v6, v6\n\t" \
  "s_lshl_b32 %[tag], %[tag], 1\n\t" \
  "s_bitcmp0_b32 %[tag], 16\n\t" \
  "v_cvt_pk_bf16_f32 v6, 0, v6\n\t" \
  "s_addc_u32 %[tag], %[tag], 0\n\t" \
  "s_lshr_b32 %[sd], %[sd], s97\n\t" \
  "v_pk_mul_f32 v[32:33], v[32:33], v[6:7] op_sel_hi:[1,0]\n\t" \
  "s_lshl_b32 %[sd], %[sd], 16\n\t" \
  "v_readfirstlane_b32 %[sdt], v6\n\t" \
  "v_pk_mul_f32 v[8:9], v[32:33], v[34:35] op_sel_hi:[1,0]\n\t" \
  "s_sub_u32 s77, %[sdt], 109051904\n\t" \
  "s_cmp_lg_u32 %[wk], 0\n\t" \
  "s_cbranch_scc1 1f\n\t" \
  "s_mov_b64 exec, 1\n\t" \
  "v_rcp_f32 v6, v6\n\t" \
  "s_add_u32 s96, %[sdt], 0xf0000000\n\t" \
  "v_mul_f32 v6, %[sd], v6\n\t" \
  "v_mul_f32 v15, 0x42800000, v6\n\t" \
  "v_and_b32 v6, 0x7fffffff, v15\n\t" \
  "v_cmp_lt_u32 vcc, 0x46800000, v6\n\t" \
  "s_mov_b64 exec, -1\n\t" \
  "s_cmp_ge_u32 s96, 0x60000000\n\t" \
  "s_cselect_b32 %[av], 0x101, %[av]\n\t" \
  "s_cmp_lg_u32 vcc_lo, 0\n\t" \
  "s_cselect_b32 %[av], 0x101, %[av]\n\t" \
  "1:\n\t" \
  "s_cmp_lg_u32 %[c], 0\n\t" \
  "s_cbranch_scc1 60f\n\t" \
  "s_lshl_b32 s98, %[wk], 18\n\t" \
  "s_lshl_b32 s99, %[b], 6\n\t" \
  "s_add_u32 s98, s98, s99\n\t" \
  "v_and_b32 v6, 3, v0\n\t" \
  "v_lshl_add_u32 v12, v6, 4, s98\n\t" \
  "v_lshrrev_b32 v7, 2, v2\n\t" \
  "v_lshl_add_u32 v12, v7, 14, v12\n\t" \
  "v_lshrrev_b32 v6, 3, v2\n\t" \
  "v_xor_b32 v14, v6, v2\n\t" \
  "v_and_or_b32 v7, v6, 4, %[wk]\n\t" \
  "v_and_b32 v10, 7, v0\n\t" \
  "v_xor_b32 v7, v7, v10\n\t" \
  "v_lshl_or_b32 v7, v10, 3, v7\n\t" \
  "v_and_b32 v6, 3, v6\n\t" \
  "v_mul_u32_u24 v6, 576, v6\n\t" \
  "v_add_lshl_u32 v13, v6, v7, 4\n\t" \
  "s_mul_i32 s98, %[wk], 9216\n\t" \
  "s_add_u32 s98, s98, 2816\n\t" \
  "v_lshl_add_u32 v14, v14, 4, s98\n\t" \
  "v_add_u32 v13, 2816, v13\n\t" \
  "s_mov_b32 s96, 0x3f803f80\n\t" \
  "v_mov_b32 v63, %[tag]\n\t" \
  "v_mov_b32 v67, %[tag]\n\t" \
  "v_mov_b32 v71, %[tag]\n\t" \
  "v_add_u32 v85, 64*16384, v12\n\t" \
  "v_add_u32 v86, 128*16384, v12\n\t" \
  "s_cmp_lt_u32 %[b], 192\n\t" \
  "s_cbranch_scc1 1f\n\t" \
  "s_waitcnt vmcnt(0)\n\t" \
  "1:\n\t" \
  "s_waitcnt vmcnt(6)\n\t" \
  TSQ(1) \
  "v_dot2_f32_bf16 v72, v16, s96, 0\n\t" \
  "v_dot2_f32_bf16 v73, v16, v16, 0\n\t" \
  "v_dot2_f32_bf16 v72, v17, s96, v72\n\t" \
  "v_dot2_f32_bf16 v73, v17, v17, v73\n\t" \
  "v_dot2_f32_bf16 v72, v18, s96, v72\n\t" \
  "v_dot2_f32_bf16 v73, v18, v18, v73\n\t" \
  "v_dot2_f32_bf16 v72, v19, s96, v72\n\t" \
  "v_dot2_f32_bf16 v73, v19, v19, v73\n\t" \
  "v_dot2_f32_bf16 v72, v20, s96, v72\n\t" \
  "v_dot2_f32_bf16 v73, v20, v20, v73\n\t" \
  "v_dot2_f32_bf16 v72, v21, s96, v72\n\t" \
  "v_dot2_f32_bf16 v73, v21, v21, v73\n\t" \
  "v_dot2_f32_bf16 v72, v22, s96, v72\n\t" \
  "v_dot2_f32_bf16 v73, v22, v22, v73\n\t" \
  "v_dot2_f32_bf16 v72, v23, s96, v72\n\t" \
  "v_dot2_f32_bf16 v73, v23, v23, v73\n\t" \
  "v_dot2_f32_bf16 v72, v24, s96, v72\n\t" \
  "v_dot2_f32_bf16 v73, v24, v24, v73\n\t" \
  "v_lshlrev_b32 v40, 16, v16\n\t" \
  "v_and_b32 v41, 0xffff0000, v16\n\t" \
  "v_lshlrev_b32 v46, 16, v19\n\t" \
  "v_permlane16_swap_b32 v72, v73\n\t" \
  "v_add_f32 v72, v72, v73\n\t" \
  "v_and_b32 v47, 0xffff0000, v19\n\t" \
  "v_permlane32_swap_b32 v40, v41\n\t" \
  "v_add_f32_dpp v72, v72, v72 quad_perm:[1,0,3,2] row_mask:0xf bank_mask:0xf bound_ctrl:1\n\t" \
  "v_lshlrev_b32 v52, 16, v22\n\t" \
  "v_and_b32 v53, 0xffff0000, v22\n\t" \
  "v_add_f32_dpp v72, v72, v72 quad_perm:[2,3,0,1] row_mask:0xf bank_mask:0xf bound_ctrl:1\n\t" \
  "v_permlane32_swap_b32 v46, v47\n\t" \
  "v_lshlrev_b32 v42, 16, v17\n\t" \
  "v_add_f32_dpp v72, v72, v72 row_half_mirror row_mask:0xf bank_mask:0xf bound_ctrl:1\n\t" \
  "v_and_b32 v43, 0xffff0000, v17\n\t" \
  "v_permlane32_swap_b32 v52, v53\n\t" \
  "v_add_f32_dpp v72, v72, v72 row_mirror row_mask:0xf bank_mask:0xf bound_ctrl:1\n\t" \
  "v_mov_b32 v73, v72\n\t" \
  "v_lshlrev_b32 v48, 16, v20\n\t" \
  "v_and_b32 v49, 0xffff0000, v20\n\t" \
  "v_permlane16_swap_b32 v72, v73\n\t" \
  "v_permlane32_swap_b32 v42, v43\n\t" \
  "v_mul_f32 v74, 0x3ae38e39, v72\n\t" \
  "v_lshlrev_b32 v54, 16, v23\n\t" \
  "v_mul_f32 v75, v74, -v74\n\t" \
  "v_and_b32 v55, 0xffff0000, v23\n\t" \
  "v_fmamk_f32 v76, v73, 0x3ae38e39, v75\n\t" \
  "v_permlane32_swap_b32 v48, v49\n\t" \
  "v_fmamk_f32 v77, v76, 0x42800000, v75\n\t" \
  "v_cvt_pk_bf16_f32 v76, 0, v76\n\t" \
  "v_cmp_nle_f32 vcc, 0, v77\n\t" \
  "v_add_f32 v76, 0x3727c5ac, v76\n\t" \
  "v_cvt_pk_bf16_f32 v78, 0, v74\n\t" \
  "v_cvt_pk_bf16_f32 v76, 0, v76\n\t" \
  "v_lshlrev_b32 v44, 16, v18\n\t" \
  "v_sqrt_f32 v76, v76\n\t" \
  "v_and_b32 v45, 0xffff0000, v18\n\t" \
  "v_permlane32_swap_b32 v54, v55\n\t" \
  "v_cvt_pk_bf16_f32 v76, 0, v76\n\t" \
  "v_lshlrev_b32 v50, 16, v21\n\t" \
  "v_rcp_f32 v76, v76\n\t" \
  "v_and_b32 v51, 0xffff0000, v21\n\t" \
  "v_permlane32_swap_b32 v44, v45\n\t" \
  "v_lshlrev_b32 v56, 16, v24\n\t" \
  "v_and_b32 v57, 0xffff0000, v24\n\t" \
  "v_permlane32_swap_b32 v50, v51\n\t" \
  "s_cbranch_vccnz 5f\n\t" \
  "6:\n\t" \
  "v_mul_f32 v80, 0x43000000, v76\n\t" \
  "v_mul_f32 v81, 0x43000000, v76\n\t" \
  "v_mul_f32 v82, v78, -v80\n\t" \
  "v_mul_f32 v83, v78, -v80\n\t" \
  "v_permlane32_swap_b32 v80, v81\n\t" \
  "v_permlane32_swap_b32 v56, v57\n\t" \
  "v_permlane32_swap_b32 v82, v83\n\t" \
  "v_pk_fma_f32 v[40:41], v[40:41], v[80:81], v[82:83]\n\t" \
  "v_pk_fma_f32 v[46:47], v[46:47], v[80:81], v[82:83]\n\t" \
  "v_pk_fma_f32 v[52:53], v[52:53], v[80:81], v[82:83]\n\t" \
  "v_cvt_pk_f16_f32 v60, v40, v41\n\t" \
  "v_cvt_pk_f16_f32 v61, v46, v47\n\t" \
  "v_cvt_pk_f16_f32 v62, v52, v53\n\t" \
  "v_pk_fma_f32 v[42:43], v[42:43], v[80:81], v[82:83]\n\t" \
  "v_pk_fma_f32 v[48:49], v[48:49], v[80:81], v[82:83]\n\t" \
  "v_pk_fma_f32 v[54:55], v[54:55], v[80:81], v[82:83]\n\t" \
  "ds_write_b128 v13, v[60:63] offset:0\n\t" \
  "v_cvt_pk_f16_f32 v64, v42, v43\n\t" \
  "v_cvt_pk_f16_f32 v65, v48, v49\n\t" \
  "v_cvt_pk_f16_f32 v66, v54, v55\n\t" \
  "v_pk_fma_f32 v[44:45], v[44:45], v[80:81], v[82:83]\n\t" \
  "v_pk_fma_f32 v[50:51], v[50:51], v[80:81], v[82:83]\n\t" \
  "v_pk_fma_f32 v[56:57], v[56:57], v[80:81], v[82:83]\n\t" \
  "ds_write_b128 v13, v[64:67] offset:1024\n\t" \
  "v_cvt_pk_f16_f32 v68, v44, v45\n\t" \
  "v_cvt_pk_f16_f32 v69, v50, v51\n\t" \
  "v_cvt_pk_f16_f32 v70, v56, v57\n\t" \
  "ds_write_b128 v13, v[68:71] offset:2048\n\t" \
  "s_lshl_b64 exec, 1, %[wk]\n\t" \
  "ds_write_b32 v25, v63\n\t" \
  "s_mov_b64 exec, -1\n\t" \
  "s_setprio 3\n\t" \
  "2:\n\t" \
  "ds_read_b32 v84, v25\n\t" \
  "ds_read_b128 v[40:43], v14 offset:0\n\t" \
  "ds_read_b128 v[44:47], v14 offset:1024\n\t" \
  "ds_read_b128 v[48:51], v14 offset:2048\n\t" \
  "s_waitcnt lgkmcnt(3)\n\t" \
  "v_cmp_ne_u32 vcc, %[tag], v84\n\t" \
  "s_cbranch_vccnz 2b\n\t" \
  "s_waitcnt lgkmcnt(2)\n\t" \
  "global_store_dwordx4 v12, v[40:43], s[70:71] sc1\n\t" \
  "s_waitcnt lgkmcnt(1)\n\t" \
  "global_store_dwordx4 v85, v[44:47], s[70:71] sc1\n\t" \
  "s_waitcnt lgkmcnt(0)\n\t" \
  "global_store_dwordx4 v86, v[48:51], s[70:71] sc1\n\t" \
  "s_setprio " P0 "\n\t" \
  TSQ(3) \
  "s_cmp_lt_u32 %[b], 192\n\t" \
  "s_waitcnt vmcnt(3)\n\t" \
  "s_cbranch_scc1 80f\n\t" \
  "s_branch 90f\n\t" \
  "5:\n\t" \
  "v_mov_b32 v88, 0\n\t" \
  "v_mov_b32 v89, 0\n\t" \
  "v_lshlrev_b32 v90, 16, v16\n\t" \
  "v_and_b32 v91, 0xffff0000, v16\n\t" \
  "v_sub_f32 v90, v90, v78\n\t" \
  "v_sub_f32 v91, v91, v78\n\t" \
  "v_pk_fma_f32 v[88:89], v[90:91], v[90:91], v[88:89]\n\t" \
  "v_lshlrev_b32 v90, 16, v17\n\t" \
  "v_and_b32 v91, 0xffff0000, v17\n\t" \
  "v_sub_f32 v90, v90, v78\n\t" \
  "v_sub_f32 v91, v91, v78\n\t" \
  "v_pk_fma_f32 v[88:89], v[90:91], v[90:91], v[88:89]\n\t" \
  "v_lshlrev_b32 v90, 16, v18\n\t" \
  "v_and_b32 v91, 0xffff0000, v18\n\t" \
  "v_sub_f32 v90, v90, v78\n\t" \
  "v_sub_f32 v91, v91, v78\n\t" \
  "v_pk_fma_f32 v[88:89], v[90:91], v[90:91], v[88:89]\n\t" \
  "v_lshlrev_b32 v90, 16, v19\n\t" \
  "v_and_b32 v91, 0xffff0000, v19\n\t" \
  "v_sub_f32 v90, v90, v78\n\t" \
  "v_sub_f32 v91, v91, v78\n\t" \
  "v_pk_fma_f32 v[88:89], v[90:91], v[90:91], v[88:89]\n\t" \
  "v_lshlrev_b32 v90, 16, v20\n\t" \
  "v_and_b32 v91, 0xffff0000, v20\n\t" \
  "v_sub_f32 v90, v90, v78\n\t" \
  "v_sub_f32 v91, v91, v78\n\t" \
  "v_pk_fma_f32 v[88:89], v[90:91], v[90:91], v[88:89]\n\t" \
  "v_lshlrev_b32 v90, 16, v21\n\t" \
  "v_and_b32 v91, 0xffff0000, v21\n\t" \
  "v_sub_f32 v90, v90, v78\n\t" \
  "v_sub_f32 v91, v91, v78\n\t" \
  "v_pk_fma_f32 v[88:89], v[90:91], v[90:91], v[88:89]\n\t" \
  "v_lshlrev_b32 v90, 16, v22\n\t" \
  "v_and_b32 v91, 0xffff0000, v22\n\t" \
  "v_sub_f32 v90, v90, v78\n\t" \
  "v_sub_f32 v91, v91, v78\n\t" \
  "v_pk_fma_f32 v[88:89], v[90:91], v[90:91], v[88:89]\n\t" \
  "v_lshlrev_b32 v90, 16, v23\n\t" \
  "v_and_b32 v91, 0xffff0000, v23\n\t" \
  "v_sub_f32 v90, v90, v78\n\t" \
  "v_sub_f32 v91, v91, v78\n\t" \
  "v_pk_fma_f32 v[88:89], v[90:91], v[90:91], v[88:89]\n\t" \
  "v_lshlrev_b32 v90, 16, v24\n\t" \
  "v_and_b32 v91, 0xffff0000, v24\n\t" \
  "v_sub_f32 v90, v90, v78\n\t" \
  "v_sub_f32 v91, v91, v78\n\t" \
  "v_pk_fma_f32 v[88:89], v[90:91], v[90:91], v[88:89]\n\t" \
  "v_add_f32 v88, v88, v89\n\t" \
  "s_nop 1\n\t" \
  "v_add_f32_dpp v88, v88, v88 quad_perm:[1,0,3,2] row_mask:0xf bank_mask:0xf bound_ctrl:1\n\t" \
  "s_nop 1\n\t" \
  "v_add_f32_dpp v88, v88, v88 quad_perm:[2,3,0,1] row_mask:0xf bank_mask:0xf bound_ctrl:1\n\t" \
  "s_nop 1\n\t" \
  "v_add_f32_dpp v88, v88, v88 row_half_mirror row_mask:0xf bank_mask:0xf bound_ctrl:1\n\t" \
  "s_nop 1\n\t" \
  "v_add_f32_dpp v88, v88, v88 row_mirror row_mask:0xf bank_mask:0xf bound_ctrl:1\n\t" \
  "v_mov_b32 v89, v88\n\t" \
  "s_nop 1\n\t" \
  "v_permlane16_swap_b32 v88, v89\n\t" \
  "v_add_f32 v88, v88, v89\n\t" \
  "v_sub_f32 v90, v74, v78\n\t" \
  "v_mul_f32 v90, v90, -v90\n\t" \
  "v_fmamk_f32 v76, v88, 0x3ae38e39, v90\n\t" \
  "v_cmp_ngt_f32 vcc, 0, v76\n\t" \
  "s_nop 1\n\t" \
  "v_cndmask_b32 v76, 0, v76, vcc\n\t" \
  "v_cvt_pk_bf16_f32 v76, 0, v76\n\t" \
  "v_add_f32 v76, 0x3727c5ac, v76\n\t" \
  "v_cvt_pk_bf16_f32 v76, 0, v76\n\t" \
  "v_sqrt_f32 v76, v76\n\t" \
  "s_nop 0\n\t" \
  "v_cvt_pk_bf16_f32 v76, 0, v76\n\t" \
  "v_rcp_f32 v76, v76\n\t" \
  "s_branch 6b\n\t" \
  "50:\n\t" \
  "s_cmp_gt_u32 %[b], 191\n\t" \
  "s_cbranch_scc1 51f\n\t" \
  "s_setprio " PC2 "\n\t" \
  "s_mul_i32 s99, %[wk], 576\n\t" \
  "v_lshrrev_b32 v6, 4, v2\n\t" \
  "v_and_b32 v7, 15, v0\n\t" \
  "v_lshl_or_b32 v7, v7, 2, %[wk]\n\t" \
  "v_min_u32 v7, 31, v7\n\t" \
  "v_lshlrev_b32 v8, 1, v6\n\t" \
  "v_sub_u32 v6, 22, v6\n\t" \
  "s_mov_b64 exec, 0xffff\n\t" \
  "v_mov_b32 v6, 26\n\t" \
  "s_mov_b64 exec, -1\n\t" \
  "v_lshlrev_b32 v8, v8, 8\n\t" \
  "v_add_lshl_u32 v8, v8, -1, v6\n\t" \
  "v_lshrrev_b32 v8, v7, v8\n\t" \
  "v_and_b32 v6, 3, v8\n\t" \
  "v_bfe_u32 v7, v8, 2, 2\n\t" \
  "v_mul_u32_u24 v6, 0x8001, v6\n\t" \
  "v_mul_u32_u24 v7, 0x8001, v7\n\t" \
  "v_and_b32 v6, 0x10001, v6\n\t" \
  "v_and_b32 v7, 0x10001, v7\n\t" \
  "v_mul_u32_u24 v6, 0x3c00, v6\n\t" \
  "v_mul_u32_u24 v7, 0x3c00, v7\n\t" \
  "v_lshl_add_u32 v8, v2, 3, s99\n\t" \
  "ds_write_b64 v8, v[6:7] offset:512\n\t" \
  "s_cmp_lg_u32 %[wk], 0\n\t" \
  "s_cbranch_scc1 1f\n\t" \
  "ds_write_b64 v8, v[40:41]\n\t" \
  "1:\n\t" \
  "s_cmp_lg_u32 %[wk], 2\n\t" \
  "s_cbranch_scc1 3f\n\t" \
  "v_and_b32 v6, 48, v0\n\t" \
  "v_and_b32 v7, 15, v0\n\t" \
  "v_lshl_add_u32 v36, v7, 5, v6\n\t" \
  "v_add_u32 v36, 2112, v36\n\t" \
  "v_and_b32 v7, 12, v0\n\t" \
  "v_lshlrev_b32 v7, 1, v7\n\t" \
  "v_sub_u32 v7, v6, v7\n\t" \
  "v_bitop3_b32 v8, v0, 3, v0 bitop3:0xc\n\t" \
  "s_movk_i32 s98, -2112\n\t" \
  "v_mad_i32_i24 v37, v8, s98, v7\n\t" \
  "v_lshrrev_b32 v6, 1, v6\n\t" \
  "v_sub_u32 v38, v37, v6\n\t" \
  "s_movk_i32 s98, 576\n\t" \
  "v_mad_u32_u24 v39, v8, s98, v7\n\t" \
  "v_add_u32 v39, 536, v39\n\t" \
  "ds_write_b128 v5, v[36:39] offset:3072\n\t" \
  "3:\n\t" \
  "s_waitcnt lgkmcnt(0)\n\t" \
  "global_load_short_d16_hi v26, v3, s[38:39]" PNT "\n\t" \
  "global_load_short_d16_hi v27, v3, s[40:41]" PNT "\n\t" \
  "s_mov_b32 exec_lo, 0\n\t" \
  "global_load_short_d16_hi v28, v3, s[42:43]" PNT "\n\t" \
  "global_load_short_d16_hi v29, v3, s[44:45]" PNT "\n\t" \
  "global_load_short_d16_hi v30, v3, s[46:47]" PNT "\n\t" \
  "global_load_short_d16_hi v31, v3, s[48:49]" PNT "\n\t" \
  "s_mov_b32 exec_lo, -1\n\t" \
  "2:\n\t" \
  "s_load_dword %[sdt], s[50:51], %[av]\n\t" \
  "s_load_dword %[sd], s[52:53], %[av]\n\t" \
  "s_load_dword %[ep], s[54:55], 0x0\n\t" \
  "s_and_b32 s98, %[wk], 1\n\t" \
  "s_mulk_i32 s98, 4608\n\t" \
  "s_lshl_b32 s99, %[wk], 4\n\t" \
  "s_and_b32 s99, s99, 32\n\t" \
  "v_and_b32 v6, 15, v0\n\t" \
  "v_and_or_b32 v7, v0, 48, s98\n\t" \
  "s_movk_i32 s98, 288\n\t" \
  "v_mad_u32_u24 v36, v6, s98, v7\n\t" \
  "v_and_b32 v6, 47, v0\n\t" \
  "v_lshl_or_b32 v6, v6, 1, s99\n\t" \
  "v_bfe_u32 v7, v0, 4, 1\n\t" \
  "v_or_b32 v7, v7, v6\n\t" \
  "s_bitcmp1_b32 %[wk], 0\n\t" \
  "s_cbranch_scc1 5f\n\t" \
  "v_lshlrev_b32 v7, 3, v7\n\t" \
  "v_sub_u32 v37, -5198, v7\n\t" \
  "v_mov_b32 v38, v37\n\t" \
  "s_mov_b32 exec_lo, 0\n\t" \
  "v_add_u32 v38, 8, v37\n\t" \
  "s_cmp_lg_u32 %[wk], 0\n\t" \
  "s_cbranch_scc1 6f\n\t" \
  "s_mov_b32 exec_hi, 1\n\t" \
  "v_mov_b32 v38, -4270\n\t" \
  "s_branch 6f\n\t" \
  "5:\n\t" \
  "v_sub_u32 v7, 192, v7\n\t" \
  "v_and_b32 v8, 3, v7\n\t" \
  "v_lshrrev_b32 v7, 2, v7\n\t" \
  "v_mul_u32_u24 v8, 48, v8\n\t" \
  "v_add_lshl_u32 v37, v7, v8, 1\n\t" \
  "v_add_u32 v37, -5158, v37\n\t" \
  "v_lshrrev_b32 v6, 1, v6\n\t" \
  "v_and_b32 v8, 1, v0\n\t" \
  "v_mul_u32_u24 v8, 159, v8\n\t" \
  "v_sub_u32 v8, v8, v6\n\t" \
  "v_add_u32 v39, -4678, v8\n\t" \
  "v_add_u32 v38, -4614, v8\n\t" \
  "s_mov_b32 exec_lo, 0xffff0000\n\t" \
  "s_mov_b32 exec_hi, 0xffff0000\n\t" \
  "v_mov_b32 v38, -4270\n\t" \
  "v_mov_b32 v39, -4270\n\t" \
  "s_cmp_lg_u32 %[wk], 1\n\t" \
  "s_cbranch_scc1 6f\n\t" \
  "s_mov_b64 exec, 1\n\t" \
  "v_mov_b32 v37, -4270\n\t" \
  "v_mov_b32 v38, -4270\n\t" \
  "s_mov_b64 exec, -1\n\t" \
  "v_and_b32 v6, 3, v0\n\t" \
  "v_lshl_add_u32 v7, v6, 1, 54\n\t" \
  "s_mov_b32 exec_lo, 0x0f0f0f0f\n\t" \
  "v_add_u32 v7, 68, v7\n\t" \
  "s_mov_b32 exec_lo, 0x40404040\n\t" \
  "v_mov_b32 v7, 1216\n\t" \
  "s_mov_b32 exec_lo, 0x80808080\n\t" \
  "v_mov_b32 v7, 1600\n\t" \
  "s_mov_b32 exec_lo, -1\n\t" \
  "v_lshrrev_b32 v6, 3, v2\n\t" \
  "s_movk_i32 s98, -2114\n\t" \
  "v_mad_i32_i24 v7, v6, s98, v7\n\t" \
  "ds_write_b32 v5, v7 offset:3072\n\t" \
  "6:\n\t" \
  "s_mov_b64 exec, -1\n\t" \
  "ds_write_b128 v5, v[36:39]\n\t" \
  "s_branch 8b\n\t" \
  "51:\n\t" \
  "ds_write_b128 v40, v[40:43] offset:112\n\t" \
  "v_and_b32 v6, 12, v0\n\t" \
  "v_mad_u32_u24 v7, v6, 31, v1\n\t" \
  "s_lshl_b32 s98, %[b], 6\n\t" \
  "s_add_u32 s98, s98, 12587008\n\t" \
  "v_lshl_add_u32 v7, v7, 4, s98\n\t" \
  "s_lshl_b32 s99, %[w], 8\n\t" \
  "s_add_u32 s99, s99, %[b]\n\t" \
  "s_sub_u32 s99, s99, 704\n\t" \
  "s_waitcnt lgkmcnt(0)\n\t" \
  "s_and_b32 s70, %[id], %[rot]\n\t" \
  "s_lshl_b32 s70, s70, 24\n\t" \
  "s_add_u32 s70, s70, " STR(WSOFS) "\n\t" \
  "s_add_u32 s70, s70, s54\n\t" \
  "s_addc_u32 s71, s55, 0\n\t" \
  "s_mul_i32 %[opc], %[c], 36864\n\t" \
  "s_add_u32 %[opc], %[opc], 2816\n\t" \
  "s_mul_i32 %[tag], %[id], 0x5bd1e996\n\t" \
  "v_lshlrev_b32 v11, 2, %[c]\n\t" \
  "s_add_u32 s98, s68, %[tag]\n\t" \
  "v_mov_b32 v8, s98\n\t" \
  "s_cmp_lt_u32 s99, 24\n\t" \
  "s_cbranch_scc0 2f\n\t" \
  "s_mov_b64 exec, 0xfff\n\t" \
  "global_store_dwordx4 v7, v[8:11], s[70:71] sc1\n\t" \
  "s_mov_b64 exec, -1\n\t" \
  "2:\n\t" \
  "s_branch 90f\n\t" \
  "65:\n\t" \
  "s_lshl_b32 s98, %[w], 2\n\t" \
  "v_and_b32 v166, 63, v0\n\t" \
  "s_mov_b32 s72, " STR(TSLOT_LO) "\n\t" \
  "s_mov_b32 s73, " STR(TSLOT_HI) "\n\t" \
  "s_lshr_b64 s[72:73], s[72:73], s98\n\t" \
  "s_and_b32 s99, s72, 15\n\t" \
  "s_lshr_b32 s98, %[b], 3\n\t" \
  "s_and_b32 s97, %[b], 7\n\t" \
  "s_cmp_lt_u32 s99, 10\n\t" \
  "s_cbranch_scc1 1f\n\t" \
  "s_sub_u32 s96, s98, 8\n\t" \
  "s_cbranch_scc0 2f\n\t" \
  "s_min_u32 s98, s98, 2\n\t" \
  "s_add_u32 s98, s98, 24\n\t" \
  "1:\n\t" \
  "s_and_b32 s96, s98, 7\n\t" \
  "s_lshr_b32 s95, s98, 3\n\t" \
  "s_mul_i32 s94, s99, 3\n\t" \
  "s_add_u32 s95, s95, s94\n\t" \
  "s_lshl_b32 s94, s95, 1\n\t" \
  "s_and_b32 s94, s94, 28\n\t" \
  "s_lshr_b32 s94, " STR(PORD) ", s94\n\t" \
  "s_and_b32 s94, s94, 15\n\t" \
  "s_and_b32 s95, s95, 1\n\t" \
  "s_lshl1_add_u32 s95, s94, s95\n\t" \
  "s_cmp_lt_u32 s99, 9\n\t" \
  "s_cselect_b32 s96, s96, 8\n\t" \
  "s_cselect_b32 s99, s95, s98\n\t" \
  "s_mul_i32 s97, s97, 9\n\t" \
  "s_add_u32 s96, s96, s97\n\t" \
  "s_lshr_b32 s98, s99, 1\n\t" \
  "s_mul_i32 s97, s98, 11\n\t" \
  "s_lshr_b32 s97, s97, 5\n\t" \
  "s_lshr_b32 s95, 0x2924, s98\n\t" \
  "s_bitcmp1_b32 s95, 0\n\t" \
  "s_cbranch_scc1 3f\n\t" \
  "s_sub_u32 s98, s98, s97\n\t" \
  "s_bitcmp1_b32 s99, 0\n\t" \
  "s_cselect_b64 s[72:73], s[58:59], s[60:61]\n\t" \
  "s_branch 5f\n\t" \
  "2:\n\t" \
  "s_bitcmp1_b32 s96, 0\n\t" \
  "s_cselect_b64 s[72:73], s[58:59], s[60:61]\n\t" \
  "s_lshr_b32 s98, s96, 1\n\t" \
  "s_lshr_b32 s96, s72, 7\n\t" \
  "s_add_u32 s96, s96, s97\n\t" \
  "s_sub_u32 s96, 3, s96\n\t" \
  "s_and_b32 s96, s96, 7\n\t" \
  "s_mul_i32 s97, s97, 9\n\t" \
  "s_add_u32 s96, s96, s97\n\t" \
  "5:\n\t" \
  "s_lshl_b32 s96, s96, 7\n\t" \
  "s_cmp_eq_u32 s98, 8\n\t" \
  "s_cselect_b32 s98, 64, s98\n\t" \
  "s_cselect_b32 s97, 0, 56\n\t" \
  "s_mul_i32 s98, s98, 73728\n\t" \
  "v_lshrrev_b32 v165, 3, v166\n\t" \
  "v_mad_u32_u24 v165, v165, s97, v166\n\t" \
  "v_mul_u32_u24 v165, 9216, v165\n\t" \
  "s_branch 6f\n\t" \
  "3:\n\t" \
  "s_mov_b64 s[72:73], s[62:63]\n\t" \
  "s_and_b32 s98, s99, 1\n\t" \
  "s_lshl1_add_u32 s98, s97, s98\n\t" \
  "s_lshl_b32 s98, s98, 7\n\t" \
  "s_mul_i32 s96, s96, 73728\n\t" \
  "v_mul_u32_u24 v165, 1152, v166\n\t" \
  "6:\n\t" \
  "s_add_u32 s98, s98, s96\n\t" \
  "s_add_u32 s72, s72, s98\n\t" \
  "s_addc_u32 s73, s73, 0\n\t" \
  "s_setpc_b64 s[90:91]\n\t" \
  "60:\n\t" \
  "s_setprio " P1 "\n\t" \
  "s_cmp_eq_u32 %[c], 1\n\t" \
  "s_cbranch_scc1 2f\n\t" \
  "v_mov_b32 v6, %[tag]\n\t" \
  "s_lshl_b64 exec, 1, %[wk]\n\t" \
  "ds_write_b32 v25, v6 offset:16\n\t" \
  "s_mov_b64 exec, -1\n\t" \
  "s_setprio " P2 "\n\t" \
  "s_cmp_eq_u32 s74, 0\n\t" \
  "s_cbranch_scc1 1f\n\t" \
  "2:\n\t" \
  "s_waitcnt vmcnt(0)\n\t" \
  "1:\n\t" \
  "s_waitcnt vmcnt(1)\n\t"

#define LN2_ASM \
  "s_mov_b32 s96, 0x3f803f80\n\t" \
  "s_mov_b32 s97, 0x40000\n\t" \
  "ds_read_b128 v[44:47], %5 offset:0\n\t" \
  "ds_read_b128 v[48:51], %5 offset:6144\n\t" \
  "ds_read_b128 v[52:55], %5 offset:2048\n\t" \
  "ds_read_b128 v[56:59], %5 offset:8192\n\t" \
  "ds_read_b128 v[60:63], %5 offset:4096\n\t" \
  "ds_read_b128 v[64:67], %5 offset:10240\n\t" \
  "v_mov_b32 v107, 1\n\t" \
  "v_mov_b32 v108, 0\n\t" \
  "1:\n\t" \
  "s_waitcnt lgkmcnt(5)\n\t" \
  "v_perm_b32 v68, v108, v44, %14\n\t" \
  "s_waitcnt lgkmcnt(4)\n\t" \
  "v_perm_b32 v69, v108, v48, %14\n\t" \
  "v_perm_b32 v74, v108, v45, %14\n\t" \
  "v_perm_b32 v75, v108, v49, %14\n\t" \
  "v_perm_b32 v80, v108, v46, %14\n\t" \
  "v_perm_b32 v81, v108, v50, %14\n\t" \
  "v_xor_b32 v102, %15, v47\n\t" \
  "v_bitop3_b32 v102, v102, v51, %15 bitop3:0xf6\n\t" \
  "s_waitcnt lgkmcnt(3)\n\t" \
  "v_bitop3_b32 v102, v102, v55, %15 bitop3:0xf6\n\t" \
  "s_waitcnt lgkmcnt(2)\n\t" \
  "v_bitop3_b32 v102, v102, v59, %15 bitop3:0xf6\n\t" \
  "s_waitcnt lgkmcnt(1)\n\t" \
  "v_bitop3_b32 v102, v102, v63, %15 bitop3:0xf6\n\t" \
  "s_waitcnt lgkmcnt(0)\n\t" \
  "v_bitop3_b32 v102, v102, v67, %15 bitop3:0xf6\n\t" \
  "v_cmp_ne_u32 vcc, 0, v102\n\t" \
  "v_pk_add_f32 v[68:69], v[68:69], v[16:17]\n\t" \
  "v_pk_add_f32 v[74:75], v[74:75], v[22:23]\n\t" \
  "v_pk_add_f32 v[80:81], v[80:81], v[28:29]\n\t" \
  "v_cvt_pk_bf16_f32 v86, v68, v69\n\t" \
  "v_cvt_pk_bf16_f32 v89, v74, v75\n\t" \
  "v_cvt_pk_bf16_f32 v92, v80, v81\n\t" \
  "s_cbranch_vccnz 5f\n\t" \
  "2:\n\t" \
  "v_dot2_f32_bf16 v96, v86, s96, 0\n\t" \
  "v_dot2_f32_bf16 v97, v86, v86, 0\n\t" \
  "v_dot2_f32_bf16 v98, v86, v34, 0\n\t" \
  "v_dot2_f32_bf16 v96, v89, s96, v96\n\t" \
  "v_dot2_f32_bf16 v97, v89, v89, v97\n\t" \
  "v_dot2_f32_bf16 v98, v89, v37, v98\n\t" \
  "v_dot2_f32_bf16 v96, v92, s96, v96\n\t" \
  "v_dot2_f32_bf16 v97, v92, v92, v97\n\t" \
  "v_dot2_f32_bf16 v98, v92, v40, v98\n\t" \
  "v_perm_b32 v70, v108, v52, %14\n\t" \
  "v_perm_b32 v71, v108, v56, %14\n\t" \
  "v_perm_b32 v76, v108, v53, %14\n\t" \
  "v_perm_b32 v77, v108, v57, %14\n\t" \
  "v_perm_b32 v82, v108, v54, %14\n\t" \
  "v_perm_b32 v83, v108, v58, %14\n\t" \
  "v_pk_add_f32 v[70:71], v[70:71], v[18:19]\n\t" \
  "v_pk_add_f32 v[76:77], v[76:77], v[24:25]\n\t" \
  "v_pk_add_f32 v[82:83], v[82:83], v[30:31]\n\t" \
  "v_cvt_pk_bf16_f32 v87, v70, v71\n\t" \
  "v_cvt_pk_bf16_f32 v90, v76, v77\n\t" \
  "v_cvt_pk_bf16_f32 v93, v82, v83\n\t" \
  "v_dot2_f32_bf16 v96, v87, s96, v96\n\t" \
  "v_dot2_f32_bf16 v97, v87, v87, v97\n\t" \
  "v_dot2_f32_bf16 v98, v87, v35, v98\n\t" \
  "v_dot2_f32_bf16 v96, v90, s96, v96\n\t" \
  "v_dot2_f32_bf16 v97, v90, v90, v97\n\t" \
  "v_dot2_f32_bf16 v98, v90, v38, v98\n\t" \
  "v_dot2_f32_bf16 v96, v93, s96, v96\n\t" \
  "v_dot2_f32_bf16 v97, v93, v93, v97\n\t" \
  "v_dot2_f32_bf16 v98, v93, v41, v98\n\t" \
  "v_perm_b32 v72, v108, v60, %14\n\t" \
  "v_perm_b32 v73, v108, v64, %14\n\t" \
  "v_perm_b32 v78, v108, v61, %14\n\t" \
  "v_perm_b32 v79, v108, v65, %14\n\t" \
  "v_perm_b32 v84, v108, v62, %14\n\t" \
  "v_perm_b32 v85, v108, v66, %14\n\t" \
  "v_pk_add_f32 v[72:73], v[72:73], v[20:21]\n\t" \
  "v_pk_add_f32 v[78:79], v[78:79], v[26:27]\n\t" \
  "v_pk_add_f32 v[84:85], v[84:85], v[32:33]\n\t" \
  "v_cvt_pk_bf16_f32 v88, v72, v73\n\t" \
  "v_cvt_pk_bf16_f32 v91, v78, v79\n\t" \
  "v_cvt_pk_bf16_f32 v94, v84, v85\n\t" \
  "v_dot2_f32_bf16 v96, v88, s96, v96\n\t" \
  "v_dot2_f32_bf16 v97, v88, v88, v97\n\t" \
  "v_dot2_f32_bf16 v98, v88, v36, v98\n\t" \
  "v_dot2_f32_bf16 v96, v91, s96, v96\n\t" \
  "v_dot2_f32_bf16 v97, v91, v91, v97\n\t" \
  "v_dot2_f32_bf16 v98, v91, v39, v98\n\t" \
  "v_dot2_f32_bf16 v96, v94, s96, v96\n\t" \
  "v_dot2_f32_bf16 v97, v94, v94, v97\n\t" \
  "v_dot2_f32_bf16 v98, v94, v42, v98\n\t" \
  "s_nop 0\n\t" \
  "s_nop 0\n\t" \
  "v_permlane16_swap_b32 v96, v97\n\t" \
  "v_add_f32 v96, v96, v97\n\t" \
  "v_add_f32_dpp v98, v98, v98 quad_perm:[1,0,3,2] row_mask:0xf bank_mask:0xf bound_ctrl:1\n\t" \
  "s_nop 0\n\t" \
  "v_add_f32_dpp v96, v96, v96 quad_perm:[1,0,3,2] row_mask:0xf bank_mask:0xf bound_ctrl:1\n\t" \
  "v_add_f32_dpp v98, v98, v98 quad_perm:[2,3,0,1] row_mask:0xf bank_mask:0xf bound_ctrl:1\n\t" \
  "s_nop 0\n\t" \
  "v_add_f32_dpp v96, v96, v96 quad_perm:[2,3,0,1] row_mask:0xf bank_mask:0xf bound_ctrl:1\n\t" \
  "v_add_f32_dpp v98, v98, v98 row_half_mirror row_mask:0xf bank_mask:0xf bound_ctrl:1\n\t" \
  "s_nop 0\n\t" \
  "v_add_f32_dpp v96, v96, v96 row_half_mirror row_mask:0xf bank_mask:0xf bound_ctrl:1\n\t" \
  "v_add_f32_dpp v98, v98, v98 row_mirror row_mask:0xf bank_mask:0xf bound_ctrl:1\n\t" \
  "s_nop 0\n\t" \
  "v_add_f32_dpp v96, v96, v96 row_mirror row_mask:0xf bank_mask:0xf bound_ctrl:1\n\t" \
  "v_mov_b32 v103, v98\n\t" \
  "v_mov_b32 v97, v96\n\t" \
  "s_nop 0\n\t" \
  "v_permlane16_swap_b32 v98, v103\n\t" \
  "v_permlane16_swap_b32 v96, v97\n\t" \
  "v_mul_f32 v99, 0x3ae38e39, v96\n\t" \
  "v_add_f32 v98, v98, v103\n\t" \
  "v_mul_f32 v100, v99, -v99\n\t" \
  "v_cvt_pk_bf16_f32 v104, 0, v99\n\t" \
  "v_fmamk_f32 v105, v97, 0x3ae38e39, v100\n\t" \
  "v_fma_f32 v98, -v104, %8, v98\n\t" \
  "v_fmamk_f32 v101, v105, 0x42800000, v100\n\t" \
  "v_cvt_pk_bf16_f32 v105, 0, v105\n\t" \
  "v_cmp_nle_f32 vcc, 0, v101\n\t" \
  "v_add_f32 v105, 0x3727c5ac, v105\n\t" \
  "v_cvt_pk_bf16_f32 v105, 0, v105\n\t" \
  "v_sqrt_f32 v105, v105\n\t" \
  "s_nop 0\n\t" \
  "v_cvt_pk_bf16_f32 v105, 0, v105\n\t" \
  "v_rcp_f32 v105, v105\n\t" \
  "s_cbranch_vccnz 4f\n\t" \
  "6:\n\t" \
  "v_mul_f32 v98, v98, v105\n\t" \
  "v_cvt_pk_bf16_f32 v98, 0, v98\n\t" \
  "v_ashrrev_i32 v103, 31, v98\n\t" \
  "s_brev_b32 s96, 1\n\t" \
  "v_bitop3_b32 v106, v98, v103, s96 bitop3:0x1e\n\t" \
  "ds_write_b16_d16_hi %9, v106\n\t" \
  "v_lshrrev_b32 v106, 16, v106\n\t" \
  "v_lshl_or_b32 v106, v106, 15, %11\n\t" \
  "ds_write_b32 %10, v106\n\t" \
  "s_mov_b64 exec, 1\n\t" \
  "ds_add_rtn_u32 v103, v108, v107 offset:48\n\t" \
  "s_mov_b64 exec, -1\n\t" \
  "ds_read_b128 v[44:47], %12\n\t" \
  "v_lshlrev_b32 v16, 16, v86\n\t" \
  "v_and_b32 v17, 0xffff0000, v86\n\t" \
  "v_lshlrev_b32 v18, 16, v87\n\t" \
  "v_and_b32 v19, 0xffff0000, v87\n\t" \
  "v_lshlrev_b32 v20, 16, v88\n\t" \
  "v_and_b32 v21, 0xffff0000, v88\n\t" \
  "v_lshlrev_b32 v22, 16, v89\n\t" \
  "v_and_b32 v23, 0xffff0000, v89\n\t" \
  "s_waitcnt lgkmcnt(0)\n\t" \
  "v_readfirstlane_b32 s96, v103\n\t" \
  "s_cmp_lg_u32 s96, %17\n\t" \
  "s_cbranch_scc1 7f\n\t" \
  "s_mov_b64 exec, %18\n\t" \
  "global_store_dwordx4 %13, v[44:47], %16 sc1\n\t" \
  "s_mov_b64 exec, -1\n\t" \
  "s_branch 7f\n\t" \
  "5:\n\t" \
  "s_sub_u32 s97, s97, 1\n\t" \
  "s_cbranch_scc1 2b\n\t" \
  "v_cmp_ne_u32 vcc, %15, v47\n\t" \
  "s_cbranch_vccz 3f\n\t" \
  "s_mov_b64 exec, vcc\n\t" \
  "global_load_dwordx4 v[44:47], %6, %16 offset:-2048 sc1\n\t" \
  "s_mov_b64 exec, -1\n\t" \
  "3:\n\t" \
  "v_cmp_ne_u32 vcc, %15, v51\n\t" \
  "s_cbranch_vccz 3f\n\t" \
  "s_mov_b64 exec, vcc\n\t" \
  "global_load_dwordx4 v[48:51], %7, %16 offset:-2048 sc1\n\t" \
  "s_mov_b64 exec, -1\n\t" \
  "3:\n\t" \
  "v_cmp_ne_u32 vcc, %15, v55\n\t" \
  "s_cbranch_vccz 3f\n\t" \
  "s_mov_b64 exec, vcc\n\t" \
  "global_load_dwordx4 v[52:55], %6, %16 offset:0 sc1\n\t" \
  "s_mov_b64 exec, -1\n\t" \
  "3:\n\t" \
  "v_cmp_ne_u32 vcc, %15, v59\n\t" \
  "s_cbranch_vccz 3f\n\t" \
  "s_mov_b64 exec, vcc\n\t" \
  "global_load_dwordx4 v[56:59], %7, %16 offset:0 sc1\n\t" \
  "s_mov_b64 exec, -1\n\t" \
  "3:\n\t" \
  "v_cmp_ne_u32 vcc, %15, v63\n\t" \
  "s_cbranch_vccz 3f\n\t" \
  "s_mov_b64 exec, vcc\n\t" \
  "global_load_dwordx4 v[60:63], %6, %16 offset:2048 sc1\n\t" \
  "s_mov_b64 exec, -1\n\t" \
  "3:\n\t" \
  "v_cmp_ne_u32 vcc, %15, v67\n\t" \
  "s_cbranch_vccz 3f\n\t" \
  "s_mov_b64 exec, vcc\n\t" \
  "global_load_dwordx4 v[64:67], %7, %16 offset:2048 sc1\n\t" \
  "s_mov_b64 exec, -1\n\t" \
  "3:\n\t" \
  "s_waitcnt vmcnt(0)\n\t" \
  "s_branch 1b\n\t" \
  "4:\n\t" \
  "v_mov_b32 v68, 0\n\t" \
  "v_mov_b32 v69, 0\n\t" \
  "v_mov_b32 v98, 0\n\t" \
  "v_lshlrev_b32 v70, 16, v86\n\t" \
  "v_and_b32 v71, 0xffff0000, v86\n\t" \
  "v_sub_f32 v70, v70, v104\n\t" \
  "v_sub_f32 v71, v71, v104\n\t" \
  "v_pk_fma_f32 v[68:69], v[70:71], v[70:71], v[68:69]\n\t" \
  "v_cvt_pk_bf16_f32 v70, v70, v71\n\t" \
  "v_dot2_f32_bf16 v98, v70, v34, v98\n\t" \
  "v_lshlrev_b32 v70, 16, v87\n\t" \
  "v_and_b32 v71, 0xffff0000, v87\n\t" \
  "v_sub_f32 v70, v70, v104\n\t" \
  "v_sub_f32 v71, v71, v104\n\t" \
  "v_pk_fma_f32 v[68:69], v[70:71], v[70:71], v[68:69]\n\t" \
  "v_cvt_pk_bf16_f32 v70, v70, v71\n\t" \
  "v_dot2_f32_bf16 v98, v70, v35, v98\n\t" \
  "v_lshlrev_b32 v70, 16, v88\n\t" \
  "v_and_b32 v71, 0xffff0000, v88\n\t" \
  "v_sub_f32 v70, v70, v104\n\t" \
  "v_sub_f32 v71, v71, v104\n\t" \
  "v_pk_fma_f32 v[68:69], v[70:71], v[70:71], v[68:69]\n\t" \
  "v_cvt_pk_bf16_f32 v70, v70, v71\n\t" \
  "v_dot2_f32_bf16 v98, v70, v36, v98\n\t" \
  "v_lshlrev_b32 v70, 16, v89\n\t" \
  "v_and_b32 v71, 0xffff0000, v89\n\t" \
  "v_sub_f32 v70, v70, v104\n\t" \
  "v_sub_f32 v71, v71, v104\n\t" \
  "v_pk_fma_f32 v[68:69], v[70:71], v[70:71], v[68:69]\n\t" \
  "v_cvt_pk_bf16_f32 v70, v70, v71\n\t" \
  "v_dot2_f32_bf16 v98, v70, v37, v98\n\t" \
  "v_lshlrev_b32 v70, 16, v90\n\t" \
  "v_and_b32 v71, 0xffff0000, v90\n\t" \
  "v_sub_f32 v70, v70, v104\n\t" \
  "v_sub_f32 v71, v71, v104\n\t" \
  "v_pk_fma_f32 v[68:69], v[70:71], v[70:71], v[68:69]\n\t" \
  "v_cvt_pk_bf16_f32 v70, v70, v71\n\t" \
  "v_dot2_f32_bf16 v98, v70, v38, v98\n\t" \
  "v_lshlrev_b32 v70, 16, v91\n\t" \
  "v_and_b32 v71, 0xffff0000, v91\n\t" \
  "v_sub_f32 v70, v70, v104\n\t" \
  "v_sub_f32 v71, v71, v104\n\t" \
  "v_pk_fma_f32 v[68:69], v[70:71], v[70:71], v[68:69]\n\t" \
  "v_cvt_pk_bf16_f32 v70, v70, v71\n\t" \
  "v_dot2_f32_bf16 v98, v70, v39, v98\n\t" \
  "v_lshlrev_b32 v70, 16, v92\n\t" \
  "v_and_b32 v71, 0xffff0000, v92\n\t" \
  "v_sub_f32 v70, v70, v104\n\t" \
  "v_sub_f32 v71, v71, v104\n\t" \
  "v_pk_fma_f32 v[68:69], v[70:71], v[70:71], v[68:69]\n\t" \
  "v_cvt_pk_bf16_f32 v70, v70, v71\n\t" \
  "v_dot2_f32_bf16 v98, v70, v40, v98\n\t" \
  "v_lshlrev_b32 v70, 16, v93\n\t" \
  "v_and_b32 v71, 0xffff0000, v93\n\t" \
  "v_sub_f32 v70, v70, v104\n\t" \
  "v_sub_f32 v71, v71, v104\n\t" \
  "v_pk_fma_f32 v[68:69], v[70:71], v[70:71], v[68:69]\n\t" \
  "v_cvt_pk_bf16_f32 v70, v70, v71\n\t" \
  "v_dot2_f32_bf16 v98, v70, v41, v98\n\t" \
  "v_lshlrev_b32 v70, 16, v94\n\t" \
  "v_and_b32 v71, 0xffff0000, v94\n\t" \
  "v_sub_f32 v70, v70, v104\n\t" \
  "v_sub_f32 v71, v71, v104\n\t" \
  "v_pk_fma_f32 v[68:69], v[70:71], v[70:71], v[68:69]\n\t" \
  "v_cvt_pk_bf16_f32 v70, v70, v71\n\t" \
  "v_dot2_f32_bf16 v98, v70, v42, v98\n\t" \
  "v_add_f32 v68, v68, v69\n\t" \
  "s_nop 1\n\t" \
  "v_add_f32_dpp v68, v68, v68 quad_perm:[1,0,3,2] row_mask:0xf bank_mask:0xf bound_ctrl:1\n\t" \
  "s_nop 1\n\t" \
  "v_add_f32_dpp v68, v68, v68 quad_perm:[2,3,0,1] row_mask:0xf bank_mask:0xf bound_ctrl:1\n\t" \
  "s_nop 1\n\t" \
  "v_add_f32_dpp v68, v68, v68 row_half_mirror row_mask:0xf bank_mask:0xf bound_ctrl:1\n\t" \
  "s_nop 1\n\t" \
  "v_add_f32_dpp v68, v68, v68 row_mirror row_mask:0xf bank_mask:0xf bound_ctrl:1\n\t" \
  "v_mov_b32 v103, v68\n\t" \
  "s_nop 1\n\t" \
  "v_permlane16_swap_b32 v68, v103\n\t" \
  "v_add_f32 v68, v68, v103\n\t" \
  "v_add_f32_dpp v98, v98, v98 quad_perm:[1,0,3,2] row_mask:0xf bank_mask:0xf bound_ctrl:1\n\t" \
  "s_nop 1\n\t" \
  "v_add_f32_dpp v98, v98, v98 quad_perm:[2,3,0,1] row_mask:0xf bank_mask:0xf bound_ctrl:1\n\t" \
  "s_nop 1\n\t" \
  "v_add_f32_dpp v98, v98, v98 row_half_mirror row_mask:0xf bank_mask:0xf bound_ctrl:1\n\t" \
  "s_nop 1\n\t" \
  "v_add_f32_dpp v98, v98, v98 row_mirror row_mask:0xf bank_mask:0xf bound_ctrl:1\n\t" \
  "v_mov_b32 v103, v98\n\t" \
  "s_nop 1\n\t" \
  "v_permlane16_swap_b32 v98, v103\n\t" \
  "v_add_f32 v98, v98, v103\n\t" \
  "v_sub_f32 v70, v99, v104\n\t" \
  "v_mul_f32 v70, v70, -v70\n\t" \
  "v_fmamk_f32 v105, v68, 0x3ae38e39, v70\n\t" \
  "v_cmp_gt_f32 vcc, 0, v105\n\t" \
  "s_nop 1\n\t" \
  "v_cndmask_b32 v105, v105, v108, vcc\n\t" \
  "v_cvt_pk_bf16_f32 v105, 0, v105\n\t" \
  "v_add_f32 v105, 0x3727c5ac, v105\n\t" \
  "v_cvt_pk_bf16_f32 v105, 0, v105\n\t" \
  "v_sqrt_f32 v105, v105\n\t" \
  "s_nop 0\n\t" \
  "v_cvt_pk_bf16_f32 v105, 0, v105\n\t" \
  "v_rcp_f32 v105, v105\n\t" \
  "s_branch 6b\n\t" \
  "7:\n\t" \
  "v_lshlrev_b32 v24, 16, v90\n\t" \
  "v_and_b32 v25, 0xffff0000, v90\n\t" \
  "v_lshlrev_b32 v26, 16, v91\n\t" \
  "v_and_b32 v27, 0xffff0000, v91\n\t" \
  "v_lshlrev_b32 v28, 16, v92\n\t" \
  "v_and_b32 v29, 0xffff0000, v92\n\t" \
  "v_lshlrev_b32 v30, 16, v93\n\t" \
  "v_and_b32 v31, 0xffff0000, v93\n\t" \
  "v_lshlrev_b32 v32, 16, v94\n\t" \
  "v_and_b32 v33, 0xffff0000, v94\n\t"

#define KGEN_ASM \
  "80:\n\t" \
  TSQ(4) \
  "s_mov_b32 s96, 0\n\t" \
  "s_mov_b32 s97, -1\n\t" \
  "v_pk_mul_f32 v[104:105], v[26:27], v[32:33]\n\t" \
  "v_pk_mul_f32 v[106:107], v[26:27], v[8:9]\n\t" \
  "v_pk_mul_f32 v[40:41], v[30:31], v[28:29] op_sel_hi:[0,1] neg_hi:[1,0]\n\t" \
  "v_fract_f32 v105, v105\n\t" \
  "v_fract_f32 v107, v107\n\t" \
  "v_pk_fma_f32 v[40:41], v[30:31], v[28:29], v[40:41] op_sel:[1,1,0] op_sel_hi:[1,0,1] neg_lo:[1,0,0] neg_hi:[1,0,0]\n\t" \
  "v_exp_f32 v104, v104\n\t" \
  "v_cos_f32 v116, v105\n\t" \
  "v_sin_f32 v117, v105\n\t" \
  "v_exp_f32 v106, v106\n\t" \
  "v_cos_f32 v118, v107\n\t" \
  "v_sin_f32 v119, v107\n\t" \
  "v_pk_mul_f32 v[116:117], v[104:105], v[116:117] op_sel_hi:[0,1]\n\t" \
  "v_pk_mul_f32 v[118:119], v[106:107], v[118:119] op_sel_hi:[0,1]\n\t" \
  "v_max_f32 v108, |v40|, |v41|\n\t" \
  "v_pk_mul_f32 v[120:121], v[116:117], v[116:117] op_sel_hi:[0,1]\n\t" \
  "v_pk_mul_f32 v[122:123], v[118:119], v[118:119] op_sel_hi:[0,1]\n\t" \
  "v_pk_fma_f32 v[120:121], v[116:117], v[116:117], v[120:121] op_sel:[1,1,0] op_sel_hi:[1,0,1] neg_lo:[1,0,0]\n\t" \
  "v_pk_fma_f32 v[122:123], v[118:119], v[118:119], v[122:123] op_sel:[1,1,0] op_sel_hi:[1,0,1] neg_lo:[1,0,0]\n\t" \
  "v_cmp_nle_f32 s[98:99], v108, 4.0\n\t" \
  "v_cmp_lt_f32 vcc, 0x3b000000, v108\n\t" \
  "v_cmp_lt_f32 s[94:95], 0, v26\n\t" \
  "v_cndmask_b32 v130, v122, v116, s[96:97]\n\t" \
  "v_cndmask_b32 v131, v123, v117, s[96:97]\n\t" \
  "v_pk_mul_f32 v[42:43], v[40:41], v[116:117] op_sel_hi:[0,1]\n\t" \
  "v_pk_mul_f32 v[44:45], v[40:41], v[120:121] op_sel_hi:[0,1]\n\t" \
  "v_pk_fma_f32 v[42:43], v[40:41], v[116:117], v[42:43] op_sel:[1,1,0] op_sel_hi:[1,0,1] neg_lo:[1,0,0]\n\t" \
  "v_pk_fma_f32 v[44:45], v[40:41], v[120:121], v[44:45] op_sel:[1,1,0] op_sel_hi:[1,0,1] neg_lo:[1,0,0]\n\t" \
  "v_pk_mul_f32 v[124:125], v[120:121], v[120:121] op_sel_hi:[0,1]\n\t" \
  "v_pk_mul_f32 v[126:127], v[122:123], v[130:131] op_sel_hi:[0,1]\n\t" \
  "v_pk_fma_f32 v[124:125], v[120:121], v[120:121], v[124:125] op_sel:[1,1,0] op_sel_hi:[1,0,1] neg_lo:[1,0,0]\n\t" \
  "v_pk_fma_f32 v[126:127], v[122:123], v[130:131], v[126:127] op_sel:[1,1,0] op_sel_hi:[1,0,1] neg_lo:[1,0,0]\n\t" \
  "s_or_b32 s98, s99, s95\n\t" \
  "s_cselect_b32 s98, 0x100, 0\n\t" \
  "s_cmp_lg_u32 vcc_hi, 0\n\t" \
  "s_cselect_b32 s99, 0x10000, 0\n\t" \
  "v_pk_mul_f32 v[56:57], v[40:41], v[118:119] op_sel_hi:[0,1]\n\t" \
  "v_pk_mul_f32 v[72:73], v[40:41], v[122:123] op_sel_hi:[0,1]\n\t" \
  "v_pk_fma_f32 v[56:57], v[40:41], v[118:119], v[56:57] op_sel:[1,1,0] op_sel_hi:[1,0,1] neg_lo:[1,0,0]\n\t" \
  "v_pk_fma_f32 v[72:73], v[40:41], v[122:123], v[72:73] op_sel:[1,1,0] op_sel_hi:[1,0,1] neg_lo:[1,0,0]\n\t" \
  "v_pk_mul_f32 v[48:49], v[40:41], v[124:125] op_sel_hi:[0,1]\n\t" \
  "v_pk_mul_f32 v[74:75], v[40:41], v[126:127] op_sel_hi:[0,1]\n\t" \
  "v_pk_fma_f32 v[48:49], v[40:41], v[124:125], v[48:49] op_sel:[1,1,0] op_sel_hi:[1,0,1] neg_lo:[1,0,0]\n\t" \
  "v_pk_fma_f32 v[74:75], v[40:41], v[126:127], v[74:75] op_sel:[1,1,0] op_sel_hi:[1,0,1] neg_lo:[1,0,0]\n\t" \
  "s_add_u32 s98, s98, %[av]\n\t" \
  "s_add_u32 s98, s98, s99\n\t" \
  "v_cvt_pk_f16_f32 v132, v40, v41\n\t" \
  "v_cvt_pk_f16_f32 v133, v42, v43\n\t" \
  "v_cvt_pk_f16_f32 v134, v44, v45\n\t" \
  "v_cvt_pk_f16_f32 v140, v56, v57\n\t" \
  "v_pk_mul_f32 v[46:47], v[42:43], v[120:121] op_sel_hi:[0,1]\n\t" \
  "v_pk_mul_f32 v[50:51], v[42:43], v[124:125] op_sel_hi:[0,1]\n\t" \
  "v_pk_fma_f32 v[46:47], v[42:43], v[120:121], v[46:47] op_sel:[1,1,0] op_sel_hi:[1,0,1] neg_lo:[1,0,0]\n\t" \
  "v_pk_fma_f32 v[50:51], v[42:43], v[124:125], v[50:51] op_sel:[1,1,0] op_sel_hi:[1,0,1] neg_lo:[1,0,0]\n\t" \
  "v_cndmask_b32 v128, v126, v120, s[96:97]\n\t" \
  "v_cndmask_b32 v129, v127, v121, s[96:97]\n\t" \
  "v_pk_mul_f32 v[52:53], v[44:45], v[124:125] op_sel_hi:[0,1]\n\t" \
  "v_pk_mul_f32 v[58:59], v[42:43], v[118:119] op_sel_hi:[0,1]\n\t" \
  "v_pk_fma_f32 v[52:53], v[44:45], v[124:125], v[52:53] op_sel:[1,1,0] op_sel_hi:[1,0,1] neg_lo:[1,0,0]\n\t" \
  "v_pk_fma_f32 v[58:59], v[42:43], v[118:119], v[58:59] op_sel:[1,1,0] op_sel_hi:[1,0,1] neg_lo:[1,0,0]\n\t" \
  "v_cvt_pk_f16_f32 v136, v48, v49\n\t" \
  "v_cvt_pk_f16_f32 v148, v72, v73\n\t" \
  "v_cvt_pk_f16_f32 v149, v74, v75\n\t" \
  "v_cvt_pk_f16_f32 v135, v46, v47\n\t" \
  "s_cmp_eq_u32 s76, 0\n\t" \
  "s_cbranch_scc1 3f\n\t" \
  "ds_write_addtid_b32 v132 offset:0\n\t" \
  "ds_write_addtid_b32 v133 offset:288\n\t" \
  "ds_write_addtid_b32 v134 offset:576\n\t" \
  "ds_write_addtid_b32 v140 offset:2304\n\t" \
  "ds_write_addtid_b32 v136 offset:1152\n\t" \
  "ds_write_addtid_b32 v148 offset:4608\n\t" \
  "ds_write_addtid_b32 v149 offset:4896\n\t" \
  "ds_write_addtid_b32 v135 offset:864\n\t" \
  "3:\n\t" \
  "v_pk_mul_f32 v[54:55], v[46:47], v[124:125] op_sel_hi:[0,1]\n\t" \
  "v_pk_mul_f32 v[60:61], v[44:45], v[118:119] op_sel_hi:[0,1]\n\t" \
  "v_pk_fma_f32 v[54:55], v[46:47], v[124:125], v[54:55] op_sel:[1,1,0] op_sel_hi:[1,0,1] neg_lo:[1,0,0]\n\t" \
  "v_pk_fma_f32 v[60:61], v[44:45], v[118:119], v[60:61] op_sel:[1,1,0] op_sel_hi:[1,0,1] neg_lo:[1,0,0]\n\t" \
  "v_pk_mul_f32 v[62:63], v[46:47], v[118:119] op_sel_hi:[0,1]\n\t" \
  "v_pk_mul_f32 v[64:65], v[48:49], v[118:119] op_sel_hi:[0,1]\n\t" \
  "v_pk_fma_f32 v[62:63], v[46:47], v[118:119], v[62:63] op_sel:[1,1,0] op_sel_hi:[1,0,1] neg_lo:[1,0,0]\n\t" \
  "v_pk_fma_f32 v[64:65], v[48:49], v[118:119], v[64:65] op_sel:[1,1,0] op_sel_hi:[1,0,1] neg_lo:[1,0,0]\n\t" \
  "v_cvt_pk_f16_f32 v137, v50, v51\n\t" \
  "v_cvt_pk_f16_f32 v138, v52, v53\n\t" \
  "v_cvt_pk_f16_f32 v141, v58, v59\n\t" \
  "v_cvt_pk_f16_f32 v139, v54, v55\n\t" \
  "v_pk_mul_f32 v[66:67], v[50:51], v[118:119] op_sel_hi:[0,1]\n\t" \
  "v_pk_mul_f32 v[68:69], v[52:53], v[118:119] op_sel_hi:[0,1]\n\t" \
  "v_pk_fma_f32 v[66:67], v[50:51], v[118:119], v[66:67] op_sel:[1,1,0] op_sel_hi:[1,0,1] neg_lo:[1,0,0]\n\t" \
  "v_pk_fma_f32 v[68:69], v[52:53], v[118:119], v[68:69] op_sel:[1,1,0] op_sel_hi:[1,0,1] neg_lo:[1,0,0]\n\t" \
  "v_pk_mul_f32 v[70:71], v[54:55], v[118:119] op_sel_hi:[0,1]\n\t" \
  "v_pk_mul_f32 v[76:77], v[72:73], v[128:129] op_sel_hi:[0,1]\n\t" \
  "v_pk_fma_f32 v[70:71], v[54:55], v[118:119], v[70:71] op_sel:[1,1,0] op_sel_hi:[1,0,1] neg_lo:[1,0,0]\n\t" \
  "v_pk_fma_f32 v[76:77], v[72:73], v[128:129], v[76:77] op_sel:[1,1,0] op_sel_hi:[1,0,1] neg_lo:[1,0,0]\n\t" \
  "v_cvt_pk_f16_f32 v142, v60, v61\n\t" \
  "v_cvt_pk_f16_f32 v143, v62, v63\n\t" \
  "v_cvt_pk_f16_f32 v144, v64, v65\n\t" \
  "v_cvt_pk_f16_f32 v145, v66, v67\n\t" \
  "v_pk_mul_f32 v[78:79], v[74:75], v[128:129] op_sel_hi:[0,1]\n\t" \
  "v_pk_mul_f32 v[80:81], v[48:49], v[122:123] op_sel_hi:[0,1]\n\t" \
  "v_pk_fma_f32 v[78:79], v[74:75], v[128:129], v[78:79] op_sel:[1,1,0] op_sel_hi:[1,0,1] neg_lo:[1,0,0]\n\t" \
  "v_pk_fma_f32 v[80:81], v[48:49], v[122:123], v[80:81] op_sel:[1,1,0] op_sel_hi:[1,0,1] neg_lo:[1,0,0]\n\t" \
  "v_pk_mul_f32 v[82:83], v[50:51], v[122:123] op_sel_hi:[0,1]\n\t" \
  "v_pk_mul_f32 v[84:85], v[52:53], v[122:123] op_sel_hi:[0,1]\n\t" \
  "v_pk_fma_f32 v[82:83], v[50:51], v[122:123], v[82:83] op_sel:[1,1,0] op_sel_hi:[1,0,1] neg_lo:[1,0,0]\n\t" \
  "v_pk_fma_f32 v[84:85], v[52:53], v[122:123], v[84:85] op_sel:[1,1,0] op_sel_hi:[1,0,1] neg_lo:[1,0,0]\n\t" \
  "v_cvt_pk_f16_f32 v146, v68, v69\n\t" \
  "v_cvt_pk_f16_f32 v147, v70, v71\n\t" \
  "v_cvt_pk_f16_f32 v150, v76, v77\n\t" \
  "v_cvt_pk_f16_f32 v151, v78, v79\n\t" \
  "s_cmp_eq_u32 s76, 0\n\t" \
  "s_cbranch_scc1 3f\n\t" \
  "ds_write_addtid_b32 v137 offset:1440\n\t" \
  "ds_write_addtid_b32 v138 offset:1728\n\t" \
  "ds_write_addtid_b32 v141 offset:2592\n\t" \
  "ds_write_addtid_b32 v139 offset:2016\n\t" \
  "ds_write_addtid_b32 v142 offset:2880\n\t" \
  "ds_write_addtid_b32 v143 offset:3168\n\t" \
  "ds_write_addtid_b32 v144 offset:3456\n\t" \
  "ds_write_addtid_b32 v145 offset:3744\n\t" \
  "ds_write_addtid_b32 v146 offset:4032\n\t" \
  "ds_write_addtid_b32 v147 offset:4320\n\t" \
  "ds_write_addtid_b32 v150 offset:5184\n\t" \
  "ds_write_addtid_b32 v151 offset:5472\n\t" \
  "3:\n\t" \
  "v_pk_mul_f32 v[86:87], v[54:55], v[122:123] op_sel_hi:[0,1]\n\t" \
  "v_pk_mul_f32 v[88:89], v[56:57], v[122:123] op_sel_hi:[0,1]\n\t" \
  "v_pk_fma_f32 v[86:87], v[54:55], v[122:123], v[86:87] op_sel:[1,1,0] op_sel_hi:[1,0,1] neg_lo:[1,0,0]\n\t" \
  "v_pk_fma_f32 v[88:89], v[56:57], v[122:123], v[88:89] op_sel:[1,1,0] op_sel_hi:[1,0,1] neg_lo:[1,0,0]\n\t" \
  "v_pk_mul_f32 v[90:91], v[56:57], v[126:127] op_sel_hi:[0,1]\n\t" \
  "v_pk_mul_f32 v[94:95], v[62:63], v[122:123] op_sel_hi:[0,1]\n\t" \
  "v_pk_fma_f32 v[90:91], v[56:57], v[126:127], v[90:91] op_sel:[1,1,0] op_sel_hi:[1,0,1] neg_lo:[1,0,0]\n\t" \
  "v_pk_fma_f32 v[94:95], v[62:63], v[122:123], v[94:95] op_sel:[1,1,0] op_sel_hi:[1,0,1] neg_lo:[1,0,0]\n\t" \
  "v_cvt_pk_f16_f32 v152, v80, v81\n\t" \
  "v_cvt_pk_f16_f32 v153, v82, v83\n\t" \
  "v_cvt_pk_f16_f32 v154, v84, v85\n\t" \
  "v_cvt_pk_f16_f32 v155, v86, v87\n\t" \
  "v_pk_mul_f32 v[92:93], v[88:89], v[128:129] op_sel_hi:[0,1]\n\t" \
  "v_pk_mul_f32 v[96:97], v[64:65], v[122:123] op_sel_hi:[0,1]\n\t" \
  "v_pk_fma_f32 v[92:93], v[88:89], v[128:129], v[92:93] op_sel:[1,1,0] op_sel_hi:[1,0,1] neg_lo:[1,0,0]\n\t" \
  "v_pk_fma_f32 v[96:97], v[64:65], v[122:123], v[96:97] op_sel:[1,1,0] op_sel_hi:[1,0,1] neg_lo:[1,0,0]\n\t" \
  "v_pk_mul_f32 v[98:99], v[66:67], v[122:123] op_sel_hi:[0,1]\n\t" \
  "v_pk_mul_f32 v[100:101], v[68:69], v[122:123] op_sel_hi:[0,1]\n\t" \
  "v_pk_fma_f32 v[98:99], v[66:67], v[122:123], v[98:99] op_sel:[1,1,0] op_sel_hi:[1,0,1] neg_lo:[1,0,0]\n\t" \
  "v_pk_fma_f32 v[100:101], v[68:69], v[122:123], v[100:101] op_sel:[1,1,0] op_sel_hi:[1,0,1] neg_lo:[1,0,0]\n\t" \
  "v_pk_mul_f32 v[102:103], v[70:71], v[122:123] op_sel_hi:[0,1]\n\t" \
  "v_mov_b32 v130, s98\n\t" \
  "v_pk_fma_f32 v[102:103], v[70:71], v[122:123], v[102:103] op_sel:[1,1,0] op_sel_hi:[1,0,1] neg_lo:[1,0,0]\n\t" \
  "v_cvt_pk_f16_f32 v156, v88, v89\n\t" \
  "v_cvt_pk_f16_f32 v157, v90, v91\n\t" \
  "v_cvt_pk_f16_f32 v159, v94, v95\n\t" \
  "v_cvt_pk_f16_f32 v158, v92, v93\n\t" \
  "v_cvt_pk_f16_f32 v160, v96, v97\n\t" \
  "v_cvt_pk_f16_f32 v161, v98, v99\n\t" \
  "v_cvt_pk_f16_f32 v162, v100, v101\n\t" \
  "v_cvt_pk_f16_f32 v163, v102, v103\n\t" \
  TSQ(28) \
  "s_cmp_lg_u32 s76, 0\n\t" \
  "s_setprio " PL1 "\n\t" \
  "s_cbranch_scc1 3f\n\t" \
  "s_setprio " PL "\n\t" \
  "ds_write_addtid_b32 v132 offset:0\n\t" \
  "ds_write_addtid_b32 v133 offset:288\n\t" \
  "ds_write_addtid_b32 v134 offset:576\n\t" \
  "ds_write_addtid_b32 v140 offset:2304\n\t" \
  "ds_write_addtid_b32 v136 offset:1152\n\t" \
  "ds_write_addtid_b32 v148 offset:4608\n\t" \
  "ds_write_addtid_b32 v149 offset:4896\n\t" \
  "ds_write_addtid_b32 v135 offset:864\n\t" \
  "ds_write_addtid_b32 v137 offset:1440\n\t" \
  "ds_write_addtid_b32 v138 offset:1728\n\t" \
  "ds_write_addtid_b32 v141 offset:2592\n\t" \
  "ds_write_addtid_b32 v139 offset:2016\n\t" \
  "ds_write_addtid_b32 v142 offset:2880\n\t" \
  "ds_write_addtid_b32 v143 offset:3168\n\t" \
  "ds_write_addtid_b32 v144 offset:3456\n\t" \
  "ds_write_addtid_b32 v145 offset:3744\n\t" \
  "ds_write_addtid_b32 v146 offset:4032\n\t" \
  "ds_write_addtid_b32 v147 offset:4320\n\t" \
  "ds_write_addtid_b32 v150 offset:5184\n\t" \
  "ds_write_addtid_b32 v151 offset:5472\n\t" \
  "3:\n\t" \
  "ds_read_b32 v34, v25 offset:16\n\t" \
  "ds_read_b128 v[36:39], v5\n\t" \
  "ds_read_b32 v35, v5 offset:3072\n\t" \
  "ds_write_addtid_b32 v152 offset:5760\n\t" \
  "ds_write_addtid_b32 v153 offset:6048\n\t" \
  "ds_write_addtid_b32 v154 offset:6336\n\t" \
  "ds_write_addtid_b32 v155 offset:6624\n\t" \
  "ds_write_addtid_b32 v156 offset:6912\n\t" \
  "ds_write_addtid_b32 v157 offset:7200\n\t" \
  "ds_write_addtid_b32 v159 offset:7776\n\t" \
  "ds_write_addtid_b32 v158 offset:7488\n\t" \
  "ds_write_addtid_b32 v160 offset:8064\n\t" \
  "ds_write_addtid_b32 v161 offset:8352\n\t" \
  "ds_write_addtid_b32 v162 offset:8640\n\t" \
  "ds_write_addtid_b32 v163 offset:8928\n\t" \
  "s_waitcnt lgkmcnt(12)\n\t" \
  "v_cmp_ne_u32 vcc, %[tag], v34\n\t" \
  "s_cbranch_vccnz 85f\n\t" \
  "86:\n\t" \
  "s_mov_b64 exec, 1\n\t" \
  "ds_add_u32 v11, v130\n\t" \
  "s_mov_b64 exec, -1\n\t" \
  "s_bitcmp0_b32 s76, 8\n\t" \
  "s_cbranch_scc1 3f\n\t" \
  "v_and_b32 v164, 63, v0\n\t" \
  "v_min_u32 v164, 23, v164\n\t" \
  "v_lshlrev_b32 v164, 6, v164\n\t" \
  "v_add_u32 v164, 12599296, v164\n\t" \
  "global_load_dword v164, v164, s[70:71] sc1\n\t" \
  "3:\n\t" \
  TSQ(5) \
  "v_add_u32 v10, s78, v36\n\t" \
  "v_add_u32 v36, %[opc], v36\n\t" \
  "v_mov_b32 v112, v15\n\t" \
  "v_mov_b32 v113, 0\n\t" \
  "v_mov_b64 v[114:115], 0\n\t" \
  "v_mov_b32 v130, 1\n\t" \
  "1:\n\t" \
  "ds_read_b32 v108, v11\n\t" \
  "s_waitcnt lgkmcnt(0)\n\t" \
  "v_readfirstlane_b32 s98, v108\n\t" \
  "s_and_b32 s99, s98, 0xff\n\t" \
  "s_cmp_lt_u32 s99, s75\n\t" \
  "s_cbranch_scc1 1b\n\t" \
  "s_mov_b32 s94, s98\n\t" \
  TSQ(29) \
  "s_mov_b32 s98, s94\n\t" \
  "ds_read_b128 v[40:43], v36 offset:0\n\t" \
  "ds_read_b128 v[44:47], v10 offset:128\n\t" \
  "ds_read_b128 v[48:51], v36 offset:64\n\t" \
  "ds_read_b128 v[52:55], v10 offset:192\n\t" \
  "ds_read_b128 v[56:59], v36 offset:9216\n\t" \
  "ds_read_b128 v[60:63], v10 offset:9344\n\t" \
  "ds_read_b128 v[64:67], v36 offset:9280\n\t" \
  "ds_read_b128 v[68:71], v10 offset:9408\n\t" \
  "ds_read_b128 v[72:75], v36 offset:18432\n\t" \
  "ds_read_b128 v[76:79], v10 offset:18560\n\t" \
  "ds_read_b128 v[80:83], v36 offset:18496\n\t" \
  "ds_read_b128 v[84:87], v10 offset:18624\n\t" \
  "ds_read_b128 v[88:91], v36 offset:27648\n\t" \
  "ds_read_b128 v[92:95], v10 offset:27776\n\t" \
  "ds_read_b128 v[96:99], v36 offset:27712\n\t" \
  "ds_read_b128 v[100:103], v10 offset:27840\n\t" \
  "v_add_u32 v37, %[chb], v37\n\t" \
  "v_add_u32 v38, %[chb], v38\n\t" \
  "v_add_u32 v39, %[chb], v39\n\t" \
  "v_add_u32 v35, %[chb], v35\n\t" \
  "s_cmp_lt_u32 s98, 0x10000\n\t" \
  "s_cselect_b32 s99, 0x100, s98\n\t" \
  "s_and_b32 s99, s99, 0xff00\n\t" \
  "s_cmp_eq_u32 s75, 4\n\t" \
  "s_cselect_b32 s99, s99, 0\n\t" \
  "s_cmp_lg_u32 s99, 0\n\t" \
  "s_cbranch_scc1 89f\n\t" \
  "s_waitcnt lgkmcnt(14)\n\t" \
  "v_mfma_f32_16x16x32_f16 v[112:115], v[40:43], v[44:47], v[112:115]\n\t" \
  "s_waitcnt lgkmcnt(12)\n\t" \
  "v_mfma_f32_16x16x32_f16 v[112:115], v[48:51], v[52:55], v[112:115]\n\t" \
  "s_waitcnt lgkmcnt(10)\n\t" \
  "v_mfma_f32_16x16x32_f16 v[112:115], v[56:59], v[60:63], v[112:115]\n\t" \
  "s_waitcnt lgkmcnt(8)\n\t" \
  "v_mfma_f32_16x16x32_f16 v[112:115], v[64:67], v[68:71], v[112:115]\n\t" \
  "s_waitcnt lgkmcnt(6)\n\t" \
  "v_mfma_f32_16x16x32_f16 v[112:115], v[72:75], v[76:79], v[112:115]\n\t" \
  "s_waitcnt lgkmcnt(4)\n\t" \
  "v_mfma_f32_16x16x32_f16 v[112:115], v[80:83], v[84:87], v[112:115]\n\t" \
  "s_waitcnt lgkmcnt(2)\n\t" \
  "v_mfma_f32_16x16x32_f16 v[112:115], v[88:91], v[92:95], v[112:115]\n\t" \
  "s_waitcnt lgkmcnt(0)\n\t" \
  "v_mfma_f32_16x16x32_f16 v[112:115], v[96:99], v[100:103], v[112:115]\n\t" \
  TSQ(30) \
  "s_bitcmp1_b32 %[wk], 0\n\t" \
  "s_cbranch_scc0 4f\n\t" \
  "s_nop 5\n\t" \
  "v_cvt_pk_f16_f32 v104, v112, v113\n\t" \
  "v_cvt_pk_f16_f32 v105, v114, v115\n\t" \
  "s_cmp_lg_u32 %[wk], 1\n\t" \
  "v_readfirstlane_b32 s98, v104\n\t" \
  "v_readfirstlane_b32 s99, v105\n\t" \
  "ds_write_b16 v37, v104 offset:0\n\t" \
  "ds_write_b16_d16_hi v38, v104 offset:0\n\t" \
  "ds_write_b16 v39, v105 offset:0\n\t" \
  "ds_write_b16 v37, v104 offset:2114\n\t" \
  "ds_write_b16_d16_hi v38, v104 offset:2114\n\t" \
  "ds_write_b16 v39, v105 offset:2114\n\t" \
  "ds_write_b16 v37, v104 offset:4228\n\t" \
  "ds_write_b16_d16_hi v38, v104 offset:4228\n\t" \
  "ds_write_b16 v39, v105 offset:4228\n\t" \
  "ds_write_b16 v37, v104 offset:6342\n\t" \
  "ds_write_b16_d16_hi v38, v104 offset:6342\n\t" \
  "ds_write_b16 v39, v105 offset:6342\n\t" \
  "s_cbranch_scc1 6f\n\t" \
  "v_mov_b32 v104, s98\n\t" \
  "s_mov_b32 exec_lo, 0x80808080\n\t" \
  "v_mov_b32 v104, s99\n\t" \
  "s_mov_b32 exec_lo, 0x0f0f0f0f\n\t" \
  "s_mov_b32 exec_hi, 0\n\t" \
  "ds_write_b16 v35, v104\n\t" \
  "s_mov_b32 exec_lo, 0xf0f0f0f0\n\t" \
  "ds_write_b16_d16_hi v35, v104\n\t" \
  "s_branch 6f\n\t" \
  "4:\n\t" \
  "s_nop 5\n\t" \
  "v_cndmask_b32 v113, v113, v112, s[96:97]\n\t" \
  "v_cndmask_b32 v115, v115, v112, s[96:97]\n\t" \
  "v_cvt_pk_f16_f32 v106, v114, v113\n\t" \
  "v_cvt_pk_f16_f32 v104, v113, v112\n\t" \
  "v_cvt_f16_f32 v105, v115\n\t" \
  "ds_write_b32 v37, v106 offset:2\n\t" \
  "ds_write_b16_d16_hi v37, v104 offset:6\n\t" \
  "ds_write_b16 v38, v105 offset:0\n\t" \
  "ds_write_b32 v37, v104 offset:2118\n\t" \
  "ds_write_b16 v37, v106 offset:2116\n\t" \
  "ds_write_b16 v38, v105 offset:2114\n\t" \
  "ds_write_b32 v37, v106 offset:4230\n\t" \
  "ds_write_b16_d16_hi v37, v104 offset:4234\n\t" \
  "ds_write_b16 v38, v105 offset:4228\n\t" \
  "ds_write_b32 v37, v104 offset:6346\n\t" \
  "ds_write_b16 v37, v106 offset:6344\n\t" \
  "ds_write_b16 v38, v105 offset:6342\n\t" \
  "6:\n\t" \
  "s_mov_b64 exec, 1\n\t" \
  "ds_add_u32 v11, v130 offset:32\n\t" \
  "s_mov_b64 exec, -1\n\t" \
  "s_bitcmp1_b32 s76, 8\n\t" \
  "s_cbranch_scc1 81f\n\t" \
  "s_setprio " PE "\n\t" \
  "s_sub_u32 s98, s74, 2\n\t" \
  "s_cmp_gt_u32 s98, 9\n\t" \
  "s_cbranch_scc1 10f\n\t" \
  "s_call_b64 s[90:91], 65b\n\t" \
  "s_cmp_lg_u32 s74, 2\n\t" \
  "s_cbranch_scc1 10f\n\t" \
  "global_load_dword v166, v165, s[72:73]" TNT "\n\t" \
  "10:\n\t" \
  TSQ(6) \
  "s_bitcmp1_b32 0x888, %[w]\n\t" \
  "s_cbranch_scc1 82f\n\t" \
  "s_cmp_gt_u32 %[w], 2\n\t" \
  "s_cbranch_scc1 90f\n\t"

#define STAGE_ASM \
  "s_setprio 3\n\t" \
  "s_mul_i32 s96, %[id], 0x5bd1e996\n\t" \
  "s_sub_u32 s97, s68, 2048\n\t" \
  "s_add_u32 s96, s96, s97\n\t" \
  "s_mov_b32 s95, 2\n\t" \
  "v_and_b32 v1, 63, v0\n\t" \
  "13:\n\t" \
  "s_waitcnt vmcnt(0)\n\t" \
  "v_subrev_u32 v164, s96, v164\n\t" \
  "v_lshlrev_b32 v2, 4, v1\n\t" \
  "v_mov_b32 v5, 1\n\t" \
  "v_max_u32_dpp v164, v164, v164 quad_perm:[1,0,3,2] row_mask:0xf bank_mask:0xf bound_ctrl:1\n\t" \
  "v_add_u32 v3, 8192, v2\n\t" \
  "v_lshrrev_b32 v4, 1, %[w]\n\t" \
  "v_max_u32_dpp v164, v164, v164 quad_perm:[2,3,0,1] row_mask:0xf bank_mask:0xf bound_ctrl:1\n\t" \
  "v_and_b32 v4, 4, v4\n\t" \
  "v_mov_b32 v7, 0\n\t" \
  "v_max_u32_dpp v164, v164, v164 row_half_mirror row_mask:0xf bank_mask:0xf bound_ctrl:1\n\t" \
  "s_lshr_b32 s94, %[w], 3\n\t" \
  "s_lshl4_add_u32 s92, %[b], s94\n\t" \
  "v_max_u32_dpp v164, v164, v164 row_mirror row_mask:0xf bank_mask:0xf bound_ctrl:1\n\t" \
  "s_add_u32 s92, s92, 4\n\t" \
  "v_readlane_b32 s98, v164, 0\n\t" \
  "v_readlane_b32 s99, v164, 16\n\t" \
  "s_lshl_b32 s92, s92, 10\n\t" \
  "s_add_u32 s92, s92, s70\n\t" \
  "s_addc_u32 s93, s71, 0\n\t" \
  "s_lshl_b32 s94, s94, 8\n\t" \
  "s_add_u32 m0, s94, 122336\n\t" \
  "s_max_u32 s98, s98, s99\n\t" \
  "s_cmp_ge_u32 s98, 4096\n\t" \
  "s_cbranch_scc1 5f\n\t" \
  "s_add_u32 s97, s97, s98\n\t" \
  "6:\n\t" \
  "s_add_u32 s97, s97, " STR(UT) "\n\t" \
  "2:\n\t" \
  "s_memrealtime s[98:99]\n\t" \
  "s_waitcnt lgkmcnt(0)\n\t" \
  "s_sub_u32 s98, s98, s97\n\t" \
  "s_cmp_ge_i32 s98, 0\n\t" \
  "s_cbranch_scc1 2f\n\t" \
  "s_sleep 1\n\t" \
  "s_branch 2b\n\t" \
  "2:\n\t" \
  TSQ(7) \
  "s_mov_b32 s75, 0x40000\n\t" \
  "global_load_dwordx4 v[120:123], v2, s[92:93] offset:-4096 sc1\n\t" \
  "global_load_dwordx4 v[124:127], v2, s[92:93] offset:-2048 sc1\n\t" \
  "global_load_dwordx4 v[128:131], v2, s[92:93] offset:0 sc1\n\t" \
  "global_load_dwordx4 v[132:135], v2, s[92:93] offset:2048 sc1\n\t" \
  "global_load_dwordx4 v[136:139], v3, s[92:93] offset:-4096 sc1\n\t" \
  "global_load_dwordx4 v[140:143], v3, s[92:93] offset:-2048 sc1\n\t" \
  "global_load_dwordx4 v[144:147], v3, s[92:93] offset:0 sc1\n\t" \
  "global_load_dwordx4 v[148:151], v3, s[92:93] offset:2048 sc1\n\t" \
  "s_cmp_eq_u32 s74, 15\n\t" \
  "s_cbranch_scc1 1f\n\t" \
  "s_call_b64 s[90:91], 65b\n\t" \
  "1:\n\t" \
  "20:\n\t" \
  "s_waitcnt vmcnt(7)\n\t" \
  "ds_write_addtid_b32 v120 offset:0\n\t" \
  "ds_write_addtid_b32 v121 offset:13024\n\t" \
  "ds_write_addtid_b32 v122 offset:26048\n\t" \
  "v_xor_b32 v6, %[tag], v123\n\t" \
  "s_waitcnt vmcnt(6)\n\t" \
  "ds_write_addtid_b32 v124 offset:512\n\t" \
  "ds_write_addtid_b32 v125 offset:13536\n\t" \
  "ds_write_addtid_b32 v126 offset:26560\n\t" \
  "v_bitop3_b32 v6, v6, v127, %[tag] bitop3:0xf6\n\t" \
  "v_cmp_ne_u32 vcc, 0, v6\n\t" \
  "s_cbranch_vccnz 31f\n\t" \
  "21:\n\t" \
  "ds_write_b32 v4, v5 offset:64\n\t" \
  TSQ(1) \
  "22:\n\t" \
  "s_waitcnt vmcnt(5)\n\t" \
  "ds_write_addtid_b32 v128 offset:1024\n\t" \
  "ds_write_addtid_b32 v129 offset:14048\n\t" \
  "ds_write_addtid_b32 v130 offset:27072\n\t" \
  "v_xor_b32 v6, %[tag], v131\n\t" \
  "s_waitcnt vmcnt(4)\n\t" \
  "ds_write_addtid_b32 v132 offset:1536\n\t" \
  "ds_write_addtid_b32 v133 offset:14560\n\t" \
  "ds_write_addtid_b32 v134 offset:27584\n\t" \
  "v_bitop3_b32 v6, v6, v135, %[tag] bitop3:0xf6\n\t" \
  "v_cmp_ne_u32 vcc, 0, v6\n\t" \
  "s_cbranch_vccnz 33f\n\t" \
  "23:\n\t" \
  "ds_write_b32 v4, v5 offset:72\n\t" \
  "24:\n\t" \
  "s_waitcnt vmcnt(3)\n\t" \
  "ds_write_addtid_b32 v136 offset:2048\n\t" \
  "ds_write_addtid_b32 v137 offset:15072\n\t" \
  "ds_write_addtid_b32 v138 offset:28096\n\t" \
  "v_xor_b32 v6, %[tag], v139\n\t" \
  "s_waitcnt vmcnt(2)\n\t" \
  "ds_write_addtid_b32 v140 offset:2560\n\t" \
  "ds_write_addtid_b32 v141 offset:15584\n\t" \
  "ds_write_addtid_b32 v142 offset:28608\n\t" \
  "v_bitop3_b32 v6, v6, v143, %[tag] bitop3:0xf6\n\t" \
  "v_cmp_ne_u32 vcc, 0, v6\n\t" \
  "s_cbranch_vccnz 35f\n\t" \
  "25:\n\t" \
  "ds_write_b32 v4, v5 offset:80\n\t" \
  "26:\n\t" \
  "s_waitcnt vmcnt(1)\n\t" \
  "ds_write_addtid_b32 v144 offset:3072\n\t" \
  "ds_write_addtid_b32 v145 offset:16096\n\t" \
  "ds_write_addtid_b32 v146 offset:29120\n\t" \
  "v_xor_b32 v6, %[tag], v147\n\t" \
  "s_waitcnt vmcnt(0)\n\t" \
  "ds_write_addtid_b32 v148 offset:3584\n\t" \
  "ds_write_addtid_b32 v149 offset:16608\n\t" \
  "ds_write_addtid_b32 v150 offset:29632\n\t" \
  "v_bitop3_b32 v6, v6, v151, %[tag] bitop3:0xf6\n\t" \
  "v_cmp_ne_u32 vcc, 0, v6\n\t" \
  "s_cbranch_vccnz 37f\n\t" \
  "27:\n\t" \
  "ds_write_b32 v4, v5 offset:88\n\t" \
  "s_cmp_lg_u32 s74, 3\n\t" \
  "s_cbranch_scc1 8f\n\t" \
  "global_load_dword v166, v165, s[72:73]" TNT "\n\t" \
  "s_branch 8f\n\t" \
  "31:\n\t" \
  "s_sub_u32 s75, s75, 1\n\t" \
  "s_cmp_lt_i32 s75, 0\n\t" \
  "s_cbranch_scc1 21b\n\t" \
  "s_waitcnt vmcnt(0)\n\t" \
  "v_cmp_ne_u32 vcc, %[tag], v123\n\t" \
  "s_cbranch_vccz 3f\n\t" \
  "s_mov_b64 exec, vcc\n\t" \
  "global_load_dwordx4 v[120:123], v2, s[92:93] offset:-4096 sc1\n\t" \
  "s_mov_b64 exec, -1\n\t" \
  "3:\n\t" \
  "v_cmp_ne_u32 vcc, %[tag], v127\n\t" \
  "s_cbranch_vccz 3f\n\t" \
  "s_mov_b64 exec, vcc\n\t" \
  "global_load_dwordx4 v[124:127], v2, s[92:93] offset:-2048 sc1\n\t" \
  "s_mov_b64 exec, -1\n\t" \
  "3:\n\t" \
  "v_cmp_ne_u32 vcc, %[tag], v131\n\t" \
  "s_cbranch_vccz 3f\n\t" \
  "s_mov_b64 exec, vcc\n\t" \
  "global_load_dwordx4 v[128:131], v2, s[92:93] offset:0 sc1\n\t" \
  "s_mov_b64 exec, -1\n\t" \
  "3:\n\t" \
  "v_cmp_ne_u32 vcc, %[tag], v135\n\t" \
  "s_cbranch_vccz 3f\n\t" \
  "s_mov_b64 exec, vcc\n\t" \
  "global_load_dwordx4 v[132:135], v2, s[92:93] offset:2048 sc1\n\t" \
  "s_mov_b64 exec, -1\n\t" \
  "3:\n\t" \
  "v_cmp_ne_u32 vcc, %[tag], v139\n\t" \
  "s_cbranch_vccz 3f\n\t" \
  "s_mov_b64 exec, vcc\n\t" \
  "global_load_dwordx4 v[136:139], v3, s[92:93] offset:-4096 sc1\n\t" \
  "s_mov_b64 exec, -1\n\t" \
  "3:\n\t" \
  "v_cmp_ne_u32 vcc, %[tag], v143\n\t" \
  "s_cbranch_vccz 3f\n\t" \
  "s_mov_b64 exec, vcc\n\t" \
  "global_load_dwordx4 v[140:143], v3, s[92:93] offset:-2048 sc1\n\t" \
  "s_mov_b64 exec, -1\n\t" \
  "3:\n\t" \
  "v_cmp_ne_u32 vcc, %[tag], v147\n\t" \
  "s_cbranch_vccz 3f\n\t" \
  "s_mov_b64 exec, vcc\n\t" \
  "global_load_dwordx4 v[144:147], v3, s[92:93] offset:0 sc1\n\t" \
  "s_mov_b64 exec, -1\n\t" \
  "3:\n\t" \
  "v_cmp_ne_u32 vcc, %[tag], v151\n\t" \
  "s_cbranch_vccz 3f\n\t" \
  "s_mov_b64 exec, vcc\n\t" \
  "global_load_dwordx4 v[148:151], v3, s[92:93] offset:2048 sc1\n\t" \
  "s_mov_b64 exec, -1\n\t" \
  "3:\n\t" \
  "s_waitcnt vmcnt(0)\n\t" \
  "s_branch 20b\n\t" \
  "33:\n\t" \
  "s_sub_u32 s75, s75, 1\n\t" \
  "s_cmp_lt_i32 s75, 0\n\t" \
  "s_cbranch_scc1 23b\n\t" \
  "s_waitcnt vmcnt(0)\n\t" \
  "v_cmp_ne_u32 vcc, %[tag], v131\n\t" \
  "s_cbranch_vccz 3f\n\t" \
  "s_mov_b64 exec, vcc\n\t" \
  "global_load_dwordx4 v[128:131], v2, s[92:93] offset:0 sc1\n\t" \
  "s_mov_b64 exec, -1\n\t" \
  "3:\n\t" \
  "v_cmp_ne_u32 vcc, %[tag], v135\n\t" \
  "s_cbranch_vccz 3f\n\t" \
  "s_mov_b64 exec, vcc\n\t" \
  "global_load_dwordx4 v[132:135], v2, s[92:93] offset:2048 sc1\n\t" \
  "s_mov_b64 exec, -1\n\t" \
  "3:\n\t" \
  "v_cmp_ne_u32 vcc, %[tag], v139\n\t" \
  "s_cbranch_vccz 3f\n\t" \
  "s_mov_b64 exec, vcc\n\t" \
  "global_load_dwordx4 v[136:139], v3, s[92:93] offset:-4096 sc1\n\t" \
  "s_mov_b64 exec, -1\n\t" \
  "3:\n\t" \
  "v_cmp_ne_u32 vcc, %[tag], v143\n\t" \
  "s_cbranch_vccz 3f\n\t" \
  "s_mov_b64 exec, vcc\n\t" \
  "global_load_dwordx4 v[140:143], v3, s[92:93] offset:-2048 sc1\n\t" \
  "s_mov_b64 exec, -1\n\t" \
  "3:\n\t" \
  "v_cmp_ne_u32 vcc, %[tag], v147\n\t" \
  "s_cbranch_vccz 3f\n\t" \
  "s_mov_b64 exec, vcc\n\t" \
  "global_load_dwordx4 v[144:147], v3, s[92:93] offset:0 sc1\n\t" \
  "s_mov_b64 exec, -1\n\t" \
  "3:\n\t" \
  "v_cmp_ne_u32 vcc, %[tag], v151\n\t" \
  "s_cbranch_vccz 3f\n\t" \
  "s_mov_b64 exec, vcc\n\t" \
  "global_load_dwordx4 v[148:151], v3, s[92:93] offset:2048 sc1\n\t" \
  "s_mov_b64 exec, -1\n\t" \
  "3:\n\t" \
  "s_waitcnt vmcnt(0)\n\t" \
  "s_branch 22b\n\t" \
  "35:\n\t" \
  "s_sub_u32 s75, s75, 1\n\t" \
  "s_cmp_lt_i32 s75, 0\n\t" \
  "s_cbranch_scc1 25b\n\t" \
  "s_waitcnt vmcnt(0)\n\t" \
  "v_cmp_ne_u32 vcc, %[tag], v139\n\t" \
  "s_cbranch_vccz 3f\n\t" \
  "s_mov_b64 exec, vcc\n\t" \
  "global_load_dwordx4 v[136:139], v3, s[92:93] offset:-4096 sc1\n\t" \
  "s_mov_b64 exec, -1\n\t" \
  "3:\n\t" \
  "v_cmp_ne_u32 vcc, %[tag], v143\n\t" \
  "s_cbranch_vccz 3f\n\t" \
  "s_mov_b64 exec, vcc\n\t" \
  "global_load_dwordx4 v[140:143], v3, s[92:93] offset:-2048 sc1\n\t" \
  "s_mov_b64 exec, -1\n\t" \
  "3:\n\t" \
  "v_cmp_ne_u32 vcc, %[tag], v147\n\t" \
  "s_cbranch_vccz 3f\n\t" \
  "s_mov_b64 exec, vcc\n\t" \
  "global_load_dwordx4 v[144:147], v3, s[92:93] offset:0 sc1\n\t" \
  "s_mov_b64 exec, -1\n\t" \
  "3:\n\t" \
  "v_cmp_ne_u32 vcc, %[tag], v151\n\t" \
  "s_cbranch_vccz 3f\n\t" \
  "s_mov_b64 exec, vcc\n\t" \
  "global_load_dwordx4 v[148:151], v3, s[92:93] offset:2048 sc1\n\t" \
  "s_mov_b64 exec, -1\n\t" \
  "3:\n\t" \
  "s_waitcnt vmcnt(0)\n\t" \
  "s_branch 24b\n\t" \
  "37:\n\t" \
  "s_sub_u32 s75, s75, 1\n\t" \
  "s_cmp_lt_i32 s75, 0\n\t" \
  "s_cbranch_scc1 27b\n\t" \
  "s_waitcnt vmcnt(0)\n\t" \
  "v_cmp_ne_u32 vcc, %[tag], v147\n\t" \
  "s_cbranch_vccz 3f\n\t" \
  "s_mov_b64 exec, vcc\n\t" \
  "global_load_dwordx4 v[144:147], v3, s[92:93] offset:0 sc1\n\t" \
  "s_mov_b64 exec, -1\n\t" \
  "3:\n\t" \
  "v_cmp_ne_u32 vcc, %[tag], v151\n\t" \
  "s_cbranch_vccz 3f\n\t" \
  "s_mov_b64 exec, vcc\n\t" \
  "global_load_dwordx4 v[148:151], v3, s[92:93] offset:2048 sc1\n\t" \
  "s_mov_b64 exec, -1\n\t" \
  "3:\n\t" \
  "s_waitcnt vmcnt(0)\n\t" \
  "s_branch 26b\n\t" \
  "5:\n\t" \
  "s_sub_u32 s95, s95, 1\n\t" \
  "s_cbranch_scc1 7f\n\t" \
  "s_lshl_b32 s98, s95, 11\n\t" \
  "s_add_u32 s98, s98, 12601344\n\t" \
  "v_min_u32 v164, 23, v1\n\t" \
  "v_lshl_add_u32 v164, v164, 6, s98\n\t" \
  "global_load_dword v164, v164, s[70:71] sc1\n\t" \
  "s_branch 13b\n\t" \
  "7:\n\t" \
  "s_add_u32 s97, s97, 2048+150\n\t" \
  "s_branch 6b\n\t" \
  "8:\n\t"

#define RESCALE_ASM \
  "s_waitcnt lgkmcnt(0)\n\t" \
  "v_and_b32 v1, 63, v0\n\t" \
  "s_lshl_b32 s98, %[d], 8\n\t" \
  "v_lshl_add_u32 v1, v1, 2, s98\n\t" \
  "global_load_dword v40, v1, s[38:39]\n\t" \
  "global_load_dword v41, v1, s[42:43]\n\t" \
  "global_load_dword v42, v1, s[44:45]\n\t" \
  "global_load_dword v43, v1, s[46:47]\n\t" \
  "global_load_dword v44, v1, s[48:49]\n\t" \
  "v_mov_b32 v45, %[sdt]\n\t" \
  "v_mul_f32 v46, 0x454fbf82, v45\n\t" \
  "v_mov_b32 v47, 0x43480000\n\t" \
  "s_waitcnt vmcnt(0)\n\t" \
  "v_and_b32 v41, 0x7fff7fff, v41\n\t" \
  "v_and_b32 v42, 0x7fff7fff, v42\n\t" \
  "v_and_b32 v43, 0x7fff7fff, v43\n\t" \
  "v_and_b32 v44, 0x7fff7fff, v44\n\t" \
  "v_pk_max_u16 v41, v41, v42\n\t" \
  "v_pk_max_u16 v43, v43, v44\n\t" \
  "v_pk_add_u16 v41, v41, v43\n\t" \
  "v_lshlrev_b32 v42, 16, v41\n\t" \
  "v_max_u32 v41, v41, v42\n\t" \
  "s_nop 1\n\t" \
  "v_max_u32_dpp v41, v41, v41 quad_perm:[1,0,3,2] row_mask:0xf bank_mask:0xf bound_ctrl:1\n\t" \
  "s_nop 1\n\t" \
  "v_max_u32_dpp v41, v41, v41 quad_perm:[2,3,0,1] row_mask:0xf bank_mask:0xf bound_ctrl:1\n\t" \
  "s_nop 1\n\t" \
  "v_max_u32_dpp v41, v41, v41 row_half_mirror row_mask:0xf bank_mask:0xf bound_ctrl:1\n\t" \
  "s_nop 1\n\t" \
  "v_max_u32_dpp v41, v41, v41 row_mirror row_mask:0xf bank_mask:0xf bound_ctrl:1\n\t" \
  "s_nop 0\n\t" \
  "v_readlane_b32 s92, v41, 0\n\t" \
  "v_readlane_b32 s93, v41, 16\n\t" \
  "v_readlane_b32 s94, v41, 32\n\t" \
  "v_readlane_b32 s95, v41, 48\n\t" \
  "s_max_u32 s92, s92, s93\n\t" \
  "s_max_u32 s94, s94, s95\n\t" \
  "s_max_u32 s92, s92, s94\n\t" \
  "s_lshr_b32 s98, s92, 23\n\t" \
  "s_lshr_b32 s99, %[sdt], 23\n\t" \
  "s_add_i32 s98, s98, s99\n\t" \
  "s_add_i32 s98, s98, -369\n\t" \
  "v_lshlrev_b32 v42, 16, v40\n\t" \
  "v_and_b32 v40, 0xffff0000, v40\n\t" \
  "v_max_f32 v40, v40, v42\n\t" \
  "v_mul_f32 v40, v40, v46\n\t" \
  "v_med3_f32 v40, v40, 0, v47\n\t" \
  "v_ceil_f32 v40, v40\n\t" \
  "v_cvt_i32_f32 v40, v40\n\t" \
  "v_mul_f32 v42, v26, v46\n\t" \
  "v_med3_f32 v42, v42, 0, v47\n\t" \
  "v_ceil_f32 v42, v42\n\t" \
  "v_cvt_i32_f32 v42, v42\n\t" \
  "v_max_u32_dpp v40, v40, v40 quad_perm:[1,0,3,2] row_mask:0xf bank_mask:0xf bound_ctrl:1\n\t" \
  "s_nop 1\n\t" \
  "v_max_u32_dpp v40, v40, v40 quad_perm:[2,3,0,1] row_mask:0xf bank_mask:0xf bound_ctrl:1\n\t" \
  "s_nop 1\n\t" \
  "v_max_u32_dpp v40, v40, v40 row_half_mirror row_mask:0xf bank_mask:0xf bound_ctrl:1\n\t" \
  "s_nop 1\n\t" \
  "v_max_u32_dpp v40, v40, v40 row_mirror row_mask:0xf bank_mask:0xf bound_ctrl:1\n\t" \
  "s_nop 0\n\t" \
  "v_readlane_b32 s92, v40, 0\n\t" \
  "v_readlane_b32 s93, v40, 16\n\t" \
  "v_readlane_b32 s94, v40, 32\n\t" \
  "v_readlane_b32 s95, v40, 48\n\t" \
  "s_max_u32 s92, s92, s93\n\t" \
  "s_max_u32 s94, s94, s95\n\t" \
  "s_max_u32 s92, s92, s94\n\t" \
  "s_add_i32 s98, s98, s92\n\t" \
  "s_add_i32 s92, s92, 7\n\t" \
  "s_lshr_b32 s92, s92, 3\n\t" \
  "s_add_i32 s98, s98, s92\n\t" \
  "s_bfe_u32 s99, %[sd], 0x80017\n\t" \
  "s_add_i32 s99, s99, -125\n\t" \
  "s_max_i32 s98, s98, s99\n\t" \
  "s_max_i32 s98, s98, -100\n\t" \
  "s_min_i32 s98, s98, 128\n\t" \
  "s_add_i32 s77, s98, 105\n\t" \
  "s_lshl_b32 s77, s77, 23\n\t" \
  "s_sub_i32 s99, 13, s98\n\t" \
  "v_add_u32 v43, s99, v42\n\t" \
  "v_mul_f32 v28, v28, v45\n\t" \
  "v_mul_f32 v29, v29, v45\n\t" \
  "s_mov_b32 exec_hi, 0\n\t" \
  "v_sub_u32 v43, 2, v42\n\t" \
  "v_mov_b32 v28, 1.0\n\t" \
  "v_mov_b32 v29, 0\n\t" \
  "s_mov_b32 exec_hi, -1\n\t" \
  "v_ldexp_f32 v28, v28, v43\n\t" \
  "v_ldexp_f32 v29, v29, v43\n\t" \
  "s_add_i32 s99, s99, 2\n\t" \
  "v_mov_b32 v43, s99\n\t" \
  "s_mov_b32 s75, 8\n\t" \
  "s_cmp_lg_u32 %[wk], 0\n\t" \
  "s_cbranch_scc1 80b\n\t" \
  "s_mov_b64 exec, 1\n\t" \
  "v_ldexp_f32 v15, %[sd], v43\n\t" \
  "s_mov_b64 exec, -1\n\t" \
  "s_branch 80b\n\t" \
  "85:\n\t" \
  "s_sleep 1\n\t" \
  "ds_read_b32 v34, v25 offset:16\n\t" \
  "ds_read_b128 v[36:39], v5\n\t" \
  "ds_read_b32 v35, v5 offset:3072\n\t" \
  "s_waitcnt lgkmcnt(0)\n\t" \
  "v_cmp_ne_u32 vcc, %[tag], v34\n\t" \
  "s_cbranch_vccnz 85b\n\t" \
  "s_branch 86b\n\t"

#define NEAR_ASM \
  "v_and_b32 v1, 63, v0\n\t" \
  "s_mov_b32 s98, 157600\n\t" \
  "v_lshl_add_u32 v2, v1, 4, s98\n\t" \
  "ds_read_b128 v[4:7], v2 offset:0\n\t" \
  "v_lshl_add_u32 v2, v1, 4, %[opc]\n\t" \
  "v_lshl_add_u32 v3, v1, 3, %[opc]\n\t" \
  "v_and_b32 v8, 1, v0\n\t" \
  "v_lshlrev_b32 v8, 2, v8\n\t" \
  "v_mov_b32 v9, 1\n\t" \
  "s_waitcnt lgkmcnt(0)\n\t" \
  "v_add_u32 v4, %[chb], v4\n\t" \
  "v_add_u32 v5, %[chb], v5\n\t" \
  "v_add_u32 v6, %[chb], v6\n\t" \
  "ds_read_b64 v[152:153], v7 offset:256\n\t" \
  "ds_read_b64 v[154:155], v7 offset:264\n\t" \
  "ds_read_b64 v[156:157], v7 offset:384\n\t" \
  "ds_read_b64 v[158:159], v7 offset:392\n\t" \
  "1:\n\t" \
  "ds_read_b32 v160, v11 offset:32\n\t" \
  "s_waitcnt lgkmcnt(0)\n\t" \
  "v_cmp_gt_u32 vcc, 4, v160\n\t" \
  "s_cbranch_vccz 2f\n\t" \
  "s_sleep 1\n\t" \
  "s_branch 1b\n\t" \
  "2:\n\t" \
  "ds_read_b64 v[88:89], v6 offset:1880\n\t" \
  "ds_read_b64 v[90:91], v6 offset:1720\n\t" \
  "ds_read_b64 v[92:93], v6 offset:1848\n\t" \
  "ds_read_b64 v[94:95], v6 offset:1688\n\t" \
  "ds_read_b64 v[96:97], v6 offset:1816\n\t" \
  "ds_read_b64 v[98:99], v6 offset:1656\n\t" \
  "ds_read_b64 v[100:101], v6 offset:1784\n\t" \
  "ds_read_b64 v[102:103], v6 offset:1624\n\t" \
  "ds_read_b64 v[16:17], v5 offset:1112\n\t" \
  "ds_read_b64 v[18:19], v5 offset:1120\n\t" \
  "ds_read_b64 v[20:21], v5 offset:1048\n\t" \
  "ds_read_b64 v[22:23], v5 offset:1056\n\t" \
  "ds_read_b64 v[24:25], v5 offset:984\n\t" \
  "ds_read_b64 v[26:27], v5 offset:992\n\t" \
  "ds_read_b64 v[28:29], v5 offset:920\n\t" \
  "ds_read_b64 v[30:31], v5 offset:928\n\t" \
  "ds_read_b64 v[32:33], v5 offset:856\n\t" \
  "ds_read_b64 v[34:35], v5 offset:864\n\t" \
  "ds_read_b64 v[36:37], v5 offset:792\n\t" \
  "ds_read_b64 v[38:39], v5 offset:800\n\t" \
  "ds_read_b64 v[40:41], v5 offset:728\n\t" \
  "ds_read_b64 v[42:43], v5 offset:736\n\t" \
  "ds_read_b64 v[44:45], v5 offset:664\n\t" \
  "ds_read_b64 v[46:47], v5 offset:672\n\t" \
  "ds_read_b64 v[48:49], v5 offset:600\n\t" \
  "ds_read_b64 v[50:51], v5 offset:608\n\t" \
  "ds_read_b64 v[52:53], v5 offset:536\n\t" \
  "ds_read_b64 v[54:55], v5 offset:544\n\t" \
  "ds_read_b64 v[56:57], v5 offset:472\n\t" \
  "ds_read_b64 v[58:59], v5 offset:480\n\t" \
  "ds_read_b64 v[60:61], v5 offset:408\n\t" \
  "ds_read_b64 v[62:63], v5 offset:416\n\t" \
  "ds_read_b64 v[64:65], v5 offset:344\n\t" \
  "ds_read_b64 v[66:67], v5 offset:352\n\t" \
  "ds_read_b64 v[68:69], v5 offset:280\n\t" \
  "ds_read_b64 v[70:71], v5 offset:288\n\t" \
  "ds_read_b64 v[72:73], v5 offset:216\n\t" \
  "ds_read_b64 v[74:75], v5 offset:224\n\t" \
  "ds_read_b64 v[76:77], v5 offset:152\n\t" \
  "ds_read_b64 v[78:79], v5 offset:160\n\t" \
  "ds_read_b64 v[80:81], v5 offset:88\n\t" \
  "ds_read_b64 v[82:83], v5 offset:96\n\t" \
  "ds_read_b64 v[84:85], v5 offset:24\n\t" \
  "ds_read_b64 v[86:87], v5 offset:32\n\t" \
  TSQ(7) \
  "3:\n\t" \
  "ds_read_b32 v160, v8 offset:64\n\t" \
  "s_waitcnt lgkmcnt(0)\n\t" \
  "v_cmp_gt_u32 vcc, 1, v160\n\t" \
  "s_cbranch_vccz 4f\n\t" \
  "s_sleep 1\n\t" \
  "s_branch 3b\n\t" \
  "4:\n\t" \
  TSQ(8) \
  "s_setprio 3\n\t" \
  "ds_read_b128 v[120:123], v4 offset:448\n\t" \
  "ds_read_b128 v[124:127], v4 offset:432\n\t" \
  "ds_read_b128 v[128:131], v4 offset:960\n\t" \
  "ds_read_b128 v[132:135], v4 offset:944\n\t" \
  "s_waitcnt lgkmcnt(3)\n\t" \
  "v_mfma_f32_16x16x32_f16 v[120:123], v[120:123], v[152:155], 0\n\t" \
  "ds_read_b128 v[136:139], v4 offset:0\n\t" \
  "s_waitcnt lgkmcnt(3)\n\t" \
  "v_mfma_f32_16x16x32_f16 v[124:127], v[124:127], v[156:159], 0\n\t" \
  "ds_read_b128 v[140:143], v4 offset:64\n\t" \
  "s_waitcnt lgkmcnt(3)\n\t" \
  "v_mfma_f32_16x16x32_f16 v[128:131], v[128:131], v[152:155], 0\n\t" \
  "ds_read_b128 v[144:147], v4 offset:128\n\t" \
  "s_waitcnt lgkmcnt(3)\n\t" \
  "v_mfma_f32_16x16x32_f16 v[132:135], v[132:135], v[156:159], 0\n\t" \
  "ds_read_b128 v[148:151], v4 offset:192\n\t" \
  "v_cvt_pk_f16_f32 v120, v120, v121\n\t" \
  "v_cvt_pk_f16_f32 v121, v122, v123\n\t" \
  "v_cvt_pk_f16_f32 v122, v124, v125\n\t" \
  "v_cvt_pk_f16_f32 v123, v126, v127\n\t" \
  "s_nop 1\n\t" \
  "v_mfma_f32_16x16x32_f16 v[116:119], v[120:123], v[100:103], 0\n\t" \
  "v_cvt_pk_f16_f32 v128, v128, v129\n\t" \
  "v_cvt_pk_f16_f32 v129, v130, v131\n\t" \
  "v_mfma_f32_16x16x32_f16 v[112:115], v[120:123], v[96:99], 0\n\t" \
  "v_cvt_pk_f16_f32 v130, v132, v133\n\t" \
  "v_cvt_pk_f16_f32 v131, v134, v135\n\t" \
  "v_mfma_f32_16x16x32_f16 v[108:111], v[120:123], v[92:95], 0\n\t" \
  "v_mfma_f32_16x16x32_f16 v[104:107], v[120:123], v[88:91], 0\n\t" \
  "ds_read_b32 v164, v8 offset:72\n\t" \
  "v_mfma_f32_16x16x32_f16 v[116:119], v[128:131], v[96:99], v[116:119]\n\t" \
  "v_mfma_f32_16x16x32_f16 v[112:115], v[128:131], v[92:95], v[112:115]\n\t" \
  "v_mfma_f32_16x16x32_f16 v[108:111], v[128:131], v[88:91], v[108:111]\n\t" \
  "s_waitcnt lgkmcnt(3)\n\t" \
  "v_mfma_f32_16x16x32_f16 v[100:103], v[44:47], v[136:139], 0\n\t" \
  "s_waitcnt lgkmcnt(0)\n\t" \
  "v_cmp_eq_u32 vcc, 0, v164\n\t" \
  "v_mfma_f32_16x16x32_f16 v[96:99], v[76:79], v[136:139], 0\n\t" \
  "ds_read_b128 v[136:139], v4 offset:256\n\t" \
  "s_cbranch_vccnz 50f\n\t" \
  "51:\n\t" \
  "v_mfma_f32_16x16x32_f16 v[100:103], v[40:43], v[140:143], v[100:103]\n\t" \
  "ds_read_b128 v[120:123], v4 offset:1472\n\t" \
  "ds_read_b128 v[124:127], v4 offset:1456\n\t" \
  "v_mfma_f32_16x16x32_f16 v[96:99], v[72:75], v[140:143], v[96:99]\n\t" \
  "ds_read_b128 v[140:143], v4 offset:320\n\t" \
  "ds_read_b128 v[128:131], v4 offset:1984\n\t" \
  "v_mfma_f32_16x16x32_f16 v[100:103], v[36:39], v[144:147], v[100:103]\n\t" \
  "ds_read_b128 v[132:135], v4 offset:1968\n\t" \
  "v_mfma_f32_16x16x32_f16 v[96:99], v[68:71], v[144:147], v[96:99]\n\t" \
  "ds_read_b128 v[144:147], v4 offset:384\n\t" \
  "v_mfma_f32_16x16x32_f16 v[100:103], v[32:35], v[148:151], v[100:103]\n\t" \
  "v_mfma_f32_16x16x32_f16 v[96:99], v[64:67], v[148:151], v[96:99]\n\t" \
  "ds_read_b128 v[148:151], v4 offset:448\n\t" \
  "s_waitcnt lgkmcnt(7)\n\t" \
  "v_mfma_f32_16x16x32_f16 v[100:103], v[28:31], v[136:139], v[100:103]\n\t" \
  "v_mfma_f32_16x16x32_f16 v[96:99], v[60:63], v[136:139], v[96:99]\n\t" \
  "ds_read_b128 v[136:139], v4 offset:768\n\t" \
  "s_waitcnt lgkmcnt(7)\n\t" \
  "v_mfma_f32_16x16x32_f16 v[120:123], v[120:123], v[152:155], 0\n\t" \
  "s_waitcnt lgkmcnt(6)\n\t" \
  "v_mfma_f32_16x16x32_f16 v[124:127], v[124:127], v[156:159], 0\n\t" \
  "s_waitcnt lgkmcnt(4)\n\t" \
  "v_mfma_f32_16x16x32_f16 v[128:131], v[128:131], v[152:155], 0\n\t" \
  "s_nop 0\n\t" \
  "s_waitcnt lgkmcnt(3)\n\t" \
  "v_mfma_f32_16x16x32_f16 v[132:135], v[132:135], v[156:159], 0\n\t" \
  "s_nop 0\n\t" \
  "v_cvt_pk_f16_f32 v120, v120, v121\n\t" \
  "v_mfma_f32_16x16x32_f16 v[100:103], v[24:27], v[140:143], v[100:103]\n\t" \
  "v_cvt_pk_f16_f32 v121, v122, v123\n\t" \
  "v_cvt_pk_f16_f32 v122, v124, v125\n\t" \
  "v_mfma_f32_16x16x32_f16 v[96:99], v[56:59], v[140:143], v[96:99]\n\t" \
  "ds_read_b128 v[140:143], v4 offset:832\n\t" \
  "s_waitcnt lgkmcnt(3)\n\t" \
  "v_mfma_f32_16x16x32_f16 v[100:103], v[20:23], v[144:147], v[100:103]\n\t" \
  "v_cvt_pk_f16_f32 v123, v126, v127\n\t" \
  "ds_read_b128 v[124:127], v4 offset:512\n\t" \
  "v_mfma_f32_16x16x32_f16 v[96:99], v[52:55], v[144:147], v[96:99]\n\t" \
  "ds_read_b128 v[144:147], v4 offset:896\n\t" \
  "v_cvt_pk_f16_f32 v128, v128, v129\n\t" \
  "v_mfma_f32_16x16x32_f16 v[116:119], v[120:123], v[92:95], v[116:119]\n\t" \
  "v_cvt_pk_f16_f32 v129, v130, v131\n\t" \
  "v_cvt_pk_f16_f32 v130, v132, v133\n\t" \
  "v_mfma_f32_16x16x32_f16 v[112:115], v[120:123], v[88:91], v[112:115]\n\t" \
  "ds_read_b128 v[120:123], v4 offset:640\n\t" \
  "v_cvt_pk_f16_f32 v131, v134, v135\n\t" \
  "s_nop 1\n\t" \
  "v_mfma_f32_16x16x32_f16 v[116:119], v[128:131], v[88:91], v[116:119]\n\t" \
  "ds_read_b128 v[128:131], v4 offset:704\n\t" \
  "s_waitcnt lgkmcnt(5)\n\t" \
  "v_mfma_f32_16x16x32_f16 v[100:103], v[16:19], v[148:151], v[100:103]\n\t" \
  "ds_read_b128 v[132:135], v4 offset:576\n\t" \
  "v_mfma_f32_16x16x32_f16 v[96:99], v[48:51], v[148:151], v[96:99]\n\t" \
  "v_mfma_f32_16x16x32_f16 v[92:95], v[80:83], v[148:151], 0\n\t" \
  "v_mfma_f32_16x16x32_f16 v[104:107], v[84:87], v[148:151], v[104:107]\n\t" \
  "ds_read_b128 v[148:151], v4 offset:960\n\t" \
  "s_waitcnt lgkmcnt(5)\n\t" \
  "v_mfma_f32_16x16x32_f16 v[96:99], v[44:47], v[124:127], v[96:99]\n\t" \
  "v_mfma_f32_16x16x32_f16 v[92:95], v[76:79], v[124:127], v[92:95]\n\t" \
  "ds_read_b128 v[124:127], v4 offset:1024\n\t" \
  "s_waitcnt lgkmcnt(2)\n\t" \
  "v_mfma_f32_16x16x32_f16 v[96:99], v[40:43], v[132:135], v[96:99]\n\t" \
  "v_mul_f32 v156, s77, v100\n\t" \
  "v_mul_f32 v157, s77, v101\n\t" \
  "v_mfma_f32_16x16x32_f16 v[92:95], v[72:75], v[132:135], v[92:95]\n\t" \
  "ds_read_b128 v[132:135], v4 offset:1088\n\t" \
  "v_mul_f32 v158, s77, v102\n\t" \
  "v_mfma_f32_16x16x32_f16 v[96:99], v[36:39], v[120:123], v[96:99]\n\t" \
  "v_mul_f32 v159, s77, v103\n\t" \
  "v_cvt_pk_bf16_f32 v156, v156, v157\n\t" \
  "v_mfma_f32_16x16x32_f16 v[92:95], v[68:71], v[120:123], v[92:95]\n\t" \
  "ds_read_b128 v[120:123], v4 offset:1152\n\t" \
  "v_cvt_pk_bf16_f32 v157, v158, v159\n\t" \
  "v_mfma_f32_16x16x32_f16 v[96:99], v[32:35], v[128:131], v[96:99]\n\t" \
  "ds_write_b64 v3, v[156:157] offset:8192\n\t" \
  "ds_write_b32 v11, v9 offset:128\n\t" \
  "v_mfma_f32_16x16x32_f16 v[92:95], v[64:67], v[128:131], v[92:95]\n\t" \
  "ds_read_b128 v[128:131], v4 offset:1216\n\t" \
  "v_mfma_f32_16x16x32_f16 v[96:99], v[28:31], v[136:139], v[96:99]\n\t" \
  "v_mfma_f32_16x16x32_f16 v[92:95], v[60:63], v[136:139], v[92:95]\n\t" \
  "ds_read_b128 v[136:139], v4 offset:1280\n\t" \
  "ds_read_b32 v164, v8 offset:80\n\t" \
  "v_mfma_f32_16x16x32_f16 v[96:99], v[24:27], v[140:143], v[96:99]\n\t" \
  "v_mfma_f32_16x16x32_f16 v[92:95], v[56:59], v[140:143], v[92:95]\n\t" \
  "ds_read_b128 v[140:143], v4 offset:1344\n\t" \
  "v_mfma_f32_16x16x32_f16 v[96:99], v[20:23], v[144:147], v[96:99]\n\t" \
  "v_mfma_f32_16x16x32_f16 v[92:95], v[52:55], v[144:147], v[92:95]\n\t" \
  "ds_read_b128 v[144:147], v4 offset:1408\n\t" \
  "s_waitcnt lgkmcnt(8)\n\t" \
  "v_mfma_f32_16x16x32_f16 v[96:99], v[16:19], v[148:151], v[96:99]\n\t" \
  "v_mfma_f32_16x16x32_f16 v[92:95], v[48:51], v[148:151], v[92:95]\n\t" \
  "v_mfma_f32_16x16x32_f16 v[88:91], v[80:83], v[148:151], 0\n\t" \
  "v_mfma_f32_16x16x32_f16 v[108:111], v[84:87], v[148:151], v[108:111]\n\t" \
  "ds_read_b128 v[148:151], v4 offset:1472\n\t" \
  "v_mfma_f32_16x16x32_f16 v[92:95], v[44:47], v[124:127], v[92:95]\n\t" \
  "v_mfma_f32_16x16x32_f16 v[88:91], v[76:79], v[124:127], v[88:91]\n\t" \
  "ds_read_b128 v[124:127], v4 offset:1536\n\t" \
  "s_nop 0\n\t" \
  "v_mfma_f32_16x16x32_f16 v[92:95], v[40:43], v[132:135], v[92:95]\n\t" \
  "v_mul_f32 v156, s77, v96\n\t" \
  "v_mul_f32 v157, s77, v97\n\t" \
  "v_mfma_f32_16x16x32_f16 v[88:91], v[72:75], v[132:135], v[88:91]\n\t" \
  "ds_read_b128 v[132:135], v4 offset:1600\n\t" \
  "s_waitcnt lgkmcnt(4)\n\t" \
  "v_mfma_f32_16x16x32_f16 v[92:95], v[36:39], v[120:123], v[92:95]\n\t" \
  "v_mul_f32 v158, s77, v98\n\t" \
  "v_mul_f32 v159, s77, v99\n\t" \
  "v_mfma_f32_16x16x32_f16 v[88:91], v[68:71], v[120:123], v[88:91]\n\t" \
  "ds_read_b128 v[120:123], v4 offset:1664\n\t" \
  "v_cvt_pk_bf16_f32 v156, v156, v157\n\t" \
  "v_mfma_f32_16x16x32_f16 v[92:95], v[32:35], v[128:131], v[92:95]\n\t" \
  "v_cvt_pk_bf16_f32 v157, v158, v159\n\t" \
  "ds_write_b64 v3, v[156:157] offset:8704\n\t" \
  "v_mfma_f32_16x16x32_f16 v[88:91], v[64:67], v[128:131], v[88:91]\n\t" \
  "ds_read_b128 v[128:131], v4 offset:1728\n\t" \
  "ds_write_b32 v11, v9 offset:144\n\t" \
  "v_mfma_f32_16x16x32_f16 v[92:95], v[28:31], v[136:139], v[92:95]\n\t" \
  "v_mfma_f32_16x16x32_f16 v[88:91], v[60:63], v[136:139], v[88:91]\n\t" \
  "ds_read_b128 v[136:139], v4 offset:1792\n\t" \
  "v_mfma_f32_16x16x32_f16 v[92:95], v[24:27], v[140:143], v[92:95]\n\t" \
  "v_mfma_f32_16x16x32_f16 v[88:91], v[56:59], v[140:143], v[88:91]\n\t" \
  "ds_read_b128 v[140:143], v4 offset:1856\n\t" \
  "s_waitcnt lgkmcnt(7)\n\t" \
  "v_mfma_f32_16x16x32_f16 v[92:95], v[20:23], v[144:147], v[92:95]\n\t" \
  "v_cmp_eq_u32 vcc, 0, v164\n\t" \
  "s_cbranch_vccnz 52f\n\t" \
  "53:\n\t" \
  "v_mfma_f32_16x16x32_f16 v[88:91], v[52:55], v[144:147], v[88:91]\n\t" \
  "ds_read_b128 v[144:147], v4 offset:1920\n\t" \
  "ds_read_b32 v160, v11 offset:256\n\t" \
  "v_mfma_f32_16x16x32_f16 v[92:95], v[16:19], v[148:151], v[92:95]\n\t" \
  "ds_read_b128 v[152:155], v2 offset:0\n\t" \
  "v_mfma_f32_16x16x32_f16 v[88:91], v[48:51], v[148:151], v[88:91]\n\t" \
  "v_mfma_f32_16x16x32_f16 v[104:107], v[80:83], v[148:151], v[104:107]\n\t" \
  "v_mfma_f32_16x16x32_f16 v[112:115], v[84:87], v[148:151], v[112:115]\n\t" \
  "ds_read_b128 v[148:151], v4 offset:1984\n\t" \
  "v_mfma_f32_16x16x32_f16 v[88:91], v[44:47], v[124:127], v[88:91]\n\t" \
  "v_mfma_f32_16x16x32_f16 v[104:107], v[76:79], v[124:127], v[104:107]\n\t" \
  "ds_read_b128 v[124:127], v4 offset:2048\n\t" \
  "s_waitcnt lgkmcnt(6)\n\t" \
  "v_mfma_f32_16x16x32_f16 v[88:91], v[40:43], v[132:135], v[88:91]\n\t" \
  "v_mfma_f32_16x16x32_f16 v[104:107], v[72:75], v[132:135], v[104:107]\n\t" \
  "ds_read_b128 v[132:135], v4 offset:2112\n\t" \
  "v_mfma_f32_16x16x32_f16 v[88:91], v[36:39], v[120:123], v[88:91]\n\t" \
  "v_mfma_f32_16x16x32_f16 v[104:107], v[68:71], v[120:123], v[104:107]\n\t" \
  "ds_read_b128 v[120:123], v4 offset:2176\n\t" \
  "v_mfma_f32_16x16x32_f16 v[88:91], v[32:35], v[128:131], v[88:91]\n\t" \
  "v_mfma_f32_16x16x32_f16 v[104:107], v[64:67], v[128:131], v[104:107]\n\t" \
  "ds_read_b128 v[128:131], v4 offset:2240\n\t" \
  "v_mfma_f32_16x16x32_f16 v[88:91], v[28:31], v[136:139], v[88:91]\n\t" \
  "s_waitcnt lgkmcnt(5)\n\t" \
  "v_fma_f32 v156, v92, s77, v152\n\t" \
  "v_mfma_f32_16x16x32_f16 v[104:107], v[60:63], v[136:139], v[104:107]\n\t" \
  "ds_read_b128 v[136:139], v4 offset:2304\n\t" \
  "v_fma_f32 v157, v93, s77, v153\n\t" \
  "v_mfma_f32_16x16x32_f16 v[88:91], v[24:27], v[140:143], v[88:91]\n\t" \
  "v_fma_f32 v158, v94, s77, v154\n\t" \
  "v_fma_f32 v159, v95, s77, v155\n\t" \
  "v_mfma_f32_16x16x32_f16 v[104:107], v[56:59], v[140:143], v[104:107]\n\t" \
  "ds_read_b128 v[140:143], v4 offset:2368\n\t" \
  "v_cvt_pk_bf16_f32 v156, v156, v157\n\t" \
  "v_mfma_f32_16x16x32_f16 v[88:91], v[20:23], v[144:147], v[88:91]\n\t" \
  "v_cvt_pk_bf16_f32 v157, v158, v159\n\t" \
  "ds_write_b64 v3, v[156:157] offset:9216\n\t" \
  "v_mfma_f32_16x16x32_f16 v[104:107], v[52:55], v[144:147], v[104:107]\n\t" \
  "ds_read_b128 v[144:147], v4 offset:2432\n\t" \
  "s_waitcnt lgkmcnt(6)\n\t" \
  "v_mfma_f32_16x16x32_f16 v[88:91], v[16:19], v[148:151], v[88:91]\n\t" \
  "ds_write_b32 v11, v160 offset:160\n\t" \
  "ds_read_b32 v164, v8 offset:88\n\t" \
  "v_mfma_f32_16x16x32_f16 v[104:107], v[48:51], v[148:151], v[104:107]\n\t" \
  "v_mfma_f32_16x16x32_f16 v[108:111], v[80:83], v[148:151], v[108:111]\n\t" \
  "v_mfma_f32_16x16x32_f16 v[116:119], v[84:87], v[148:151], v[116:119]\n\t" \
  "ds_read_b128 v[148:151], v4 offset:2496\n\t" \
  "ds_read_b128 v[84:87], v4 offset:3008\n\t" \
  "v_mfma_f32_16x16x32_f16 v[104:107], v[44:47], v[124:127], v[104:107]\n\t" \
  "ds_read_b64 v[96:97], v7 offset:0\n\t" \
  "ds_read_b64 v[98:99], v7 offset:8\n\t" \
  "v_mfma_f32_16x16x32_f16 v[108:111], v[76:79], v[124:127], v[108:111]\n\t" \
  "ds_read_b128 v[124:127], v4 offset:2560\n\t" \
  "ds_read_b64 v[100:101], v7 offset:128\n\t" \
  "v_mfma_f32_16x16x32_f16 v[104:107], v[40:43], v[132:135], v[104:107]\n\t" \
  "ds_read_b64 v[102:103], v7 offset:136\n\t" \
  "ds_read_b32 v161, v11 offset:272\n\t" \
  "v_mfma_f32_16x16x32_f16 v[108:111], v[72:75], v[132:135], v[108:111]\n\t" \
  "ds_read_b128 v[132:135], v4 offset:2624\n\t" \
  "s_waitcnt lgkmcnt(13)\n\t" \
  "v_mfma_f32_16x16x32_f16 v[104:107], v[36:39], v[120:123], v[104:107]\n\t" \
  "ds_read_b128 v[152:155], v2 offset:1024\n\t" \
  "v_mfma_f32_16x16x32_f16 v[108:111], v[68:71], v[120:123], v[108:111]\n\t" \
  "ds_read_b128 v[120:123], v4 offset:2688\n\t" \
  "v_mfma_f32_16x16x32_f16 v[104:107], v[32:35], v[128:131], v[104:107]\n\t" \
  "v_mfma_f32_16x16x32_f16 v[108:111], v[64:67], v[128:131], v[108:111]\n\t" \
  "ds_read_b128 v[128:131], v4 offset:2752\n\t" \
  "v_mfma_f32_16x16x32_f16 v[104:107], v[28:31], v[136:139], v[104:107]\n\t" \
  "s_waitcnt lgkmcnt(12)\n\t" \
  "v_cmp_eq_u32 vcc, 0, v164\n\t" \
  "v_mfma_f32_16x16x32_f16 v[108:111], v[60:63], v[136:139], v[108:111]\n\t" \
  "ds_read_b128 v[136:139], v4 offset:2816\n\t" \
  "s_waitcnt lgkmcnt(9)\n\t" \
  "v_mfma_f32_16x16x32_f16 v[96:99], v[84:87], v[96:99], 0\n\t" \
  "s_cbranch_vccnz 54f\n\t" \
  "55:\n\t" \
  "s_waitcnt lgkmcnt(6)\n\t" \
  "v_mfma_f32_16x16x32_f16 v[100:103], v[84:87], v[100:103], 0\n\t" \
  "v_mfma_f32_16x16x32_f16 v[104:107], v[24:27], v[140:143], v[104:107]\n\t" \
  "v_mfma_f32_16x16x32_f16 v[108:111], v[56:59], v[140:143], v[108:111]\n\t" \
  "ds_read_b128 v[140:143], v4 offset:2880\n\t" \
  "s_nop 0\n\t" \
  "v_mfma_f32_16x16x32_f16 v[104:107], v[20:23], v[144:147], v[104:107]\n\t" \
  "v_cvt_pk_f16_f32 v84, v96, v97\n\t" \
  "v_cvt_pk_f16_f32 v85, v98, v99\n\t" \
  "v_mfma_f32_16x16x32_f16 v[108:111], v[52:55], v[144:147], v[108:111]\n\t" \
  "ds_read_b128 v[144:147], v4 offset:2944\n\t" \
  "v_cvt_pk_f16_f32 v86, v100, v101\n\t" \
  "v_mfma_f32_16x16x32_f16 v[104:107], v[16:19], v[148:151], v[104:107]\n\t" \
  "v_cvt_pk_f16_f32 v87, v102, v103\n\t" \
  "ds_read_b64 v[96:97], v6 offset:1560\n\t" \
  "v_mfma_f32_16x16x32_f16 v[108:111], v[48:51], v[148:151], v[108:111]\n\t" \
  "ds_read_b64 v[98:99], v6 offset:1464\n\t" \
  "v_mfma_f32_16x16x32_f16 v[112:115], v[80:83], v[148:151], v[112:115]\n\t" \
  "ds_read_b128 v[148:151], v4 offset:3008\n\t" \
  "v_mfma_f32_16x16x32_f16 v[108:111], v[44:47], v[124:127], v[108:111]\n\t" \
  "s_waitcnt lgkmcnt(8)\n\t" \
  "v_fma_f32 v156, v88, s77, v152\n\t" \
  "v_mfma_f32_16x16x32_f16 v[112:115], v[76:79], v[124:127], v[112:115]\n\t" \
  "ds_read_b128 v[124:127], v4 offset:3072\n\t" \
  "v_fma_f32 v157, v89, s77, v153\n\t" \
  "v_mfma_f32_16x16x32_f16 v[108:111], v[40:43], v[132:135], v[108:111]\n\t" \
  "v_fma_f32 v158, v90, s77, v154\n\t" \
  "v_fma_f32 v159, v91, s77, v155\n\t" \
  "v_mfma_f32_16x16x32_f16 v[112:115], v[72:75], v[132:135], v[112:115]\n\t" \
  "ds_read_b128 v[132:135], v4 offset:3136\n\t" \
  "s_waitcnt lgkmcnt(6)\n\t" \
  "v_mfma_f32_16x16x32_f16 v[108:111], v[36:39], v[120:123], v[108:111]\n\t" \
  "v_cvt_pk_bf16_f32 v156, v156, v157\n\t" \
  "v_cvt_pk_bf16_f32 v157, v158, v159\n\t" \
  "v_mfma_f32_16x16x32_f16 v[112:115], v[68:71], v[120:123], v[112:115]\n\t" \
  "ds_read_b128 v[120:123], v4 offset:3200\n\t" \
  "ds_write_b64 v3, v[156:157] offset:9728\n\t" \
  "v_mfma_f32_16x16x32_f16 v[108:111], v[32:35], v[128:131], v[108:111]\n\t" \
  "ds_write_b32 v11, v161 offset:176\n\t" \
  "v_mfma_f32_16x16x32_f16 v[112:115], v[64:67], v[128:131], v[112:115]\n\t" \
  "ds_read_b128 v[128:131], v4 offset:3264\n\t" \
  "s_waitcnt lgkmcnt(7)\n\t" \
  "v_mfma_f32_16x16x32_f16 v[116:119], v[84:87], v[96:99], v[116:119]\n\t" \
  "ds_read_b128 v[84:87], v4 offset:2992\n\t" \
  "ds_read_b64 v[96:97], v7 offset:0\n\t" \
  "v_mfma_f32_16x16x32_f16 v[108:111], v[28:31], v[136:139], v[108:111]\n\t" \
  "ds_read_b64 v[98:99], v7 offset:8\n\t" \
  "ds_read_b64 v[100:101], v7 offset:128\n\t" \
  "v_mfma_f32_16x16x32_f16 v[112:115], v[60:63], v[136:139], v[112:115]\n\t" \
  "ds_read_b128 v[136:139], v4 offset:3328\n\t" \
  "ds_read_b64 v[102:103], v7 offset:136\n\t" \
  "v_mfma_f32_16x16x32_f16 v[108:111], v[24:27], v[140:143], v[108:111]\n\t" \
  "ds_read_b32 v162, v11 offset:288\n\t" \
  "ds_read_b128 v[152:155], v2 offset:2048\n\t" \
  "v_mfma_f32_16x16x32_f16 v[112:115], v[56:59], v[140:143], v[112:115]\n\t" \
  "ds_read_b128 v[140:143], v4 offset:3392\n\t" \
  "v_mfma_f32_16x16x32_f16 v[108:111], v[20:23], v[144:147], v[108:111]\n\t" \
  "v_mfma_f32_16x16x32_f16 v[112:115], v[52:55], v[144:147], v[112:115]\n\t" \
  "ds_read_b128 v[144:147], v4 offset:3456\n\t" \
  "s_waitcnt lgkmcnt(13)\n\t" \
  "v_mfma_f32_16x16x32_f16 v[108:111], v[16:19], v[148:151], v[108:111]\n\t" \
  "v_mfma_f32_16x16x32_f16 v[112:115], v[48:51], v[148:151], v[112:115]\n\t" \
  "v_mfma_f32_16x16x32_f16 v[116:119], v[80:83], v[148:151], v[116:119]\n\t" \
  "ds_read_b128 v[148:151], v4 offset:3520\n\t" \
  "v_mfma_f32_16x16x32_f16 v[112:115], v[44:47], v[124:127], v[112:115]\n\t" \
  "v_mfma_f32_16x16x32_f16 v[116:119], v[76:79], v[124:127], v[116:119]\n\t" \
  "ds_read_b128 v[124:127], v4 offset:3584\n\t" \
  "s_waitcnt lgkmcnt(6)\n\t" \
  "v_mfma_f32_16x16x32_f16 v[96:99], v[84:87], v[96:99], 0\n\t" \
  "v_mfma_f32_16x16x32_f16 v[100:103], v[84:87], v[100:103], 0\n\t" \
  "s_waitcnt lgkmcnt(4)\n\t" \
  "v_fma_f32 v156, v104, s77, v152\n\t" \
  "v_mfma_f32_16x16x32_f16 v[112:115], v[40:43], v[132:135], v[112:115]\n\t" \
  "v_fma_f32 v157, v105, s77, v153\n\t" \
  "v_fma_f32 v158, v106, s77, v154\n\t" \
  "v_mfma_f32_16x16x32_f16 v[116:119], v[72:75], v[132:135], v[116:119]\n\t" \
  "ds_read_b128 v[132:135], v4 offset:3648\n\t" \
  "v_fma_f32 v159, v107, s77, v155\n\t" \
  "v_mfma_f32_16x16x32_f16 v[112:115], v[36:39], v[120:123], v[112:115]\n\t" \
  "v_cvt_pk_bf16_f32 v156, v156, v157\n\t" \
  "v_cvt_pk_bf16_f32 v157, v158, v159\n\t" \
  "v_mfma_f32_16x16x32_f16 v[116:119], v[68:71], v[120:123], v[116:119]\n\t" \
  "ds_read_b128 v[120:123], v4 offset:3712\n\t" \
  "ds_write_b64 v3, v[156:157] offset:10240\n\t" \
  "v_mfma_f32_16x16x32_f16 v[112:115], v[32:35], v[128:131], v[112:115]\n\t" \
  "ds_write_b32 v11, v162 offset:192\n\t" \
  "v_cvt_pk_f16_f32 v84, v96, v97\n\t" \
  "v_mfma_f32_16x16x32_f16 v[116:119], v[64:67], v[128:131], v[116:119]\n\t" \
  "ds_read_b128 v[128:131], v4 offset:3776\n\t" \
  "v_cvt_pk_f16_f32 v85, v98, v99\n\t" \
  "v_mfma_f32_16x16x32_f16 v[112:115], v[28:31], v[136:139], v[112:115]\n\t" \
  "v_cvt_pk_f16_f32 v86, v100, v101\n\t" \
  "v_cvt_pk_f16_f32 v87, v102, v103\n\t" \
  "v_mfma_f32_16x16x32_f16 v[116:119], v[60:63], v[136:139], v[116:119]\n\t" \
  "ds_read_b128 v[136:139], v4 offset:3840\n\t" \
  "s_waitcnt lgkmcnt(6)\n\t" \
  "v_mfma_f32_16x16x32_f16 v[112:115], v[24:27], v[140:143], v[112:115]\n\t" \
  "ds_read_b64 v[96:97], v6 offset:1368\n\t" \
  "ds_read_b64 v[98:99], v6 offset:1272\n\t" \
  "v_mfma_f32_16x16x32_f16 v[116:119], v[56:59], v[140:143], v[116:119]\n\t" \
  "ds_read_b128 v[140:143], v4 offset:3904\n\t" \
  "ds_read_b32 v163, v11 offset:304\n\t" \
  "v_mfma_f32_16x16x32_f16 v[112:115], v[20:23], v[144:147], v[112:115]\n\t" \
  "ds_read_b128 v[152:155], v2 offset:3072\n\t" \
  "v_mfma_f32_16x16x32_f16 v[116:119], v[52:55], v[144:147], v[116:119]\n\t" \
  "ds_read_b128 v[144:147], v4 offset:3968\n\t" \
  "v_mfma_f32_16x16x32_f16 v[112:115], v[16:19], v[148:151], v[112:115]\n\t" \
  "v_mfma_f32_16x16x32_f16 v[116:119], v[48:51], v[148:151], v[116:119]\n\t" \
  "ds_read_b128 v[148:151], v4 offset:4032\n\t" \
  "s_waitcnt lgkmcnt(5)\n\t" \
  "v_mfma_f32_16x16x32_f16 v[116:119], v[84:87], v[96:99], v[116:119]\n\t" \
  "v_mfma_f32_16x16x32_f16 v[116:119], v[44:47], v[124:127], v[116:119]\n\t" \
  "v_mfma_f32_16x16x32_f16 v[116:119], v[40:43], v[132:135], v[116:119]\n\t" \
  "v_mfma_f32_16x16x32_f16 v[116:119], v[36:39], v[120:123], v[116:119]\n\t" \
  "s_waitcnt lgkmcnt(2)\n\t" \
  "v_fma_f32 v156, v108, s77, v152\n\t" \
  "v_mfma_f32_16x16x32_f16 v[116:119], v[32:35], v[128:131], v[116:119]\n\t" \
  "v_fma_f32 v157, v109, s77, v153\n\t" \
  "v_fma_f32 v158, v110, s77, v154\n\t" \
  "v_mfma_f32_16x16x32_f16 v[116:119], v[28:31], v[136:139], v[116:119]\n\t" \
  "v_fma_f32 v159, v111, s77, v155\n\t" \
  "v_cvt_pk_bf16_f32 v156, v156, v157\n\t" \
  "v_mfma_f32_16x16x32_f16 v[116:119], v[24:27], v[140:143], v[116:119]\n\t" \
  "v_cvt_pk_bf16_f32 v157, v158, v159\n\t" \
  "s_waitcnt lgkmcnt(1)\n\t" \
  "v_mfma_f32_16x16x32_f16 v[116:119], v[20:23], v[144:147], v[116:119]\n\t" \
  "ds_write_b64 v3, v[156:157] offset:10752\n\t" \
  "s_waitcnt lgkmcnt(1)\n\t" \
  "v_mfma_f32_16x16x32_f16 v[116:119], v[16:19], v[148:151], v[116:119]\n\t" \
  "ds_write_b32 v11, v163 offset:208\n\t" \
  "ds_read_b32 v164, v11 offset:320\n\t" \
  "ds_read_b128 v[152:155], v2 offset:4096\n\t" \
  "ds_read_b128 v[100:103], v2 offset:5120\n\t" \
  "s_waitcnt lgkmcnt(1)\n\t" \
  "v_fma_f32 v156, v112, s77, v152\n\t" \
  "v_fma_f32 v157, v113, s77, v153\n\t" \
  "v_fma_f32 v158, v114, s77, v154\n\t" \
  "v_fma_f32 v159, v115, s77, v155\n\t" \
  "v_cvt_pk_bf16_f32 v156, v156, v157\n\t" \
  "v_cvt_pk_bf16_f32 v157, v158, v159\n\t" \
  "ds_write_b64 v3, v[156:157] offset:11264\n\t" \
  "s_waitcnt lgkmcnt(1)\n\t" \
  "v_fma_f32 v96, v116, s77, v100\n\t" \
  "v_fma_f32 v97, v117, s77, v101\n\t" \
  "v_fma_f32 v98, v118, s77, v102\n\t" \
  "v_fma_f32 v99, v119, s77, v103\n\t" \
  "v_cvt_pk_bf16_f32 v96, v96, v97\n\t" \
  "v_cvt_pk_bf16_f32 v97, v98, v99\n\t" \
  "ds_write_b64 v3, v[96:97] offset:11776\n\t" \
  "ds_write_b32 v11, v164 offset:224\n\t" \
  TSQ(11) \
  "v_and_b32 v160, v160, v161\n\t" \
  "v_and_b32 v162, v162, v163\n\t" \
  "v_bitop3_b32 v160, v160, v162, v164 bitop3:0x80\n\t" \
  "v_cmp_eq_u32 vcc, 0, v160\n\t" \
  "s_cbranch_vccz 40f\n\t" \
  "5:\n\t" \
  "ds_read_b32 v160, v11 offset:320\n\t" \
  "s_waitcnt lgkmcnt(0)\n\t" \
  "v_cmp_gt_u32 vcc, 1, v160\n\t" \
  "s_cbranch_vccz 6f\n\t" \
  "s_sleep 1\n\t" \
  "s_branch 5b\n\t" \
  "6:\n\t" \
  "ds_read_b128 v[152:155], v2 offset:0\n\t" \
  "v_mov_b32 v160, 1\n\t" \
  "s_waitcnt lgkmcnt(0)\n\t" \
  "v_fma_f32 v156, v92, s77, v152\n\t" \
  "v_fma_f32 v157, v93, s77, v153\n\t" \
  "v_fma_f32 v158, v94, s77, v154\n\t" \
  "v_fma_f32 v159, v95, s77, v155\n\t" \
  "v_cvt_pk_bf16_f32 v156, v156, v157\n\t" \
  "v_cvt_pk_bf16_f32 v157, v158, v159\n\t" \
  "ds_write_b64 v3, v[156:157] offset:9216\n\t" \
  "ds_write_b32 v11, v160 offset:160\n\t" \
  "ds_read_b128 v[152:155], v2 offset:1024\n\t" \
  "v_mov_b32 v161, 1\n\t" \
  "s_waitcnt lgkmcnt(0)\n\t" \
  "v_fma_f32 v156, v88, s77, v152\n\t" \
  "v_fma_f32 v157, v89, s77, v153\n\t" \
  "v_fma_f32 v158, v90, s77, v154\n\t" \
  "v_fma_f32 v159, v91, s77, v155\n\t" \
  "v_cvt_pk_bf16_f32 v156, v156, v157\n\t" \
  "v_cvt_pk_bf16_f32 v157, v158, v159\n\t" \
  "ds_write_b64 v3, v[156:157] offset:9728\n\t" \
  "ds_write_b32 v11, v161 offset:176\n\t" \
  "ds_read_b128 v[152:155], v2 offset:2048\n\t" \
  "v_mov_b32 v162, 1\n\t" \
  "s_waitcnt lgkmcnt(0)\n\t" \
  "v_fma_f32 v156, v104, s77, v152\n\t" \
  "v_fma_f32 v157, v105, s77, v153\n\t" \
  "v_fma_f32 v158, v106, s77, v154\n\t" \
  "v_fma_f32 v159, v107, s77, v155\n\t" \
  "v_cvt_pk_bf16_f32 v156, v156, v157\n\t" \
  "v_cvt_pk_bf16_f32 v157, v158, v159\n\t" \
  "ds_write_b64 v3, v[156:157] offset:10240\n\t" \
  "ds_write_b32 v11, v162 offset:192\n\t" \
  "ds_read_b128 v[152:155], v2 offset:3072\n\t" \
  "v_mov_b32 v163, 1\n\t" \
  "s_waitcnt lgkmcnt(0)\n\t" \
  "v_fma_f32 v156, v108, s77, v152\n\t" \
  "v_fma_f32 v157, v109, s77, v153\n\t" \
  "v_fma_f32 v158, v110, s77, v154\n\t" \
  "v_fma_f32 v159, v111, s77, v155\n\t" \
  "v_cvt_pk_bf16_f32 v156, v156, v157\n\t" \
  "v_cvt_pk_bf16_f32 v157, v158, v159\n\t" \
  "ds_write_b64 v3, v[156:157] offset:10752\n\t" \
  "ds_write_b32 v11, v163 offset:208\n\t" \
  "ds_read_b128 v[152:155], v2 offset:4096\n\t" \
  "v_mov_b32 v164, 1\n\t" \
  "s_waitcnt lgkmcnt(0)\n\t" \
  "v_fma_f32 v156, v112, s77, v152\n\t" \
  "v_fma_f32 v157, v113, s77, v153\n\t" \
  "v_fma_f32 v158, v114, s77, v154\n\t" \
  "v_fma_f32 v159, v115, s77, v155\n\t" \
  "v_cvt_pk_bf16_f32 v156, v156, v157\n\t" \
  "v_cvt_pk_bf16_f32 v157, v158, v159\n\t" \
  "ds_write_b64 v3, v[156:157] offset:11264\n\t" \
  "ds_read_b128 v[100:103], v2 offset:5120\n\t" \
  "v_mov_b32 v164, 1\n\t" \
  "s_waitcnt lgkmcnt(0)\n\t" \
  "v_fma_f32 v96, v116, s77, v100\n\t" \
  "v_fma_f32 v97, v117, s77, v101\n\t" \
  "v_fma_f32 v98, v118, s77, v102\n\t" \
  "v_fma_f32 v99, v119, s77, v103\n\t" \
  "v_cvt_pk_bf16_f32 v96, v96, v97\n\t" \
  "v_cvt_pk_bf16_f32 v97, v98, v99\n\t" \
  "ds_write_b64 v3, v[96:97] offset:11776\n\t" \
  "ds_write_b32 v11, v164 offset:224\n\t" \
  "s_branch 40f\n\t" \
  "50:\n\t" \
  "s_sleep 1\n\t" \
  "ds_read_b32 v164, v8 offset:72\n\t" \
  "s_waitcnt lgkmcnt(0)\n\t" \
  "v_cmp_eq_u32 vcc, 0, v164\n\t" \
  "s_cbranch_vccnz 50b\n\t" \
  "s_branch 51b\n\t" \
  "52:\n\t" \
  "s_sleep 1\n\t" \
  "ds_read_b32 v164, v8 offset:80\n\t" \
  "s_waitcnt lgkmcnt(0)\n\t" \
  "v_cmp_eq_u32 vcc, 0, v164\n\t" \
  "s_cbranch_vccnz 52b\n\t" \
  "s_branch 53b\n\t" \
  "54:\n\t" \
  "s_sleep 1\n\t" \
  "ds_read_b32 v164, v8 offset:88\n\t" \
  "s_waitcnt lgkmcnt(0)\n\t" \
  "v_cmp_eq_u32 vcc, 0, v164\n\t" \
  "s_cbranch_vccnz 54b\n\t" \
  "s_branch 55b\n\t" \
  "40:\n\t"

#define FAR_ASM \
  "v_and_b32 v1, 63, v0\n\t" \
  "s_mov_b32 s98, 157600\n\t" \
  "v_lshl_add_u32 v2, v1, 4, s98\n\t" \
  "ds_read_b128 v[4:7], v2 offset:0\n\t" \
  "v_lshl_add_u32 v2, v1, 4, %[opc]\n\t" \
  "v_and_b32 v8, 1, v0\n\t" \
  "v_lshlrev_b32 v8, 2, v8\n\t" \
  "v_mov_b32 v9, 1\n\t" \
  "s_waitcnt lgkmcnt(0)\n\t" \
  "v_add_u32 v4, %[chb], v4\n\t" \
  "v_add_u32 v6, %[chb], v6\n\t" \
  "ds_read_b64 v[40:41], v7 offset:0\n\t" \
  "ds_read_b64 v[42:43], v7 offset:8\n\t" \
  "ds_read_b64 v[44:45], v7 offset:128\n\t" \
  "ds_read_b64 v[46:47], v7 offset:136\n\t" \
  "1:\n\t" \
  "ds_read_b32 v144, v11 offset:32\n\t" \
  "s_waitcnt lgkmcnt(0)\n\t" \
  "v_cmp_gt_u32 vcc, 4, v144\n\t" \
  "s_cbranch_vccz 2f\n\t" \
  "s_sleep 1\n\t" \
  "s_branch 1b\n\t" \
  "2:\n\t" \
  "ds_read_b64 v[16:17], v6 offset:1560\n\t" \
  "ds_read_b64 v[18:19], v6 offset:1464\n\t" \
  "ds_read_b64 v[20:21], v6 offset:1368\n\t" \
  "ds_read_b64 v[22:23], v6 offset:1272\n\t" \
  "ds_read_b64 v[24:25], v6 offset:1528\n\t" \
  "ds_read_b64 v[26:27], v6 offset:1432\n\t" \
  "ds_read_b64 v[28:29], v6 offset:1336\n\t" \
  "ds_read_b64 v[30:31], v6 offset:1240\n\t" \
  "ds_read_b64 v[32:33], v6 offset:1496\n\t" \
  "ds_read_b64 v[34:35], v6 offset:1400\n\t" \
  "ds_read_b64 v[36:37], v6 offset:1304\n\t" \
  "ds_read_b64 v[38:39], v6 offset:1208\n\t" \
  TSQ(7) \
  "3:\n\t" \
  "ds_read_b32 v144, v8 offset:64\n\t" \
  "s_waitcnt lgkmcnt(0)\n\t" \
  "v_cmp_gt_u32 vcc, 1, v144\n\t" \
  "s_cbranch_vccz 4f\n\t" \
  "s_sleep 1\n\t" \
  "s_branch 3b\n\t" \
  "4:\n\t" \
  TSQ(8) \
  "s_setprio 3\n\t" \
  "ds_read_b128 v[48:51], v4 offset:448\n\t" \
  "ds_read_b128 v[52:55], v4 offset:432\n\t" \
  "ds_read_b128 v[56:59], v4 offset:960\n\t" \
  "ds_read_b128 v[60:63], v4 offset:944\n\t" \
  "s_waitcnt lgkmcnt(3)\n\t" \
  "v_mfma_f32_16x16x32_f16 v[72:75], v[48:51], v[40:43], 0\n\t" \
  "v_mfma_f32_16x16x32_f16 v[76:79], v[48:51], v[44:47], 0\n\t" \
  "s_waitcnt lgkmcnt(2)\n\t" \
  "v_mfma_f32_16x16x32_f16 v[80:83], v[52:55], v[40:43], 0\n\t" \
  "v_mfma_f32_16x16x32_f16 v[84:87], v[52:55], v[44:47], 0\n\t" \
  "ds_read_b32 v144, v8 offset:72\n\t" \
  "s_waitcnt lgkmcnt(2)\n\t" \
  "v_mfma_f32_16x16x32_f16 v[88:91], v[56:59], v[40:43], 0\n\t" \
  "s_nop 0\n\t" \
  "v_cvt_pk_f16_f32 v104, v72, v73\n\t" \
  "v_mfma_f32_16x16x32_f16 v[92:95], v[56:59], v[44:47], 0\n\t" \
  "v_cvt_pk_f16_f32 v105, v74, v75\n\t" \
  "s_waitcnt lgkmcnt(1)\n\t" \
  "v_mfma_f32_16x16x32_f16 v[96:99], v[60:63], v[40:43], 0\n\t" \
  "v_cvt_pk_f16_f32 v106, v76, v77\n\t" \
  "v_cvt_pk_f16_f32 v107, v78, v79\n\t" \
  "v_mfma_f32_16x16x32_f16 v[100:103], v[60:63], v[44:47], 0\n\t" \
  "v_cvt_pk_f16_f32 v108, v80, v81\n\t" \
  "v_cvt_pk_f16_f32 v109, v82, v83\n\t" \
  "v_mfma_f32_16x16x32_f16 v[128:131], v[104:107], v[16:19], 0\n\t" \
  "v_cvt_pk_f16_f32 v110, v84, v85\n\t" \
  "v_cvt_pk_f16_f32 v111, v86, v87\n\t" \
  "s_nop 1\n\t" \
  "v_mfma_f32_16x16x32_f16 v[128:131], v[108:111], v[20:23], v[128:131]\n\t" \
  "v_cvt_pk_f16_f32 v112, v88, v89\n\t" \
  "v_cvt_pk_f16_f32 v113, v90, v91\n\t" \
  "v_mfma_f32_16x16x32_f16 v[132:135], v[104:107], v[24:27], 0\n\t" \
  "s_waitcnt lgkmcnt(0)\n\t" \
  "v_cmp_eq_u32 vcc, 0, v144\n\t" \
  "v_mfma_f32_16x16x32_f16 v[132:135], v[108:111], v[28:31], v[132:135]\n\t" \
  "s_cbranch_vccnz 50f\n\t" \
  "51:\n\t" \
  "ds_read_b128 v[64:67], v4 offset:1472\n\t" \
  "v_mfma_f32_16x16x32_f16 v[120:123], v[104:107], v[32:35], 0\n\t" \
  "ds_read_b128 v[68:71], v4 offset:1456\n\t" \
  "ds_read_b128 v[48:51], v4 offset:1984\n\t" \
  "v_mfma_f32_16x16x32_f16 v[120:123], v[108:111], v[36:39], v[120:123]\n\t" \
  "ds_read_b128 v[52:55], v4 offset:1968\n\t" \
  "s_waitcnt lgkmcnt(3)\n\t" \
  "v_mfma_f32_16x16x32_f16 v[72:75], v[64:67], v[40:43], 0\n\t" \
  "v_cvt_pk_f16_f32 v114, v92, v93\n\t" \
  "v_cvt_pk_f16_f32 v115, v94, v95\n\t" \
  "v_mfma_f32_16x16x32_f16 v[76:79], v[64:67], v[44:47], 0\n\t" \
  "v_cvt_pk_f16_f32 v116, v96, v97\n\t" \
  "s_waitcnt lgkmcnt(2)\n\t" \
  "v_mfma_f32_16x16x32_f16 v[80:83], v[68:71], v[40:43], 0\n\t" \
  "v_cvt_pk_f16_f32 v117, v98, v99\n\t" \
  "v_cvt_pk_f16_f32 v118, v100, v101\n\t" \
  "v_mfma_f32_16x16x32_f16 v[84:87], v[68:71], v[44:47], 0\n\t" \
  "v_cvt_pk_f16_f32 v119, v102, v103\n\t" \
  "v_mul_f32 v136, s77, v128\n\t" \
  "v_mfma_f32_16x16x32_f16 v[132:135], v[112:115], v[16:19], v[132:135]\n\t" \
  "v_mul_f32 v137, s77, v129\n\t" \
  "v_mul_f32 v138, s77, v130\n\t" \
  "v_mfma_f32_16x16x32_f16 v[132:135], v[116:119], v[20:23], v[132:135]\n\t" \
  "v_mul_f32 v139, s77, v131\n\t" \
  "ds_write_b128 v2, v[136:139] offset:0\n\t" \
  "v_mfma_f32_16x16x32_f16 v[120:123], v[112:115], v[24:27], v[120:123]\n\t" \
  "ds_write_b32 v11, v9 offset:256\n\t" \
  "s_setprio 2\n\t" \
  "v_mfma_f32_16x16x32_f16 v[120:123], v[116:119], v[28:31], v[120:123]\n\t" \
  "ds_read_b32 v144, v8 offset:80\n\t" \
  "v_cvt_pk_f16_f32 v104, v72, v73\n\t" \
  "v_mfma_f32_16x16x32_f16 v[124:127], v[112:115], v[32:35], 0\n\t" \
  "v_cvt_pk_f16_f32 v105, v74, v75\n\t" \
  "v_cvt_pk_f16_f32 v106, v76, v77\n\t" \
  "v_mfma_f32_16x16x32_f16 v[124:127], v[116:119], v[36:39], v[124:127]\n\t" \
  "v_cvt_pk_f16_f32 v107, v78, v79\n\t" \
  "s_waitcnt lgkmcnt(3)\n\t" \
  "v_mfma_f32_16x16x32_f16 v[88:91], v[48:51], v[40:43], 0\n\t" \
  "v_cvt_pk_f16_f32 v108, v80, v81\n\t" \
  "v_cvt_pk_f16_f32 v109, v82, v83\n\t" \
  "v_mfma_f32_16x16x32_f16 v[92:95], v[48:51], v[44:47], 0\n\t" \
  "v_cvt_pk_f16_f32 v110, v84, v85\n\t" \
  "v_cvt_pk_f16_f32 v111, v86, v87\n\t" \
  "v_mfma_f32_16x16x32_f16 v[96:99], v[52:55], v[40:43], 0\n\t" \
  "s_waitcnt lgkmcnt(0)\n\t" \
  "v_cmp_eq_u32 vcc, 0, v144\n\t" \
  "v_mfma_f32_16x16x32_f16 v[100:103], v[52:55], v[44:47], 0\n\t" \
  "s_cbranch_vccnz 52f\n\t" \
  "53:\n\t" \
  "ds_read_b128 v[56:59], v4 offset:2496\n\t" \
  "v_mfma_f32_16x16x32_f16 v[120:123], v[104:107], v[16:19], v[120:123]\n\t" \
  "ds_read_b128 v[60:63], v4 offset:2480\n\t" \
  "v_mul_f32 v140, s77, v132\n\t" \
  "v_mfma_f32_16x16x32_f16 v[120:123], v[108:111], v[20:23], v[120:123]\n\t" \
  "v_mul_f32 v141, s77, v133\n\t" \
  "v_mul_f32 v142, s77, v134\n\t" \
  "v_mfma_f32_16x16x32_f16 v[124:127], v[104:107], v[24:27], v[124:127]\n\t" \
  "v_mul_f32 v143, s77, v135\n\t" \
  "ds_write_b128 v2, v[140:143] offset:1024\n\t" \
  "v_mfma_f32_16x16x32_f16 v[124:127], v[108:111], v[28:31], v[124:127]\n\t" \
  "ds_write_b32 v11, v9 offset:272\n\t" \
  "s_setprio 1\n\t" \
  "v_mfma_f32_16x16x32_f16 v[128:131], v[104:107], v[32:35], 0\n\t" \
  "v_cvt_pk_f16_f32 v112, v88, v89\n\t" \
  "v_cvt_pk_f16_f32 v113, v90, v91\n\t" \
  "v_mfma_f32_16x16x32_f16 v[128:131], v[108:111], v[36:39], v[128:131]\n\t" \
  "v_cvt_pk_f16_f32 v114, v92, v93\n\t" \
  "s_waitcnt lgkmcnt(3)\n\t" \
  "v_mfma_f32_16x16x32_f16 v[72:75], v[56:59], v[40:43], 0\n\t" \
  "v_cvt_pk_f16_f32 v115, v94, v95\n\t" \
  "v_cvt_pk_f16_f32 v116, v96, v97\n\t" \
  "v_mfma_f32_16x16x32_f16 v[76:79], v[56:59], v[44:47], 0\n\t" \
  "v_cvt_pk_f16_f32 v117, v98, v99\n\t" \
  "s_waitcnt lgkmcnt(2)\n\t" \
  "v_mfma_f32_16x16x32_f16 v[80:83], v[60:63], v[40:43], 0\n\t" \
  "v_cvt_pk_f16_f32 v118, v100, v101\n\t" \
  "v_cvt_pk_f16_f32 v119, v102, v103\n\t" \
  "v_mfma_f32_16x16x32_f16 v[84:87], v[60:63], v[44:47], 0\n\t" \
  "v_mul_f32 v136, s77, v120\n\t" \
  "v_mul_f32 v137, s77, v121\n\t" \
  "v_mfma_f32_16x16x32_f16 v[124:127], v[112:115], v[16:19], v[124:127]\n\t" \
  "v_mul_f32 v138, s77, v122\n\t" \
  "v_mul_f32 v139, s77, v123\n\t" \
  "v_mfma_f32_16x16x32_f16 v[124:127], v[116:119], v[20:23], v[124:127]\n\t" \
  "ds_write_b128 v2, v[136:139] offset:2048\n\t" \
  "ds_write_b32 v11, v9 offset:288\n\t" \
  "v_mfma_f32_16x16x32_f16 v[128:131], v[112:115], v[24:27], v[128:131]\n\t" \
  "s_setprio 0\n\t" \
  "v_cvt_pk_f16_f32 v104, v72, v73\n\t" \
  "v_mfma_f32_16x16x32_f16 v[128:131], v[116:119], v[28:31], v[128:131]\n\t" \
  "v_cvt_pk_f16_f32 v105, v74, v75\n\t" \
  "v_cvt_pk_f16_f32 v106, v76, v77\n\t" \
  "v_mfma_f32_16x16x32_f16 v[132:135], v[112:115], v[32:35], 0\n\t" \
  "v_cvt_pk_f16_f32 v107, v78, v79\n\t" \
  "v_cvt_pk_f16_f32 v108, v80, v81\n\t" \
  "v_mfma_f32_16x16x32_f16 v[132:135], v[116:119], v[36:39], v[132:135]\n\t" \
  "v_cvt_pk_f16_f32 v109, v82, v83\n\t" \
  "v_cvt_pk_f16_f32 v110, v84, v85\n\t" \
  "v_mfma_f32_16x16x32_f16 v[128:131], v[104:107], v[16:19], v[128:131]\n\t" \
  "v_cvt_pk_f16_f32 v111, v86, v87\n\t" \
  "v_mul_f32 v140, s77, v124\n\t" \
  "s_nop 0\n\t" \
  "v_mfma_f32_16x16x32_f16 v[128:131], v[108:111], v[20:23], v[128:131]\n\t" \
  "v_mul_f32 v141, s77, v125\n\t" \
  "v_mul_f32 v142, s77, v126\n\t" \
  "v_mfma_f32_16x16x32_f16 v[132:135], v[104:107], v[24:27], v[132:135]\n\t" \
  "v_mul_f32 v143, s77, v127\n\t" \
  "ds_write_b128 v2, v[140:143] offset:3072\n\t" \
  "v_mfma_f32_16x16x32_f16 v[132:135], v[108:111], v[28:31], v[132:135]\n\t" \
  "ds_write_b32 v11, v9 offset:304\n\t" \
  "s_nop 0\n\t" \
  "v_mul_f32 v136, s77, v128\n\t" \
  "v_mul_f32 v137, s77, v129\n\t" \
  "v_mul_f32 v138, s77, v130\n\t" \
  "v_mul_f32 v139, s77, v131\n\t" \
  "ds_write_b128 v2, v[136:139] offset:4096\n\t" \
  "s_nop 0\n\t" \
  "v_mul_f32 v140, s77, v132\n\t" \
  "v_mul_f32 v141, s77, v133\n\t" \
  "v_mul_f32 v142, s77, v134\n\t" \
  "v_mul_f32 v143, s77, v135\n\t" \
  "ds_write_b128 v2, v[140:143] offset:5120\n\t" \
  "ds_write_b32 v11, v9 offset:320\n\t" \
  "s_branch 40f\n\t" \
  "50:\n\t" \
  "s_sleep 1\n\t" \
  "ds_read_b32 v144, v8 offset:72\n\t" \
  "s_waitcnt lgkmcnt(0)\n\t" \
  "v_cmp_eq_u32 vcc, 0, v144\n\t" \
  "s_cbranch_vccnz 50b\n\t" \
  "s_branch 51b\n\t" \
  "52:\n\t" \
  "s_sleep 1\n\t" \
  "ds_read_b32 v144, v8 offset:80\n\t" \
  "s_waitcnt lgkmcnt(0)\n\t" \
  "v_cmp_eq_u32 vcc, 0, v144\n\t" \
  "s_cbranch_vccnz 52b\n\t" \
  "s_branch 53b\n\t" \
  "40:\n\t"

#define INTER 0
#define FFN_ASM \
  "s_and_b32 s88, %[tag], 1023\n\t" \
  "s_bfe_u32 s89, %[tag], 0xa0010\n\t" \
  "s_bfe_u32 s90, %[tag], 0xa0008\n\t" \
  "s_lshr_b32 s91, %[tag], 22\n\t" \
  "s_mov_b32 s87, 0x40000\n\t" \
  "s_cmp_lt_u32 %[w], 8\n\t" \
  "s_cbranch_scc1 30f\n\t" \
  "s_mov_b32 s92, 0x05040100\n\t" \
  "s_mov_b32 s93, 0x07060302\n\t" \
  "s_and_b32 s86, %[w], 3\n\t" \
  "s_lshl_b32 s85, s86, 5\n\t" \
  "s_add_u32 s85, s85, 46720\n\t" \
  "s_lshl_b32 s86, s86, 1\n\t" \
  "s_mul_i32 s84, s86, 1568\n\t" \
  "s_add_u32 s84, s84, 48768\n\t" \
  "s_add_u32 s82, s70, 9435648\n\t" \
  "s_addc_u32 s83, s71, 0\n\t" \
  "v_mov_b32 v156, 1\n\t" \
  "s_setprio 3\n\t" \
  "11:\n\t" \
  "s_memrealtime s[96:97]\n\t" \
  "s_waitcnt lgkmcnt(0)\n\t" \
  "s_sub_u32 s96, s96, %[tg]\n\t" \
  "s_cmp_lt_i32 s96, 0\n\t" \
  "s_cbranch_scc1 11b\n\t" \
  "global_load_dwordx3 v[110:112], v91, %[wsb] sc1\n\t" \
  "global_load_dwordx3 v[114:116], v92, %[wsb] sc1\n\t" \
  "1:\n\t" \
  "s_waitcnt vmcnt(0)\n\t" \
  TSQ(22) \
  "v_perm_b32 v118, v110, v93, s92\n\t" \
  "v_perm_b32 v119, v110, v93, s93\n\t" \
  "v_perm_b32 v120, v111, v94, s92\n\t" \
  "v_perm_b32 v121, v111, v94, s93\n\t" \
  "v_perm_b32 v122, v114, v95, s92\n\t" \
  "v_perm_b32 v123, v114, v95, s93\n\t" \
  "v_perm_b32 v124, v115, v96, s92\n\t" \
  "v_perm_b32 v125, v115, v96, s93\n\t" \
  "v_max3_u32 v130, v118, v119, v120\n\t" \
  "v_max3_u32 v131, v121, v122, v123\n\t" \
  "v_max3_u32 v126, v124, v125, v130\n\t" \
  "v_xor_b32 v132, %[tag], v112\n\t" \
  "v_max_u32 v126, v126, v131\n\t" \
  "v_bitop3_b32 v132, v132, v116, %[tag] bitop3:0xf6\n\t" \
  "v_med3_u32 v127, v118, v119, v120\n\t" \
  "v_max_u32_dpp v129, v126, v126 row_ror:4 row_mask:0xf bank_mask:0xf\n\t" \
  "v_cmp_ne_u32 vcc, 0, v132\n\t" \
  "v_min3_u32 v128, v118, v119, v120\n\t" \
  "v_max_u32_dpp v131, v129, v129 row_ror:8 row_mask:0xf bank_mask:0xf\n\t" \
  "v_max_u32_dpp v129, v129, v129 row_ror:8 row_mask:0xf bank_mask:0xf\n\t" \
  "v_med3_u32 v128, v127, v128, v121\n\t" \
  "v_med3_u32 v127, v130, v127, v121\n\t" \
  "v_permlane16_swap_b32 v129, v131\n\t" \
  "v_max_u32 v129, v129, v131\n\t" \
  "v_max_u32 v131, v129, v131\n\t" \
  "v_max_u32 v130, v130, v121\n\t" \
  "v_med3_u32 v128, v127, v128, v122\n\t" \
  "v_permlane32_swap_b32 v129, v131\n\t" \
  "v_max_u32 v129, v129, v131\n\t" \
  "v_lshrrev_b32 v129, 1, v129\n\t" \
  "s_cbranch_vccnz 20f\n\t" \
  "ds_write_b32 v97, v129\n\t" \
  "s_mov_b64 exec, 1\n\t" \
  "ds_add_u32 v88, v156 offset:120\n\t" \
  "s_mov_b64 exec, -1\n\t" \
  "v_med3_u32 v127, v130, v127, v122\n\t" \
  "v_max_u32 v130, v130, v122\n\t" \
  "v_med3_u32 v128, v127, v128, v123\n\t" \
  "v_med3_u32 v127, v130, v127, v123\n\t" \
  "v_max_u32 v130, v130, v123\n\t" \
  "v_med3_u32 v128, v127, v128, v124\n\t" \
  "v_med3_u32 v127, v130, v127, v124\n\t" \
  "v_max_u32 v130, v130, v124\n\t" \
  "v_med3_u32 v128, v127, v128, v125\n\t" \
  "v_med3_u32 v127, v130, v127, v125\n\t" \
  "12:\n\t" \
  "ds_read_b32 v157, v88 offset:120\n\t" \
  "ds_read_b32 v133, v98 offset:0\n\t" \
  "ds_read_b128 v[134:137], v99 offset:0\n\t" \
  "v_lshrrev_b32 v130, 1, v126\n\t" \
  "v_lshrrev_b32 v131, 1, v127\n\t" \
  "s_waitcnt lgkmcnt(2)\n\t" \
  "v_cmp_gt_u32 vcc, 4, v157\n\t" \
  "s_cbranch_vccnz 12b\n\t" \
  "s_waitcnt lgkmcnt(0)\n\t" \
  "v_sub_u32 v134, v133, v134\n\t" \
  "v_sub_u32 v135, v133, v135\n\t" \
  "v_sub_u32 v136, v133, v136\n\t" \
  "v_sub_u32 v137, v133, v137\n\t" \
  "v_lshrrev_b32 v138, 31, v134\n\t" \
  "v_alignbit_b32 v138, v138, v135, 31\n\t" \
  "v_alignbit_b32 v138, v138, v136, 31\n\t" \
  "v_alignbit_b32 v138, v138, v137, 31\n\t" \
  "v_bcnt_u32_b32 v138, v138, 0\n\t" \
  "s_nop 1\n\t" \
  "v_add_u32_dpp v138, v138, v138 quad_perm:[1,0,3,2] row_mask:0xf bank_mask:0xf bound_ctrl:1\n\t" \
  "s_nop 1\n\t" \
  "v_add_u32_dpp v138, v138, v138 quad_perm:[2,3,0,1] row_mask:0xf bank_mask:0xf bound_ctrl:1\n\t" \
  "v_cmp_eq_u32 vcc, 7, v138\n\t" \
  "s_ff1_i32_b64 s96, vcc\n\t" \
  "v_readlane_b32 s96, v133, s96\n\t" \
  "s_lshl_b32 s81, s96, 1\n\t" \
  "v_cmp_le_u32 s[94:95], s81, v126\n\t" \
  "v_cmp_le_u32 s[98:99], s81, v127\n\t" \
  "v_cmp_le_u32 vcc, s81, v128\n\t" \
  "v_mbcnt_lo_u32_b32 v134, s94, 0\n\t" \
  "v_mbcnt_lo_u32_b32 v135, s98, 0\n\t" \
  "v_mbcnt_hi_u32_b32 v134, s95, v134\n\t" \
  "v_mbcnt_hi_u32_b32 v135, s99, v135\n\t" \
  "s_bcnt1_i32_b64 s96, s[94:95]\n\t" \
  "s_bcnt1_i32_b64 s97, s[98:99]\n\t" \
  "v_lshl_add_u32 v134, v134, 2, s85\n\t" \
  "v_mad_i32_i24 v135, v135, -4, s85\n\t" \
  "s_add_u32 s96, s96, s97\n\t" \
  "s_cmp_gt_u32 s96, " TOPCAP "\n\t" \
  "s_cbranch_scc1 22f\n\t" \
  "" MIDBR " 25f\n\t" \
  "s_mov_b64 exec, s[94:95]\n\t" \
  "ds_write_b32 v134, v130\n\t" \
  "s_mov_b64 exec, s[98:99]\n\t" \
  "ds_write_b32 v135, v131 offset:28\n\t" \
  "2:\n\t" \
  "s_mov_b64 exec, 1\n\t" \
  "ds_add_u32 v88, v156 offset:124\n\t" \
  "s_mov_b64 exec, -1\n\t" \
  "s_mov_b32 m0, s84\n\t" \
  "12:\n\t" \
  "ds_read_b32 v157, v88 offset:124\n\t" \
  "ds_read_b32 v133, v100 offset:0\n\t" \
  "ds_read_b128 v[140:143], v101 offset:0\n\t" \
  "ds_read_b128 v[144:147], v101 offset:16\n\t" \
  "ds_read_b128 v[148:151], v101 offset:32\n\t" \
  "ds_read_b128 v[152:155], v101 offset:48\n\t" \
  "s_waitcnt lgkmcnt(5)\n\t" \
  "v_cmp_gt_u32 vcc, 4, v157\n\t" \
  "s_cbranch_vccnz 12b\n\t" \
  "s_waitcnt lgkmcnt(3)\n\t" \
  "v_sub_u32 v140, v133, v140\n\t" \
  "v_sub_u32 v141, v133, v141\n\t" \
  "v_lshrrev_b32 v138, 31, v140\n\t" \
  "v_sub_u32 v142, v133, v142\n\t" \
  "v_lshrrev_b32 v139, 31, v141\n\t" \
  "v_sub_u32 v143, v133, v143\n\t" \
  "v_alignbit_b32 v138, v138, v142, 31\n\t" \
  "s_waitcnt lgkmcnt(2)\n\t" \
  "v_sub_u32 v144, v133, v144\n\t" \
  "v_alignbit_b32 v139, v139, v143, 31\n\t" \
  "v_sub_u32 v145, v133, v145\n\t" \
  "v_alignbit_b32 v138, v138, v144, 31\n\t" \
  "v_sub_u32 v146, v133, v146\n\t" \
  "v_alignbit_b32 v139, v139, v145, 31\n\t" \
  "v_sub_u32 v147, v133, v147\n\t" \
  "v_alignbit_b32 v138, v138, v146, 31\n\t" \
  "s_waitcnt lgkmcnt(1)\n\t" \
  "v_sub_u32 v148, v133, v148\n\t" \
  "v_alignbit_b32 v139, v139, v147, 31\n\t" \
  "v_sub_u32 v149, v133, v149\n\t" \
  "v_alignbit_b32 v138, v138, v148, 31\n\t" \
  "v_sub_u32 v150, v133, v150\n\t" \
  "v_alignbit_b32 v139, v139, v149, 31\n\t" \
  "v_sub_u32 v151, v133, v151\n\t" \
  "v_alignbit_b32 v138, v138, v150, 31\n\t" \
  "s_waitcnt lgkmcnt(0)\n\t" \
  "v_sub_u32 v152, v133, v152\n\t" \
  "v_alignbit_b32 v139, v139, v151, 31\n\t" \
  "v_sub_u32 v153, v133, v153\n\t" \
  "v_alignbit_b32 v138, v138, v152, 31\n\t" \
  "v_sub_u32 v154, v133, v154\n\t" \
  "v_alignbit_b32 v139, v139, v153, 31\n\t" \
  "v_sub_u32 v155, v133, v155\n\t" \
  "v_alignbit_b32 v138, v138, v154, 31\n\t" \
  "v_bcnt_u32_b32 v138, v138, 0\n\t" \
  "v_alignbit_b32 v139, v139, v155, 31\n\t" \
  "v_bfe_u32 v130, v133, 4, 11\n\t" \
  "v_bcnt_u32_b32 v138, v139, v138\n\t" \
  "v_mul_u32_u24 v130, 1536, v130\n\t" \
  "v_mov_b32 v139, v138\n\t" \
  "s_add_u32 s97, s86, 1\n\t" \
  "s_nop 0\n\t" \
  "v_permlane32_swap_b32 v138, v139\n\t" \
  "v_add_u32 v138, v138, v139\n\t" \
  "v_cmp_eq_u32 vcc, s86, v138\n\t" \
  "v_cmp_eq_u32 s[94:95], s97, v138\n\t" \
  "s_ff1_i32_b32 s96, vcc_lo\n\t" \
  "s_ff1_i32_b32 s97, s94\n\t" \
  "v_readlane_b32 s96, v130, s96\n\t" \
  "v_readlane_b32 s97, v130, s97\n\t" \
  "s_sub_u32 s94, s82, s96\n\t" \
  "s_subb_u32 s95, s83, 0\n\t" \
  "global_load_lds_dwordx4 v102, s[94:95] sc1\n\t" \
  "s_mov_b32 exec_hi, 0\n\t" \
  "global_load_lds_dwordx4 v102, s[94:95] offset:1024 sc1\n\t" \
  "s_mov_b32 exec_hi, -1\n\t" \
  "s_add_u32 m0, s84, 1568\n\t" \
  "s_sub_u32 s94, s82, s97\n\t" \
  "s_subb_u32 s95, s83, 0\n\t" \
  "global_load_lds_dwordx4 v102, s[94:95] sc1\n\t" \
  "s_mov_b32 exec_hi, 0\n\t" \
  "global_load_lds_dwordx4 v102, s[94:95] offset:1024 sc1\n\t" \
  "s_mov_b32 exec_hi, -1\n\t" \
  TSQ(23) \
  "v_min_u32 v138, 8, v138\n\t" \
  "v_lshlrev_b32 v138, 2, v138\n\t" \
  "ds_write_b32 v138, v133 offset:46976\n\t" \
  "v_mov_b64 v[140:141], 0\n\t" \
  "v_mov_b64 v[142:143], 0\n\t" \
  "v_mov_b64 v[144:145], 0\n\t" \
  "v_mov_b64 v[146:147], 0\n\t" \
  "v_mov_b64 v[148:149], 0\n\t" \
  "v_mov_b64 v[150:151], 0\n\t" \
  "v_mov_b64 v[152:153], 0\n\t" \
  "v_mov_b64 v[154:155], 0\n\t" \
  "v_mov_b32 v91, 0xfffffc00\n\t" \
  "s_setprio 0\n\t" \
  "s_waitcnt vmcnt(0)\n\t" \
  "s_waitcnt lgkmcnt(0)\n\t" \
  "s_barrier\n\t" \
  TSQ(24) \
  "s_cmp_lg_u32 %[w], 11\n\t" \
  "s_cbranch_scc1 3f\n\t" \
  "ds_read_b32 v110, v106 offset:0\n\t" \
  "s_waitcnt lgkmcnt(0)\n\t" \
  "v_bfe_u32 v130, v110, 4, 11\n\t" \
  "v_xor_b32 v130, 0x7ff, v130\n\t" \
  "s_movk_i32 s96, 1152\n\t" \
  "v_mad_u32_u24 v111, v130, s96, v107\n\t" \
  "global_load_short_d16_hi v113, v111, %[x]\n\t" \
  "v_lshrrev_b32 v131, 6, v130\n\t" \
  "v_bfe_u32 v138, v130, 1, 3\n\t" \
  "v_lshl_or_b32 v131, v131, 3, v138\n\t" \
  "v_lshrrev_b32 v130, 4, v130\n\t" \
  "v_xor_b32 v130, v130, v108\n\t" \
  "v_and_b32 v130, 3, v130\n\t" \
  "v_and_b32 v138, -4, v108\n\t" \
  "v_lshl_add_u32 v130, v130, 4, v138\n\t" \
  "s_movk_i32 s96, 12288\n\t" \
  "v_mad_u32_u24 v112, v131, s96, v130\n\t" \
  "global_load_dwordx4 v[114:117], v112, %[wsb] sc1\n\t" \
  "global_store_short v111, v140, %[out] sc1\n\t" \
  "v_lshlrev_b32 v130, 1, v110\n\t" \
  "v_ashrrev_i32 v131, 31, v130\n\t" \
  "s_brev_b32 s96, 1\n\t" \
  "v_bitop3_b32 v130, v130, v131, s96 bitop3:0x4b\n\t" \
  "v_and_b32 v130, 0xffff0000, v130\n\t" \
  "v_mul_f32 v130, 0xbfb8aa3b, v130\n\t" \
  "v_exp_f32 v130, v130\n\t" \
  "s_nop 0\n\t" \
  "v_add_f32 v130, 1.0, v130\n\t" \
  "v_rcp_f32 v130, v130\n\t" \
  "s_nop 0\n\t" \
  "v_cvt_pk_bf16_f32 v119, 0, v130\n\t" \
  "3:\n\t" \
  "s_barrier\n\t" \
  "s_cmp_lg_u32 %[w], 11\n\t" \
  "s_cbranch_scc1 3f\n\t" \
  "ds_read_u16_d16_hi v118, v109\n\t" \
  "3:\n\t" \
  "s_barrier\n\t" \
  TSQ(25) \
  "ds_read_b128 v[124:127], v103 offset:0\n\t" \
  "ds_read_b128 v[128:131], v103 offset:16\n\t" \
  "s_waitcnt lgkmcnt(1)\n\t" \
  "v_mfma_f32_16x16x32_bf16 v[156:159], v[124:127], v[80:83], 0\n\t" \
  "v_smfmac_f32_16x16x64_bf16 v[140:143], v[124:127], v[16:23], v90\n\t" \
  "v_smfmac_f32_16x16x64_bf16 v[144:147], v[124:127], v[24:31], v90\n\t" \
  "v_smfmac_f32_16x16x64_bf16 v[148:151], v[124:127], v[32:39], v90\n\t" \
  "v_smfmac_f32_16x16x64_bf16 v[152:155], v[124:127], v[40:47], v90\n\t" \
  "s_waitcnt lgkmcnt(0)\n\t" \
  "v_mfma_f32_16x16x32_bf16 v[156:159], v[128:131], v[84:87], v[156:159]\n\t" \
  "v_smfmac_f32_16x16x64_bf16 v[140:143], v[128:131], v[48:55], v90\n\t" \
  "v_smfmac_f32_16x16x64_bf16 v[144:147], v[128:131], v[56:63], v90\n\t" \
  "v_smfmac_f32_16x16x64_bf16 v[148:151], v[128:131], v[64:71], v90\n\t" \
  "v_smfmac_f32_16x16x64_bf16 v[152:155], v[128:131], v[72:79], v90\n\t" \
  "v_add_u32 v92, 4096, v104\n\t" \
  "s_nop 2\n\t" \
  "v_bitop3_b32 v32, v156, v91, s88 bitop3:0xea\n\t" \
  "v_bitop3_b32 v33, v157, v91, s89 bitop3:0xea\n\t" \
  "v_bitop3_b32 v34, v158, v91, s90 bitop3:0xea\n\t" \
  "v_bitop3_b32 v35, v159, v91, s91 bitop3:0xea\n\t" \
  "s_mov_b32 exec_hi, 0\n\t" \
  "global_store_dwordx4 v105, v[32:35], %[wsb] sc1\n\t" \
  "s_mov_b32 exec_hi, -1\n\t" \
  "v_bitop3_b32 v16, v140, v91, s88 bitop3:0xea\n\t" \
  "v_bitop3_b32 v17, v144, v91, s89 bitop3:0xea\n\t" \
  "v_bitop3_b32 v18, v148, v91, s90 bitop3:0xea\n\t" \
  "v_bitop3_b32 v19, v152, v91, s91 bitop3:0xea\n\t" \
  "global_store_dwordx4 v104, v[16:19], %[wsb] offset:-2048 sc1\n\t" \
  "v_bitop3_b32 v20, v141, v91, s88 bitop3:0xea\n\t" \
  "v_bitop3_b32 v21, v145, v91, s89 bitop3:0xea\n\t" \
  "v_bitop3_b32 v22, v149, v91, s90 bitop3:0xea\n\t" \
  "v_bitop3_b32 v23, v153, v91, s91 bitop3:0xea\n\t" \
  "global_store_dwordx4 v104, v[20:23], %[wsb] offset:0 sc1\n\t" \
  "v_bitop3_b32 v24, v142, v91, s88 bitop3:0xea\n\t" \
  "v_bitop3_b32 v25, v146, v91, s89 bitop3:0xea\n\t" \
  "v_bitop3_b32 v26, v150, v91, s90 bitop3:0xea\n\t" \
  "v_bitop3_b32 v27, v154, v91, s91 bitop3:0xea\n\t" \
  "global_store_dwordx4 v104, v[24:27], %[wsb] offset:2048 sc1\n\t" \
  "v_bitop3_b32 v28, v143, v91, s88 bitop3:0xea\n\t" \
  "v_bitop3_b32 v29, v147, v91, s89 bitop3:0xea\n\t" \
  "v_bitop3_b32 v30, v151, v91, s90 bitop3:0xea\n\t" \
  "v_bitop3_b32 v31, v155, v91, s91 bitop3:0xea\n\t" \
  "global_store_dwordx4 v92, v[28:31], %[wsb] offset:0 sc1\n\t" \
  TSQ(26) \
  "s_cmp_lg_u32 %[w], 11\n\t" \
  "s_cbranch_scc1 50f\n\t" \
  "s_waitcnt vmcnt(6)\n\t" \
  "4:\n\t" \
  "v_cmp_ne_u32 vcc, %[tag], v117\n\t" \
  "s_cbranch_vccnz 24f\n\t" \
  "5:\n\t" \
  "v_cmp_gt_u32 vcc, 384, v107\n\t" \
  "v_mov_b32 v130, 768\n\t" \
  "v_cmp_gt_u32 s[94:95], v130, v107\n\t" \
  "v_and_b32 v131, 16, v110\n\t" \
  "v_cndmask_b32 v115, v115, v114, vcc\n\t" \
  "v_cndmask_b32 v114, v116, v115, s[94:95]\n\t" \
  "v_lshlrev_b32 v114, v131, v114\n\t" \
  "v_and_b32 v114, 0xffff0000, v114\n\t" \
  "v_and_b32 v113, 0xffff0000, v113\n\t" \
  "v_and_b32 v118, 0xffff0000, v118\n\t" \
  "v_add_f32 v120, v113, v114\n\t" \
  "v_cvt_pk_bf16_f32 v120, 0, v120\n\t" \
  "s_barrier\n\t" \
  "ds_read2st64_b32 v[124:125], v89 offset0:0 offset1:1\n\t" \
  "ds_read2st64_b32 v[126:127], v89 offset0:2 offset1:3\n\t" \
  "ds_read2st64_b32 v[128:129], v89 offset0:4 offset1:5\n\t" \
  "s_waitcnt lgkmcnt(1)\n\t" \
  "v_pk_add_f32 v[124:125], v[124:125], v[126:127]\n\t" \
  "s_waitcnt lgkmcnt(0)\n\t" \
  "v_pk_add_f32 v[124:125], v[124:125], v[128:129]\n\t" \
  "v_add_f32 v124, v124, v125\n\t" \
  "v_cvt_pk_bf16_f32 v124, 0, v124\n\t" \
  "v_mul_f32 v124, v124, v119\n\t" \
  "v_cvt_pk_bf16_f32 v124, 0, v124\n\t" \
  "v_add_f32 v124, v124, v118\n\t" \
  "v_cvt_pk_bf16_f32 v124, 0, v124\n\t" \
  "v_add_f32 v124, v124, v120\n\t" \
  "v_cvt_pk_bf16_f32 v124, v124, v124\n\t" \
  "global_store_short v111, v124, %[out] sc1\n\t" \
  "s_branch 50f\n\t" \
  "20:\n\t" \
  "s_sub_u32 s87, s87, 1\n\t" \
  "s_cmp_lt_i32 s87, 0\n\t" \
  "s_cbranch_scc1 21f\n\t" \
  "s_sleep 1\n\t" \
  "v_add_u32 v130, 90112, v91\n\t" \
  "v_add_u32 v131, 90112, v92\n\t" \
  "s_mov_b64 exec, vcc\n\t" \
  "global_load_dwordx3 v[110:112], v130, %[wsb] sc1\n\t" \
  "global_load_dwordx3 v[114:116], v131, %[wsb] sc1\n\t" \
  "s_mov_b64 exec, -1\n\t" \
  "s_branch 1b\n\t" \
  "21:\n\t" \
  "v_mov_b32 v112, %[tag]\n\t" \
  "v_mov_b32 v116, %[tag]\n\t" \
  "s_branch 1b\n\t" \
  "25:\n\t" \
  "s_mov_b32 s97, 0\n\t" \
  "v_cmp_le_u32 vcc, s81, v118\n\t" \
  "v_lshrrev_b32 v130, 1, v118\n\t" \
  "v_mov_b32 v131, s97\n\t" \
  "v_mbcnt_lo_u32_b32 v131, vcc_lo, v131\n\t" \
  "v_mbcnt_hi_u32_b32 v131, vcc_hi, v131\n\t" \
  "s_bcnt1_i32_b64 s96, vcc\n\t" \
  "v_min_u32 v131, 7, v131\n\t" \
  "s_add_u32 s97, s97, s96\n\t" \
  "v_lshl_add_u32 v131, v131, 2, s85\n\t" \
  "s_mov_b64 exec, vcc\n\t" \
  "ds_write_b32 v131, v130\n\t" \
  "s_mov_b64 exec, -1\n\t" \
  "v_cmp_le_u32 vcc, s81, v119\n\t" \
  "v_lshrrev_b32 v130, 1, v119\n\t" \
  "v_mov_b32 v131, s97\n\t" \
  "v_mbcnt_lo_u32_b32 v131, vcc_lo, v131\n\t" \
  "v_mbcnt_hi_u32_b32 v131, vcc_hi, v131\n\t" \
  "s_bcnt1_i32_b64 s96, vcc\n\t" \
  "v_min_u32 v131, 7, v131\n\t" \
  "s_add_u32 s97, s97, s96\n\t" \
  "v_lshl_add_u32 v131, v131, 2, s85\n\t" \
  "s_mov_b64 exec, vcc\n\t" \
  "ds_write_b32 v131, v130\n\t" \
  "s_mov_b64 exec, -1\n\t" \
  "v_cmp_le_u32 vcc, s81, v120\n\t" \
  "v_lshrrev_b32 v130, 1, v120\n\t" \
  "v_mov_b32 v131, s97\n\t" \
  "v_mbcnt_lo_u32_b32 v131, vcc_lo, v131\n\t" \
  "v_mbcnt_hi_u32_b32 v131, vcc_hi, v131\n\t" \
  "s_bcnt1_i32_b64 s96, vcc\n\t" \
  "v_min_u32 v131, 7, v131\n\t" \
  "s_add_u32 s97, s97, s96\n\t" \
  "v_lshl_add_u32 v131, v131, 2, s85\n\t" \
  "s_mov_b64 exec, vcc\n\t" \
  "ds_write_b32 v131, v130\n\t" \
  "s_mov_b64 exec, -1\n\t" \
  "v_cmp_le_u32 vcc, s81, v121\n\t" \
  "v_lshrrev_b32 v130, 1, v121\n\t" \
  "v_mov_b32 v131, s97\n\t" \
  "v_mbcnt_lo_u32_b32 v131, vcc_lo, v131\n\t" \
  "v_mbcnt_hi_u32_b32 v131, vcc_hi, v131\n\t" \
  "s_bcnt1_i32_b64 s96, vcc\n\t" \
  "v_min_u32 v131, 7, v131\n\t" \
  "s_add_u32 s97, s97, s96\n\t" \
  "v_lshl_add_u32 v131, v131, 2, s85\n\t" \
  "s_mov_b64 exec, vcc\n\t" \
  "ds_write_b32 v131, v130\n\t" \
  "s_mov_b64 exec, -1\n\t" \
  "v_cmp_le_u32 vcc, s81, v122\n\t" \
  "v_lshrrev_b32 v130, 1, v122\n\t" \
  "v_mov_b32 v131, s97\n\t" \
  "v_mbcnt_lo_u32_b32 v131, vcc_lo, v131\n\t" \
  "v_mbcnt_hi_u32_b32 v131, vcc_hi, v131\n\t" \
  "s_bcnt1_i32_b64 s96, vcc\n\t" \
  "v_min_u32 v131, 7, v131\n\t" \
  "s_add_u32 s97, s97, s96\n\t" \
  "v_lshl_add_u32 v131, v131, 2, s85\n\t" \
  "s_mov_b64 exec, vcc\n\t" \
  "ds_write_b32 v131, v130\n\t" \
  "s_mov_b64 exec, -1\n\t" \
  "v_cmp_le_u32 vcc, s81, v123\n\t" \
  "v_lshrrev_b32 v130, 1, v123\n\t" \
  "v_mov_b32 v131, s97\n\t" \
  "v_mbcnt_lo_u32_b32 v131, vcc_lo, v131\n\t" \
  "v_mbcnt_hi_u32_b32 v131, vcc_hi, v131\n\t" \
  "s_bcnt1_i32_b64 s96, vcc\n\t" \
  "v_min_u32 v131, 7, v131\n\t" \
  "s_add_u32 s97, s97, s96\n\t" \
  "v_lshl_add_u32 v131, v131, 2, s85\n\t" \
  "s_mov_b64 exec, vcc\n\t" \
  "ds_write_b32 v131, v130\n\t" \
  "s_mov_b64 exec, -1\n\t" \
  "v_cmp_le_u32 vcc, s81, v124\n\t" \
  "v_lshrrev_b32 v130, 1, v124\n\t" \
  "v_mov_b32 v131, s97\n\t" \
  "v_mbcnt_lo_u32_b32 v131, vcc_lo, v131\n\t" \
  "v_mbcnt_hi_u32_b32 v131, vcc_hi, v131\n\t" \
  "s_bcnt1_i32_b64 s96, vcc\n\t" \
  "v_min_u32 v131, 7, v131\n\t" \
  "s_add_u32 s97, s97, s96\n\t" \
  "v_lshl_add_u32 v131, v131, 2, s85\n\t" \
  "s_mov_b64 exec, vcc\n\t" \
  "ds_write_b32 v131, v130\n\t" \
  "s_mov_b64 exec, -1\n\t" \
  "v_cmp_le_u32 vcc, s81, v125\n\t" \
  "v_lshrrev_b32 v130, 1, v125\n\t" \
  "v_mov_b32 v131, s97\n\t" \
  "v_mbcnt_lo_u32_b32 v131, vcc_lo, v131\n\t" \
  "v_mbcnt_hi_u32_b32 v131, vcc_hi, v131\n\t" \
  "s_bcnt1_i32_b64 s96, vcc\n\t" \
  "v_min_u32 v131, 7, v131\n\t" \
  "s_add_u32 s97, s97, s96\n\t" \
  "v_lshl_add_u32 v131, v131, 2, s85\n\t" \
  "s_mov_b64 exec, vcc\n\t" \
  "ds_write_b32 v131, v130\n\t" \
  "s_mov_b64 exec, -1\n\t" \
  "s_cmp_le_u32 s97, 8\n\t" \
  "s_cbranch_scc1 2b\n\t" \
  "22:\n\t" \
  "v_mov_b32 v134, s85\n\t" \
  "s_mov_b32 s97, 8\n\t" \
  "23:\n\t" \
  "v_max3_u32 v130, v118, v119, v120\n\t" \
  "v_max3_u32 v130, v130, v121, v122\n\t" \
  "v_max3_u32 v130, v130, v123, v124\n\t" \
  "v_max_u32 v130, v130, v125\n\t" \
  "s_nop 1\n\t" \
  "v_max_u32_dpp v130, v130, v130 quad_perm:[1,0,3,2] row_mask:0xf bank_mask:0xf bound_ctrl:1\n\t" \
  "s_nop 1\n\t" \
  "v_max_u32_dpp v130, v130, v130 quad_perm:[2,3,0,1] row_mask:0xf bank_mask:0xf bound_ctrl:1\n\t" \
  "s_nop 1\n\t" \
  "v_max_u32_dpp v130, v130, v130 row_half_mirror row_mask:0xf bank_mask:0xf bound_ctrl:1\n\t" \
  "s_nop 1\n\t" \
  "v_max_u32_dpp v130, v130, v130 row_mirror row_mask:0xf bank_mask:0xf bound_ctrl:1\n\t" \
  "v_mov_b32 v131, v130\n\t" \
  "s_nop 1\n\t" \
  "v_permlane16_swap_b32 v130, v131\n\t" \
  "v_max_u32 v130, v130, v131\n\t" \
  "v_mov_b32 v131, v130\n\t" \
  "s_nop 1\n\t" \
  "v_permlane32_swap_b32 v130, v131\n\t" \
  "v_max_u32 v130, v130, v131\n\t" \
  "v_cmp_ne_u32 vcc, v130, v118\n\t" \
  "s_nop 1\n\t" \
  "v_cndmask_b32 v118, 0, v118, vcc\n\t" \
  "v_cmp_ne_u32 vcc, v130, v119\n\t" \
  "s_nop 1\n\t" \
  "v_cndmask_b32 v119, 0, v119, vcc\n\t" \
  "v_cmp_ne_u32 vcc, v130, v120\n\t" \
  "s_nop 1\n\t" \
  "v_cndmask_b32 v120, 0, v120, vcc\n\t" \
  "v_cmp_ne_u32 vcc, v130, v121\n\t" \
  "s_nop 1\n\t" \
  "v_cndmask_b32 v121, 0, v121, vcc\n\t" \
  "v_cmp_ne_u32 vcc, v130, v122\n\t" \
  "s_nop 1\n\t" \
  "v_cndmask_b32 v122, 0, v122, vcc\n\t" \
  "v_cmp_ne_u32 vcc, v130, v123\n\t" \
  "s_nop 1\n\t" \
  "v_cndmask_b32 v123, 0, v123, vcc\n\t" \
  "v_cmp_ne_u32 vcc, v130, v124\n\t" \
  "s_nop 1\n\t" \
  "v_cndmask_b32 v124, 0, v124, vcc\n\t" \
  "v_cmp_ne_u32 vcc, v130, v125\n\t" \
  "s_nop 1\n\t" \
  "v_cndmask_b32 v125, 0, v125, vcc\n\t" \
  "v_lshrrev_b32 v130, 1, v130\n\t" \
  "ds_write_b32 v134, v130\n\t" \
  "v_add_u32 v134, 4, v134\n\t" \
  "s_sub_u32 s97, s97, 1\n\t" \
  "s_cmp_lg_u32 s97, 0\n\t" \
  "s_cbranch_scc1 23b\n\t" \
  "s_branch 2b\n\t" \
  "24:\n\t" \
  "s_sub_u32 s87, s87, 1\n\t" \
  "s_cmp_lt_i32 s87, 0\n\t" \
  "s_cbranch_scc1 5b\n\t" \
  "global_load_dwordx4 v[114:117], v112, %[wsb] sc1\n\t" \
  "s_waitcnt vmcnt(0)\n\t" \
  "s_branch 4b\n\t" \
  "30:\n\t" \
  "s_mul_i32 s96, %[w], 4608\n\t" \
  "s_add_u32 m0, s96, 8704\n\t" \
  "v_mov_b64 v[124:125], 0\n\t" \
  "v_mov_b64 v[126:127], 0\n\t" \
  "v_mov_b64 v[128:129], 0\n\t" \
  "v_mov_b64 v[130:131], 0\n\t" \
  "v_mov_b64 v[132:133], 0\n\t" \
  "v_mov_b64 v[134:135], 0\n\t" \
  "v_mov_b64 v[136:137], 0\n\t" \
  "v_mov_b64 v[138:139], 0\n\t" \
  "s_barrier\n\t" \
  TSQ(24) \
  "ds_read_b128 v[118:121], v91 offset:1024\n\t" \
  "ds_read_b128 v[110:113], v91 offset:0\n\t" \
  "ds_read_b128 v[114:117], v91 offset:512\n\t" \
  "31:\n\t" \
  "s_waitcnt lgkmcnt(2)\n\t" \
  "v_xor_b32 v121, %[tag], v121\n\t" \
  "s_waitcnt lgkmcnt(1)\n\t" \
  "v_bitop3_b32 v122, v121, v113, %[tag] bitop3:0xf6\n\t" \
  "v_mov_b32 v113, v118\n\t" \
  "v_smfmac_f32_16x16x32_bf16 v[124:127], v[120:121], v[80:83], v90\n\t" \
  "s_waitcnt lgkmcnt(0)\n\t" \
  "v_bitop3_b32 v122, v122, v117, %[tag] bitop3:0xf6\n\t" \
  "v_mov_b32 v117, v119\n\t" \
  "v_smfmac_f32_16x16x32_bf16 v[128:131], v[120:121], v[82:85], v90\n\t" \
  "v_cmp_ne_u32 vcc, 0, v122\n\t" \
  "v_smfmac_f32_16x16x32_bf16 v[132:135], v[120:121], v[84:87], v90\n\t" \
  "v_smfmac_f32_16x16x32_bf16 v[136:139], v[120:121], v[86:89], v90\n\t" \
  "s_cbranch_vccnz 40f\n\t" \
  "32:\n\t" \
  "s_nop 3\n\t" \
  "v_smfmac_f32_16x16x64_bf16 v[124:127], v[110:113], v[16:23], v90\n\t" \
  "v_smfmac_f32_16x16x64_bf16 v[128:131], v[110:113], v[24:31], v90\n\t" \
  "v_smfmac_f32_16x16x64_bf16 v[132:135], v[110:113], v[32:39], v90\n\t" \
  "v_smfmac_f32_16x16x64_bf16 v[136:139], v[110:113], v[40:47], v90\n\t" \
  "v_smfmac_f32_16x16x64_bf16 v[124:127], v[114:117], v[48:55], v90\n\t" \
  "v_smfmac_f32_16x16x64_bf16 v[128:131], v[114:117], v[56:63], v90\n\t" \
  "v_smfmac_f32_16x16x64_bf16 v[132:135], v[114:117], v[64:71], v90\n\t" \
  "v_smfmac_f32_16x16x64_bf16 v[136:139], v[114:117], v[72:79], v90\n\t" \
  "s_nop 4\n\t" \
  "ds_write_addtid_b32 v124 offset:0\n\t" \
  "ds_write_addtid_b32 v125 offset:288\n\t" \
  "ds_write_addtid_b32 v126 offset:576\n\t" \
  "ds_write_addtid_b32 v127 offset:864\n\t" \
  "ds_write_addtid_b32 v128 offset:1152\n\t" \
  "ds_write_addtid_b32 v129 offset:1440\n\t" \
  "ds_write_addtid_b32 v130 offset:1728\n\t" \
  "ds_write_addtid_b32 v131 offset:2016\n\t" \
  "ds_write_addtid_b32 v132 offset:2304\n\t" \
  "ds_write_addtid_b32 v133 offset:2592\n\t" \
  "ds_write_addtid_b32 v134 offset:2880\n\t" \
  "ds_write_addtid_b32 v135 offset:3168\n\t" \
  "ds_write_addtid_b32 v136 offset:3456\n\t" \
  "ds_write_addtid_b32 v137 offset:3744\n\t" \
  "ds_write_addtid_b32 v138 offset:4032\n\t" \
  "ds_write_addtid_b32 v139 offset:4320\n\t" \
  "s_waitcnt lgkmcnt(0)\n\t" \
  "s_barrier\n\t" \
  "ds_read_b32 v110, v92 offset:0\n\t" \
  "ds_read_b32 v111, v92 offset:32\n\t" \
  "ds_read_b32 v112, v92 offset:4608\n\t" \
  "ds_read_b32 v113, v92 offset:4640\n\t" \
  "ds_read_b32 v114, v92 offset:9216\n\t" \
  "ds_read_b32 v115, v92 offset:9248\n\t" \
  "ds_read_b32 v116, v92 offset:13824\n\t" \
  "ds_read_b32 v117, v92 offset:13856\n\t" \
  "ds_read_b32 v118, v92 offset:18432\n\t" \
  "ds_read_b32 v119, v92 offset:18464\n\t" \
  "ds_read_b32 v120, v92 offset:23040\n\t" \
  "ds_read_b32 v121, v92 offset:23072\n\t" \
  "ds_read_b32 v122, v92 offset:27648\n\t" \
  "ds_read_b32 v123, v92 offset:27680\n\t" \
  "ds_read_b32 v124, v92 offset:32256\n\t" \
  "ds_read_b32 v125, v92 offset:32288\n\t" \
  "s_waitcnt lgkmcnt(12)\n\t" \
  "v_pk_add_f32 v[110:111], v[110:111], v[112:113]\n\t" \
  "s_waitcnt lgkmcnt(8)\n\t" \
  "v_pk_add_f32 v[114:115], v[114:115], v[116:117]\n\t" \
  "v_pk_add_f32 v[110:111], v[110:111], v[114:115]\n\t" \
  "s_waitcnt lgkmcnt(4)\n\t" \
  "v_pk_add_f32 v[118:119], v[118:119], v[120:121]\n\t" \
  "s_waitcnt lgkmcnt(0)\n\t" \
  "v_pk_add_f32 v[122:123], v[122:123], v[124:125]\n\t" \
  "v_pk_add_f32 v[118:119], v[118:119], v[122:123]\n\t" \
  "v_pk_add_f32 v[110:111], v[110:111], v[118:119]\n\t" \
  "v_cvt_pk_bf16_f32 v110, 0, v110\n\t" \
  "v_mul_f32 v112, 0xbfb8aa3b, v110\n\t" \
  "v_exp_f32 v112, v112\n\t" \
  "v_cvt_pk_bf16_f32 v111, 0, v111\n\t" \
  "v_add_f32 v112, 1.0, v112\n\t" \
  "v_rcp_f32 v112, v112\n\t" \
  "s_nop 0\n\t" \
  "v_mul_f32 v110, v110, v112\n\t" \
  "v_cvt_pk_bf16_f32 v110, 0, v110\n\t" \
  "v_mul_f32 v110, v110, v111\n\t" \
  "v_cvt_pk_bf16_f32 v110, v110, v110\n\t" \
  "ds_write_b16 v93, v110\n\t" \
  "s_waitcnt lgkmcnt(0)\n\t" \
  "s_barrier\n\t" \
  TSQ(25) \
  "s_cmp_gt_u32 %[w], 5\n\t" \
  "s_cbranch_scc1 50f\n\t" \
  "s_memrealtime s[94:95]\n\t" \
  "s_lshl_b32 s96, %[w], 8\n\t" \
  "s_add_u32 m0, s96, 47232\n\t" \
  "v_add_u32 v95, 73728, v94\n\t" \
  "v_add_u32 v96, 147456, v94\n\t" \
  "s_waitcnt lgkmcnt(0)\n\t" \
  "s_add_u32 s94, s94, " STR(FW) "\n\t" \
  "11:\n\t" \
  "s_memrealtime s[96:97]\n\t" \
  "s_waitcnt lgkmcnt(0)\n\t" \
  "s_sub_u32 s96, s96, s94\n\t" \
  "s_cmp_lt_i32 s96, 0\n\t" \
  "s_cbranch_scc1 11b\n\t" \
  "33:\n\t" \
  "global_load_dwordx4 v[110:113], v94, %[wsb] sc1\n\t" \
  "global_load_dwordx4 v[114:117], v95, %[wsb] sc1\n\t" \
  "global_load_dwordx4 v[118:121], v96, %[wsb] sc1\n\t" \
  "s_waitcnt vmcnt(2)\n\t" \
  "v_xor_b32 v110, s88, v110\n\t" \
  "v_xor_b32 v111, s89, v111\n\t" \
  "v_xor_b32 v112, s90, v112\n\t" \
  "v_xor_b32 v113, s91, v113\n\t" \
  "v_or3_b32 v122, v110, v111, v112\n\t" \
  "v_or_b32 v122, v122, v113\n\t" \
  "s_waitcnt vmcnt(1)\n\t" \
  "v_xor_b32 v114, s88, v114\n\t" \
  "v_xor_b32 v115, s89, v115\n\t" \
  "v_xor_b32 v116, s90, v116\n\t" \
  "v_xor_b32 v117, s91, v117\n\t" \
  "v_or3_b32 v122, v122, v114, v115\n\t" \
  "v_or3_b32 v122, v122, v116, v117\n\t" \
  "v_pk_add_f32 v[110:111], v[110:111], v[114:115]\n\t" \
  "v_pk_add_f32 v[112:113], v[112:113], v[116:117]\n\t" \
  "s_waitcnt vmcnt(0)\n\t" \
  TSQ(26) \
  "v_xor_b32 v118, s88, v118\n\t" \
  "v_xor_b32 v119, s89, v119\n\t" \
  "v_xor_b32 v120, s90, v120\n\t" \
  "v_xor_b32 v121, s91, v121\n\t" \
  "v_or3_b32 v122, v122, v118, v119\n\t" \
  "v_or3_b32 v122, v122, v120, v121\n\t" \
  "v_pk_add_f32 v[110:111], v[110:111], v[118:119]\n\t" \
  "v_pk_add_f32 v[112:113], v[112:113], v[120:121]\n\t" \
  "v_and_b32 v122, 1023, v122\n\t" \
  "v_permlane32_swap_b32 v110, v111\n\t" \
  "v_cmp_ne_u32 vcc, 0, v122\n\t" \
  "v_permlane32_swap_b32 v112, v113\n\t" \
  "v_add_f32 v110, v110, v111\n\t" \
  "v_add_f32 v112, v112, v113\n\t" \
  "s_cbranch_vccnz 42f\n\t" \
  "34:\n\t" \
  "s_nop 0\n\t" \
  "v_permlane16_swap_b32 v110, v112\n\t" \
  "v_add_f32 v110, v110, v112\n\t" \
  "ds_write_addtid_b32 v110\n\t" \
  "s_waitcnt lgkmcnt(0)\n\t" \
  "s_barrier\n\t" \
  "s_branch 50f\n\t" \
  "40:\n\t" \
  "s_sub_u32 s87, s87, 1\n\t" \
  "s_cmp_lt_i32 s87, 0\n\t" \
  "s_cbranch_scc1 32b\n\t" \
  "s_sleep 2\n\t" \
  "ds_read_b32 v122, v97\n\t" \
  "s_waitcnt lgkmcnt(0)\n\t" \
  "v_bfe_u32 v122, v122, 4, 11\n\t" \
  "s_movk_i32 s96, -1536\n\t" \
  "v_mad_i32_i24 v122, v122, s96, v98\n\t" \
  "global_load_dwordx4 v[110:113], v122, %[wsb] offset:0 sc1\n\t" \
  "global_load_dwordx4 v[114:117], v122, %[wsb] offset:512 sc1\n\t" \
  "global_load_dwordx4 v[118:121], v122, %[wsb] offset:1024 sc1\n\t" \
  "v_mov_b64 v[124:125], 0\n\t" \
  "v_mov_b64 v[126:127], 0\n\t" \
  "v_mov_b64 v[128:129], 0\n\t" \
  "v_mov_b64 v[130:131], 0\n\t" \
  "v_mov_b64 v[132:133], 0\n\t" \
  "v_mov_b64 v[134:135], 0\n\t" \
  "v_mov_b64 v[136:137], 0\n\t" \
  "v_mov_b64 v[138:139], 0\n\t" \
  "s_waitcnt vmcnt(0)\n\t" \
  "ds_write_b128 v91, v[110:113] offset:0\n\t" \
  "ds_write_b128 v91, v[114:117] offset:512\n\t" \
  "ds_write_b128 v91, v[118:121] offset:1024\n\t" \
  "s_waitcnt lgkmcnt(0)\n\t" \
  "s_branch 31b\n\t" \
  "42:\n\t" \
  "s_sub_u32 s87, s87, 1\n\t" \
  "s_cmp_lt_i32 s87, 0\n\t" \
  "s_cbranch_scc1 34b\n\t" \
  "s_sleep 1\n\t" \
  "s_branch 33b\n\t" \
  "50:\n\t"

constexpr uint32_t KREP = 176 * 64;
#define KEY_ADR(tid) uint32_t k0 = WS_KEYS + (blockIdx.x & 7) * KREP + (tid) * 16, k1 = k0 + 4096u + ((tid) >> 4) * 192 + max((int)((tid) & 14) - 4, 0) * 16
__device__ __forceinline__ uint32_t key_place(int b) {
  const int q = (b < 80) ? b : 40 + (b >> 1), A = q >> 4, r = 4 * (q & 3) + ((q >> 2) & 3);
  return (A < 4) ? r * 256 + A * 64 : 4096 + r * 448 + ((A < 5) ? 0 : (A - 5) * 128 + 64 + (b & 1) * 64);
}
__device__ __forceinline__ void key_sweep(char* wsb, uint32_t tag, uint32_t k0, uint32_t k1, uint32_t klo, uint32_t* q) {
  u32x3 r0, r1;
#define KLD(io, o0, o1) asm volatile("global_load_dwordx3 %0, %2, %4 sc1\n\tglobal_load_dwordx3 %1, %3, %4 sc1\n\t" \
      "s_waitcnt vmcnt(0)" : io(r0), io(r1) : "v"(o0), "v"(o1), "s"(wsb) : "memory")
  KLD("=&v", k0, k1);
#pragma unroll 1
  for (int spin = 1; spin < SPIN_CAP; ++spin) {
    uint32_t bad = r0[2] ^ tag;
    XOR_OR(bad, r1[2], tag);
    if (LIKELY(!__any(bad != 0u))) break;
    __builtin_amdgcn_s_sleep(PSLEEP2);
    const uint32_t kr = (uint32_t)min(spin, NCOPY - 1) * (8u * KREP);
    if (bad) KLD("+v", k0 + kr, k1 + kr);
  }
  const uint32_t s[4] = {r0[0], r0[1], r1[0], r1[1]};
#pragma unroll
  for (int k = 0; k < 4; ++k) {
    const uint32_t lo = klo - (uint32_t)((k & 1) * 1024 + (k >> 1) * 32768) * 0x10001u;
    q[2 * k] = perm_lo(lo, s[k]);
    q[2 * k + 1] = perm_hi(lo, s[k]);
  }
}
__device__ __forceinline__ uint32_t key_lows(int tid) {
  const uint32_t l = (uint32_t)(0x7ff - (64 * (tid & 12) + 4 * (tid & 48) + 4 * (tid >> 6) + (tid & 3))) << 5;
  return l | ((l - 512u) << 16);
}

#define FFN_REGS "v112", "v113", "v114", "v115", "v116", "v117", "v118", "v119", "v120", "v121", "v122", "v123", "v124", "v125", "v126", "v127", "v128", "v129", "v130", "v131", "v132", "v133", "v134", "v135", "v136", "v137", "v138", "v139", "v140", "v141", "v142", "v143", "v144", "v145", "v146", "v147", "v148", "v149", "v150", "v151", "v152", "v153", "v154", "v155", "v156", "v157", "v158", "v159"
#define LN2_REGS "v44", "v45", "v46", "v47", "v48", "v49", "v50", "v51", "v52", "v53", "v54", "v55", "v56", "v57", "v58", "v59", "v60", "v61", "v62", "v63", "v64", "v65", "v66", "v67", "v68", "v69", "v70", "v71", "v72", "v73", "v74", "v75", "v76", "v77", "v78", "v79", "v80", "v81", "v82", "v83", "v84", "v85", "v86", "v87", "v88", "v89", "v90", "v91", "v92", "v93", "v94", "v95", "v96", "v97", "v98", "v99", "v100", "v101", "v102", "v103", "v107", "v108"
#define RING_REGS "v120", "v121", "v122", "v123", "v124", "v125", "v126", "v127", "v128", "v129", "v130", "v131", "v132", "v133", "v134", "v135", "v136", "v137", "v138", "v139", "v140", "v141", "v142", "v143", "v144", "v145", "v146", "v147", "v148", "v149", "v150", "v151"
#define UDONE() LWAITS(4u * (tidx() & 1), 88, 1, 4)
#define HK(w) (((w) == 4) ? 3 : (w) & 3)
struct VOut { uint32_t a[9]; };
__device__ __forceinline__ VOut v_prep(int k) {
  const int b = blockIdx.x, lane = tidx() & 63;
  const uint32_t vo = WS_VG + vrun(b) + ((lane & 12) + (lane >> 4)) * 24576 + (((lane ^ (b >> 3)) & 3) << 4);
  const uint32_t xo = G_OP + 8192 + 8 * lane, co = 128 + 4 * min(lane & 3, 2);
  const uint32_t x1 = xo + 512 * k, x2 = xo + 512 * (6 + (k >> 1)) + 4 * (k & 1);
  VOut p = {{co, co + 16 * k, x1, x1 + 8 * OPW, x2, x2 + 8 * OPW, vo, vo + 12288u, vo + 12288u * (k & 1)}};
  PIN9(p.a);
  return p;
}
__device__ __forceinline__ void v_out(const VOut& p, char* wsb, uint32_t tag, int k) {
#define VO_RD(r, a, b_, o) "ds_read_b32 v[" #r "], %" #a " offset:" #o "\n\tds_read_b32 v[" #r "+1], %" #a " offset:%13+" #o "\n\tds_read_b32 v[" #r "+2], %" #b_ " offset:" #o "\n\t"
#define VO_POLL(a, o, more) "1:\n\tds_read_b32 v136, %" #a " offset:" #o "\n\t" more "s_waitcnt lgkmcnt(0)\n\tv_cmp_eq_u32 vcc, 0, v136\n\ts_cbranch_vccz 2f\n\ts_sleep " ESL "\n\ts_branch 1b\n2:\n\t"
#define VO_TILE(r, j, sb) VO_POLL(1, 48*j, "") VO_RD(r, 2, 3, 1536*j) VO_RD(r+4, 2, 3, 4+1536*j) "s_waitcnt lgkmcnt(0)\n\t" \
      "global_store_dwordx4 %6, v[" #r ":" #r "+3], %" #sb " sc1\n\tglobal_store_dwordx4 %7, v[" #r "+4:" #r "+7], %" #sb " sc1\n\t"
  asm volatile("v_mov_b32 v123, %12\n\tv_mov_b32 v127, %12\n\tv_mov_b32 v131, %12\n\tv_mov_b32 v135, %12\n\ts_cmp_eq_u32 %14, 3\n\ts_cbranch_scc1 3f\n\t"
               VO_TILE(120, 0, 9)
               VO_TILE(128, 1, 10) "3:\n\t" VO_POLL(0, 80, "")
               "s_setprio 3\n\t" VO_POLL(0, 96, VO_RD(120, 4, 5, 0)) "global_store_dwordx4 %8, v[120:123], %11 sc1\n\ts_nop 0"
               : : "v"(p.a[0]), "v"(p.a[1]), "v"(p.a[2]), "v"(p.a[3]), "v"(p.a[4]), "v"(p.a[5]), "v"(p.a[6]), "v"(p.a[7]), "v"(p.a[8]),
                   "s"(wsb + k * (16 * 24576)), "s"(wsb + (k + 3) * (16 * 24576)), "s"(wsb + (6 + (k >> 1)) * (16 * 24576)), "s"(tag), "i"(4 * OPW), "s"(k)
               : "vcc", "scc", "memory", "v120", "v121", "v122", "v123", "v124", "v125", "v126", "v127", "v128", "v129", "v130", "v131", "v132", "v133", "v134", "v135", "v136");
  TS(11);
}
struct Touch { uint64_t b; uint32_t when; };
#define TOUCH(t, pol) asm volatile("global_load_dword v166, v165, %0" pol : : "s"(t.b) : "v166")
#define LTOUCH() do { if (th.when - 4u < 8u) { for (uint32_t i_ = 4; i_ < th.when; ++i_) __builtin_amdgcn_s_sleep(9); TOUCH(th, TNT); } } while (0)
__device__ __forceinline__ void hint_put(char* wsb, uint32_t v) {
  const int b = blockIdx.x, lane = tidx() & 63;
  if (lane < 12) st16s(wsb, WS_HINT + ((b >> 6) * 8 + (b & 7)) * 64 + 16 * (lane & 3) + 512 * (lane & 12), (u32x4){v, 0u, 0u, 0u});
}
__device__ __forceinline__ uint32_t hint_max(char* wsb, uint32_t tag, int tries = 3, uint32_t ago = 60) {
  const uint32_t base = NOW() - 2048u;
#pragma unroll 1
  for (int spin = 0; spin < tries; ++spin) {
    uint32_t m;
    asm volatile(".set i_, 0\n\t.rept " STR(NS) "\n\ts_load_dword s[72+i_], %1, 64*i_\n\t.set i_, i_+1\n\t.endr\n\ts_mov_b32 %0, 0\n\ts_waitcnt lgkmcnt(0)\n\t"
                 ".set i_, 0\n\t.rept " STR(NS) "\n\ts_sub_u32 s[72+i_], s[72+i_], %2\n\ts_max_u32 %0, %0, s[72+i_]\n\t.set i_, i_+1\n\t.endr"
                 : "=&s"(m) : "s"(wsb + WS_HINT + spin * 2048), "s"(base + tag) : "scc", "memory", "s72", "s73", "s74", "s75", "s76", "s77", "s78", "s79", "s80", "s81", "s82", "s83", "s84", "s85", "s86", "s87", "s88", "s89", "s90", "s91", "s92", "s93", "s94", "s95");
    if (LIKELY(m < 4096u)) return base + m;
  }
  return NOW() - ago;
}
__device__ __forceinline__ void ln2_block(const KaArgs& A, char* smem, char* wsb, uint32_t tag, const Touch& th) {
  typedef __attribute__((ext_vector_type(16))) float f32x16;
  typedef __attribute__((ext_vector_type(8))) uint32_t u32x8;
  const int tid = tidx() - 256;
  const int w = __builtin_amdgcn_readfirstlane(tid >> 6), lane = tid & 63, l5 = tid & 31;
  const int b = blockIdx.x, two = (b < 80), gs = two ? w >> 2 : 0, G = two ? 2 * b + gs : 80 + b;
  const int e = (tid >> 5) & 3, su = 2 * gs + ((w >> 1) & 1), row = 4 * su + e;
  const int tokw = tok0(G >> 1) + 2 * (G & 1) + ((w >> 1) & 1), tok = tokw + 16 * e;
  TS(12);
  uint32_t xv[9], wv[9], nv[9];
  volatile LDS_AS uint32_t* hw = (volatile LDS_AS uint32_t*)(smem + 52);
  uint32_t xo = (uint32_t)(tok * D_ * 2 + 4 * l5), wo = 4u * l5, vo = WS_VG + (uint32_t)(G * 12288 + (w & 3) * 3072 + lane * 16);
  asm volatile("" : "+v"(xo), "+v"(wo), "+v"(vo));
  VOut vp;
  if (w < 3 || w == 4) vp = v_prep(HK(w + 4));
  UDONE();
  if (two || w >= 4) {
    LD9(xv, xo, A.x);
    LD9(wv, wo, A.wr);
  }
  LTOUCH();
  SB0();
  if (w < 3 || w == 4) {
    v_out(vp, wsb, tag, HK(w + 4));
  } else if (w == 5) {
    SETPRIO(2);
    LWAIT(4u * min(lane & 3, 2), 128 + 16 * MK, 1);
    const uint32_t t0 = NOW();
    if ((b & 56) == 8) hint_put(wsb, t0 + tag);
    SETPRIO(0);
    WAIT_T(t0, HT);
    hw[1] = hint_max(wsb, tag);
  }
  asm volatile("s_mov_b32 m0, %0" : : "s"(G_IMG + gs * 12288 + (w & 3) * 3072));
  TS(31);
  RAWBAR();
  if (!two && w < 4) { TSE(); __builtin_amdgcn_endpgm(); }
  SETPRIO(3);
  const uint32_t tg = __builtin_amdgcn_readfirstlane(hw[1]) + D2;
  TS(13);
  TSV(19, tg);
  while ((int)(NOW() - tg) < (two ? ((b < 64) ? VT4 : VT5) : VT)) {}
  TS(14);
  asm volatile("global_load_lds_dwordx4 %0, %1 sc1\n\tglobal_load_lds_dwordx4 %0, %1 offset:1024 sc1\n\t"
               "global_load_lds_dwordx4 %0, %1 offset:2048 sc1\n\ts_waitcnt vmcnt(3)" : : "v"(vo), "s"(wsb) : "memory");
  SB0();
  PIN9(xv); PIN9(wv);
  char* rec = smem + G_CH;
  uint32_t* cand = (uint32_t*)(rec + 64);
  uint32_t* so = (uint32_t*)(rec + 1024) + w * 576;
  if (w == 4) {
    if (lane < 4) *(u32x2*)(rec + 16 * lane + 8) = (u32x2){tag, tag};
    if (lane < 16) cand[lane] = 0u;
  }
  float ws = 0.f;
#pragma unroll
  for (int k = 0; k < 9; ++k) ws = DOT2(wv[k], 0x3f803f80u, ws);
  ws = half_sum(ws);
  f32x16 xa;
#pragma unroll
  for (int k = 0; k < 8; ++k) { xa[2 * k] = bflo(xv[k]); xa[2 * k + 1] = bfhi(xv[k]); }
  f32x2 xb = bf2x(xv[8]);
  u32x8 wa = {wv[0], wv[1], wv[2], wv[3], wv[4], wv[5], wv[6], wv[7]};
  uint32_t kl = (uint32_t)(0x7ff - tok) << 4;
  uint32_t ko = (uint32_t)(WS_KEYS + (lane >> 2) * KREP + key_place(b) + (lane & 3) * 16);
  uint32_t ur = (uint32_t)(l5 * 64 + ((e ^ (l5 >> 2)) & 3) * 16), ga = WS_VG + G * 12288 + 2048 + ur;
  ur += G_IMG + gs * 12288;
  uint32_t ra = G_CH + 16 * su + 2 * e, ca = G_CH + 64 + 4 * row, ru = G_CH + 16 * (lane & 3), gb = ga + 6144u;
  asm volatile("s_waitcnt vmcnt(0) lgkmcnt(0)\n\ts_barrier" : "+{v[16:31]}"(xa), "+{v[32:33]}"(xb), "+{v[34:41]}"(wa), "+{v42}"(wv[8]), "+v"(ws),
               "+v"(kl), "+v"(ko), "+v"(ur), "+v"(ga), "+v"(ra), "+v"(ca), "+v"(ru), "+v"(gb) : : "memory");
  TS(15);
  float mb, rc;
  uint32_t me;
  asm volatile(LN2_ASM : "+{v[16:31]}"(xa), "+{v[32:33]}"(xb), "={v104}"(mb), "={v105}"(rc), "={v106}"(me)
               : "v"(ur), "v"(ga), "v"(gb), "v"(ws), "v"(ra), "v"(ca), "v"(kl), "v"(ru), "v"(ko), "s"((w & 2) ? 0x03020c0cu : 0x01000c0cu), "s"(tag), "s"(wsb),
                 "s"(two ? 7 : 3), "s"(~0ull >> (64 - 32 * NCOPY)), "{v[34:41]}"(wa), "{v42}"(wv[8])
               : "memory", "vcc", "scc", "s96", "s97", "s98", "s99", LN2_REGS);
  TS(16);
  bool first;
  {
    f32x2 h[9];
#pragma unroll
    for (int k = 0; k < 8; ++k) h[k] = (f32x2){xa[2 * k], xa[2 * k + 1]};
    h[8] = xb;
    mb *= -rc;
#pragma unroll
    for (int k = 0; k < 9; ++k) { const f32x2 c = h[k] * rc + mb; nv[k] = pack2(c[0], c[1]); }
    PIN9(nv);
    RAWBAR();
    {
      uint32_t c = 0;
#pragma unroll
      for (int k = 0; k < 4; ++k) {
        const u32x4 cq = *(const u32x4*)(cand + 4 * k);
#pragma unroll
        for (int j = 0; j < 4; ++j) c = __builtin_amdgcn_alignbit(c, me - cq[j], 31);
      }
      first = __builtin_popcount(c) < XR;
    }
#define XSTORE() _Pragma("unroll") for (int j = 0; j < 3; ++j) \
      st16s(wsb, WS_XN + (uint32_t)(tok * 1536 + j * 512 + l5 * 16), ((j < 2) ? (u32x4){nv[4 * j], nv[4 * j + 1], nv[4 * j + 2], tag} : (u32x4){nv[3], nv[7], nv[8], tag}))
    if (first) { XSTORE(); }
    SETPRIO(0);
    TS(17);
    uint32_t* sp = so + (lane >> 5) * 288 + l5;
#pragma unroll
    for (int k = 0; k < 9; ++k) {
      const f32x2 o = h[k] + bf2x(nv[k]);
      sp[32 * k] = pack2(o[0], o[1]);
    }
  }
  if (w >= 4) {
    uint32_t q[8];
    KEY_ADR(tid - 256);
    WAIT_T(tg, OT);
    key_sweep(wsb, tag, k0, k1, key_lows(tid - 256), q);
    const uint32_t m1 = max(max(max(q[0], q[1]), max(q[2], q[3])), max(max(q[4], q[5]), max(q[6], q[7]))), mine = cand[tid & 15] << 1;
    uint32_t cr = 0;
#pragma unroll 1
    for (int r = 0; r < 8 + 8 * two; ++r) {
      const uint32_t me = __builtin_amdgcn_readlane(mine, r);
      uint32_t c = __builtin_popcountll(__builtin_amdgcn_ballot_w64(m1 > me));
      if (c < 8u) {
        c = 0;
#pragma unroll
        for (int j = 0; j < 8; ++j) c += __builtin_popcountll(__builtin_amdgcn_ballot_w64(q[j] > me));
      }
      cr = ((tid & 15) == r) ? c : cr;
    }
    cand[16 + 4 * (tid & 15) + (w & 3)] = cr;
  }
  RAWBAR();
  const u32x4 c4 = *(const u32x4*)&cand[16 + 4 * row];
  const bool routed = (c4[0] + c4[1]) + (c4[2] + c4[3]) < 8u;
  const uint64_t rm = __builtin_amdgcn_ballot_w64(routed);
  if (!first && routed) { XSTORE(); }
  const int tokA = tokw + 32 * (w & 1);
#pragma unroll
  for (int k = 0; k < 3; ++k) {
    const int e = lane + 64 * k, r = (e >= 72);
    if (e < 144 && !((rm >> (32 * r)) & 1u)) st16s(A.out, (uint32_t)((tokA + 16 * r) * (D_ * 2) + (e - 72 * r) * 16), *(const u32x4*)(so + 4 * e));
  }
  TS(18);
  TSE();
}

__device__ __forceinline__ void ffn_block(const KaArgs& A, char* smem, char* wsb, uint32_t ep, uint32_t tag, uint32_t tstart, int pw) {
  typedef __attribute__((ext_vector_type(32))) uint32_t u32x32;
  typedef __attribute__((ext_vector_type(16))) uint32_t u32x16;
  const int lane = tidx() & 63, w3 = (pw * 11) >> 5, w = (pw - 3 * w3 == 2) ? 8 + w3 : pw - w3, tid = 64 * w + lane;
  const int u = (blockIdx.x >= 192) ? blockIdx.x - 192 : blockIdx.x - 120, uc = 9 * (u & 7) + (u >> 3);
  const int n = lane & 15, kq = lane >> 4;
  const bool dn = w >= 8;
  const int wd = w & 3;
  TS(12);
  constexpr int F_WDL = 65536, F_PART = 8704, F_HID = F_PART + 8 * 4608, F_CAND = F_HID + 8 * 144, F_RED = F_CAND + 512, F_XN = F_RED + 6 * 256, XS = 1568;
  uint32_t* cand = (uint32_t*)(smem + F_CAND);
  u32x32 B0, B1;
  typedef __attribute__((ext_vector_type(8))) uint32_t u32x8;
  u32x8 QA, QH;
  u32x16 QB;
  uint32_t tm = 0;
  const int l5 = 4 * w + kq;
  {
    const char *W, *W2;
    int64_t d0, d1, d2, d3;
    asm volatile("v_bfe_u32 v6, %[t], 4, 2\n\tv_and_b32 v7, 7, %[t]\n\ts_cmp_gt_u32 %[w], 7\n\ts_cbranch_scc1 1f\n\t"
                 "v_mov_b64 v[2:3], %[pg]\n\ts_mov_b32 exec_lo, 0xff00ff00\n\ts_mov_b32 exec_hi, 0xff00ff00\n\tv_mov_b64 v[2:3], %[pu]\n\ts_mov_b64 exec, -1\n\t"
                 "s_mul_i32 s98, %[w], 16*4608\n\ts_lshl_b32 s99, %[u], 7\n\ts_add_u32 s98, s98, s99\n\tv_lshl_add_u32 v7, v7, 4, s98\n\t"
                 "v_mul_u32_u24 v4, 129, v6\n\tv_and_b32 v4, 0x82, v4\n\ts_movk_i32 s98, 9216\n\tv_mad_u32_u24 v8, v4, s98, v7\n\t"
                 "v_or_b32 v6, 256, v6\n\ts_movk_i32 s98, 18432\n\tv_mad_u32_u24 v6, v6, s98, v7\n\t"
                 "s_mov_b64 %[d0], 9216\n\ts_mov_b64 %[d1], 63*9216\n\ts_mov_b32 s92, -61*9216\n\ts_mov_b32 s93, -1\n\ts_mov_b64 %[d3], 187*9216\n\ts_branch 2f\n1:\n\t"
                 "v_mov_b64 v[2:3], %[pd]\n\ts_and_b32 s97, %[w], 3\n\ts_mul_i32 s96, %[u], 64*1152\n\ts_lshl_b32 s98, s97, 8\n\ts_add_u32 s98, s98, s96\n\t"
                 "v_lshlrev_b32 v4, 2, v6\n\tv_lshl_or_b32 v4, v6, 3, v4\n\tv_and_b32 v4, 20, v4\n\tv_and_b32 v8, 15, %[t]\n\tv_lshl_add_u32 v8, v8, 4, s98\n\ts_movk_i32 s98, 1152\n\tv_mad_u32_u24 v8, v4, s98, v8\n\t"
                 "s_mulk_i32 s97, 16*1152\n\ts_add_u32 s97, s97, s96\n\ts_addk_i32 s97, 1024\n\tv_lshl_add_u32 v7, v7, 4, s97\n\tv_bfe_u32 v6, %[t], 3, 3\n\t"
                 "s_movk_i32 s98, 2304\n\tv_mad_u32_u24 v6, v6, s98, v7\n\t"
                 "s_mov_b64 %[d0], 1152\n\ts_mov_b64 %[d1], 1152\n\ts_mov_b64 %[d2], 29*1152\n\ts_mov_b32 s94, -27*1152\n\ts_mov_b32 s95, -1\n2:\n\t"
                 "v_mov_b32 v7, 0\n\tv_mov_b32 v9, 0\n\tv_lshl_add_u64 v[4:5], v[2:3], 0, v[6:7]\n\tv_lshl_add_u64 v[2:3], v[2:3], 0, v[8:9]"
                 : "=&{v[2:3]}"(W), "=&{v[4:5]}"(W2), [d0] "=&s"(d0), [d1] "=&s"(d1), [d2] "=&{s[92:93]}"(d2), [d3] "=&{s[94:95]}"(d3)
                 : [t] "v"(lane), [w] "s"(w), [u] "s"(uc), [pg] "s"(A.wg), [pu] "s"(A.wu), [pd] "s"(A.wd) : "scc", "s96", "s97", "s98", "s99", "v6", "v7", "v8", "v9");
    asm volatile("" : "+s"(d0), "+s"(d1), "+s"(d2), "+s"(d3));
    u32x4 wa[8], wb[8], w8[2];
    const bool hlp = (u >> 6) & (0x170 >> pw);
    VOut vp;
    if (hlp) vp = v_prep(HK(pw));
    TS(13);
    if (u >= 64) {
      if ((LE >> pw) & 1) UDONE(); else LWAIT(0u, 112, 4);
    } else {
      if (dn) SETPRIO(DPRI);
      if ((0xdb6 & ~EB) >> pw & 1) asm volatile("s_barrier");
    }
    TS(14);
#define WL1(o, d) "global_load_dwordx4 %" #o ", %8, off" WNT "\n\tv_lshl_add_u64 %8, %8, 0, %" #d "\n\t"
#define WLD(r_) asm volatile(WL1(0, 9) WL1(1, 10) WL1(2, 9) WL1(3, 11) WL1(4, 9) WL1(5, 10) WL1(6, 9) WL1(7, 12) \
      : "=&v"(r_[0]), "=&v"(r_[1]), "=&v"(r_[2]), "=&v"(r_[3]), "=&v"(r_[4]), "=&v"(r_[5]), "=&v"(r_[6]), "=&v"(r_[7]), "+v"(W) : "s"(d0), "s"(d1), "s"(d2), "s"(d3))
#define PIN8(r_) asm volatile("" : "+v"(r_[0]), "+v"(r_[1]), "+v"(r_[2]), "+v"(r_[3]), "+v"(r_[4]), "+v"(r_[5]), "+v"(r_[6]), "+v"(r_[7]))
#define PRM(a_, b_, hi) ((hi) ? perm_hi(a_, b_) : perm_lo(a_, b_))
#define WTR(r_, B_) do { PIN8(r_); \
      _Pragma("unroll") for (int e = 0; e < 32; ++e) B_[e] = PRM(r_[e & 6][(e >> 4) + 2 * (e & 1)], r_[(e & 6) + 1][(e >> 4) + 2 * (e & 1)], (e >> 3) & 1); } while (0)
    WLD(wa);
    WLD(wb);
    asm volatile("global_load_dwordx4 %0, %2, off" WNT "\n\tv_lshl_add_u64 %2, %2, 0, %3\n\tglobal_load_dwordx4 %1, %2, off" WNT : "=&v"(w8[0]), "=&v"(w8[1]), "+v"(W2) : "s"(d0));
    if (hlp) {
      v_out(vp, wsb, tag, HK(pw));
      LARRIVE(0u, 112);
      SETPRIO(0);
      VMWAIT(5);
    }
    SETPRIO(0);
    if (u < 64) {
      if (EB >> pw & 1) asm volatile("s_barrier");
      tag = __builtin_amdgcn_readfirstlane(*(volatile LDS_AS uint32_t*)(smem + 96));
    }
    SB0();
    TS(15);
  QH[0] = 0u;
  QH[2] = (lane & 8) ? 0xeeeeeeeeu : 0x44444444u;
  if (dn) {
    KEY_ADR(tid - 512);
    const uint32_t klo = key_lows(tid - 512);
    const uint32_t dd = (u < 64) ? 64 * (u & 7) + lane : 512 + 8 * (u & 6) + n, fc = (u < 64) ? u >> 3 : 4 * (u & 1) + kq;
    const uint32_t q = (u < 64) ? 2 * (lane & 4) + (lane >> 3) : n, i = (u < 64) ? lane & 3 : kq;
    const uint32_t i3 = ((dd >> 6) * 86u) >> 8, p = dd - 192u * i3;
    QH[1] = F_RED + 4 * (32 * (i & 1) + 16 * (i >> 1) + q);
    QH[3] = k0; QH[4] = k1; QH[5] = klo; QH[6] = klo - 1024u * 0x10001u; QH[7] = klo - 32768u * 0x10001u; QB[0] = klo - 33792u * 0x10001u;
    QB[1] = F_CAND + 128 + 4 * (4 * wd + (lane & 3)); QB[2] = F_CAND + 128 + (lane & 60); QB[3] = F_CAND + 128 + 16 * (lane & 3);
    QB[4] = F_CAND + 4 * (lane & 31); QB[5] = F_CAND + 2 * (lane & 32); QB[6] = 16 * lane; QB[7] = F_HID + (n & 7) * 144 + 32 * kq;
    QB[8] = WS_SLAB + u * SLABB + (uint32_t)(4 * (kq & 1) * 512 + 128 * wd + 64 * (kq >> 1) + 4 * n) * 4u + 2048u;
    QB[9] = WS_SLAB + u * SLABB + (uint32_t)(4096 + 128 * wd + 4 * lane) * 4u;
    QB[10] = F_CAND + 256 + 4 * fc; QB[11] = 2 * dd; QB[12] = WS_VG + vrun(p) + ((p >> 3) & 3);
    const uint32_t jj = dd >> 6, l2 = (jj & 3) == 3 || jj == 8;
    QB[13] = F_XN + fc * XS + (l2 ? 1024 + (jj >> 2) * 4 : (jj >> 2) * 512 + (jj & 3) * 4) + ((dd >> 1) & 31) * 16 + 2 * (dd & 1);
  } else {
    const uint32_t g = INTER ? tid >> 5 : 2 * (tid >> 6) + ((tid >> 4) & 1), k = INTER ? (tid >> 3) & 3 : 2 * ((tid >> 5) & 1) + ((tid >> 3) & 1), c = tid & 7;
    const uint32_t fpos = (u < 64) ? (u >> 1) * 128 + (u & 1) * 32 : 64 * u;
    QH[1] = 0u;
    QH[3] = F_XN + (n & 7) * XS + l5 * 16;
    QH[4] = F_PART + g * 288 + k * 64 + c * (INTER ? 8 : 4);
    QH[5] = F_HID + (4 * (k & 1) + (g & 3)) * 144 + 2 * (8 * c + 4 * (k >> 1) + (g >> 2));
    QH[6] = WS_SLAB + (uint32_t)(12 * w + kq) * SLABB + (fpos + (n >> 3) * ((u < 64) ? 64 : 32) + 4 * (n & 7)) * 4u;
    QB[1] = F_CAND + 256 + 4 * (n & 7);
    QB[2] = WS_XN + (uint32_t)((L_ - 1) * 1536 + l5 * 16);
  }
    asm volatile("" : "+{v[88:95]}"(QH), "+{v[96:111]}"(QB));
    TS(16);
    TS(17);
    VMWAIT(10);
    WTR(wa, B0);
    asm volatile("" : "+{v[16:47]}"(B0));
    asm volatile("s_cmp_lg_u32 %0, 0\n\ts_cbranch_scc1 1f\n\ts_waitcnt vmcnt(0)\n1:\n\ts_waitcnt vmcnt(5)" : : "s"((u >> 6) & (0x160 >> pw)) : "scc", "memory");
    SB0();
    TS(18);
    WTR(wb, B1);
    asm volatile("" : "+{v[48:79]}"(B1));
    asm volatile("" : "+v"(w8[0]), "+v"(w8[1]));
#pragma unroll
    for (int e = 0; e < 8; ++e) QA[e] = PRM(w8[0][(e >> 2) + 2 * (e & 1)], w8[1][(e >> 2) + 2 * (e & 1)], (e >> 1) & 1);
    TS(19);
    if (dn) {
      if (u >= 64) LWAIT(0u, 36, 4);
      uint32_t* o2 = (uint32_t*)(smem + F_WDL + ((lane >> 1) & 3) * 2048 + (lane >> 5) * 1024 + (16 * wd + 8 * (lane & 1)) * 16) + ((lane >> 3) & 3);
#pragma unroll
      for (int e = 0; e < 8; ++e) o2[4 * ((e >> 1) + 4 * (e & 1))] = QA[e];
      if (w == 9) {
        cand[lane & 31] = 0u;
        WAIT_T(tstart, HF);
        cand[80] = hint_max(wsb, tag) + D2;
      }
      LARRIVE(0u, 116);
      LWAITS(0u, 116, 4, 2);
      tm = __builtin_amdgcn_readfirstlane(cand[80]);
      const u32x4 e0 = *(frag_p)(smem + F_WDL + wd * 2048 + lane * 16), e1 = *(frag_p)(smem + F_WDL + wd * 2048 + 1024 + lane * 16);
#pragma unroll
      for (int e = 0; e < 4; ++e) { QA[e] = e0[e]; QA[4 + e] = e1[e]; }
    }
  }
  asm volatile("" : "+{v[80:87]}"(QA));
  TS(21);
  TSV(20, tm);
  if (dn) WAIT_T(tm, KT - 12);
  asm volatile(FFN_ASM : "+{v[16:47]}"(B0), "+{v[88:95]}"(QH), "+{v[96:111]}"(QB)
               : [w] "s"(w), [tag] "s"(tag), [wsb] "{s[70:71]}"(wsb), [tg] "s"(tm + KT), [x] "s"(A.x), [out] "s"(A.out), "{v[48:79]}"(B1), "{v[80:87]}"(QA)
               : "memory", "vcc", "scc", "s81", "s82", "s83", "s84", "s85", "s86", "s87", "s88", "s89", "s90", "s91", "s92", "s93", "s94", "s95", "s96", "s97", "s98", "s99",
                 FFN_REGS);
  if (u == 0 && pw == 9 && (tidx() & 63) < 4) st16s(A.ws, (tidx() & 3) * 16u, (u32x4){ep + 0x9e3779b1u, 0u, 0u, 0u});
  TS(27);
  TSE();
}

#define ALL_REGS "v1", "v2", "v3", "v4", "v5", "v6", "v7", "v8", "v9", "v10", "v11", "v12", "v13", "v14", "v15", "v16", "v17", "v18", "v19", "v20", "v21", "v22", "v23", "v24", "v25", "v26", "v27", "v28", "v29", "v30", "v31", "v32", "v33", "v34", "v35", "v36", "v37", "v38", "v39", "v40", "v41", "v42", "v43", "v44", "v45", "v46", "v47", "v48", "v49", "v50", "v51", "v52", "v53", "v54", "v55", "v56", "v57", "v58", "v59", "v60", "v61", "v62", "v63", "v64", "v65", "v66", "v67", "v68", "v69", "v70", "v71", "v72", "v73", "v74", "v75", "v76", "v77", "v78", "v79", "v80", "v81", "v82", "v83", "v84", "v85", "v86", "v87", "v88", "v89", "v90", "v91", "v92", "v93", "v94", "v95", "v96", "v97", "v98", "v99", "v100", "v101", "v102", "v103", "v104", "v105", "v106", "v107", "v108", "v109", "v110", "v111", "v112", "v113", "v114", "v115", "v116", "v117", "v118", "v119", "v120", "v121", "v122", "v123", "v124", "v125", "v126", "v127", "v128", "v129", "v130", "v131", "v132", "v133", "v134", "v135", "v136", "v137", "v138", "v139", "v140", "v141", "v142", "v143", "v144", "v145", "v146", "v147", "v148", "v149", "v150", "v151", "v152", "v153", "v154", "v155", "v156", "v157", "v158", "v159", "v160", "v161", "v162", "v163", "v164", "v165", "v166"
extern "C" __global__ __launch_bounds__(768, 3) __attribute__((amdgpu_num_vgpr(79))) void k_all(const KaArgs) {
  __shared__ __attribute__((aligned(128))) char smem[GRP];
  typedef __attribute__((ext_vector_type(8))) uint64_t u64x8;
  typedef __attribute__((ext_vector_type(8))) uint32_t u32x8;
  u64x8 a0, a1;
  Touch th;
  uint64_t t64;
  char* wsb;
  int w, c, wk, d;
  uint32_t ep, tag, dtb, sD, av, chb, opc;
  const int b = blockIdx.x;
  asm volatile(ENTRY_ASM KGEN_ASM NEAR_ASM TSQ(9) "s_branch 90f\n81:\n\t" STAGE_ASM TSQ(8) "s_branch 90f\n89:\n\t" RESCALE_ASM "82:\n\t" FAR_ASM TSQ(9) "90:"
               : [w] "=&s"(w), [c] "=&s"(c), [wk] "=&s"(wk), [d] "=&s"(d), [ep] "=&s"(ep), [sdt] "=&s"(dtb), [sd] "=&s"(sD), [tag] "=&s"(tag),
                 [av] "=&s"(av), [chb] "=&s"(chb), [opc] "=&s"(opc),
                 "=&{s[36:51]}"(a0), "=&{s[52:67]}"(a1), "=&{s[68:69]}"(t64), "=&{s[70:71]}"(wsb), "=&{s[72:73]}"(th.b), "=&{s74}"(th.when)
               : [ka] "s"(__builtin_amdgcn_kernarg_segment_ptr()), [b] "s"(b), [id] "s"((uint32_t)amdgcn_dispatch_id()), [rot] "i"(NROT - 1), "{v0}"(threadIdx.x)
               : "memory", "vcc", "scc", "s75", "s76", "s77", "s78", "s79", "s90", "s91", "s92", "s93", "s94", "s95", "s96", "s97", "s98", "s99", ALL_REGS TSCL);
  const KaArgs A = {(const u16*)a0[0], (const u16*)a0[1], (const u16*)a0[2], (const u16*)a0[3], (const u16*)a0[4], (const u16*)a0[5], (const u16*)a0[6],
                    (const u16*)a0[7], (const u16*)a1[0], (char*)a1[1], (const u16*)a1[2], (const u16*)a1[3], (const u16*)a1[4], (const u16*)a1[5], (u16*)a1[6], nullptr};
  const uint32_t tstart = (uint32_t)t64;
  TSV(0, tstart);
  const int wq = w & 3;
  if (LIKELY(b < 192)) {
  SETPRIO(0);
  TS(10);
  if (b < 176) {
    if (w < 4) { asm volatile("s_waitcnt lgkmcnt(0)\n\ts_barrier"); TSE(); __builtin_amdgcn_endpgm(); }
    ln2_block(A, smem, wsb, tag, th);
    __builtin_amdgcn_endpgm();
  }
  SETPRIO(0);
  if (b < 184) {
    if (th.when - 4u < 8u) { UDONE(); LTOUCH(); }
    if ((0x170 >> w) & 1) v_out(v_prep(HK(w)), wsb, tag, HK(w));
    TSE();
    __builtin_amdgcn_endpgm();
  }
    SETPRIO(2);
  } else {
    SETPRIO(0);
    if (c == 0) { *(uint32_t*)(smem + 96) = tag; RAWBAR(); }
  }
  ffn_block(A, smem, wsb, ep, tag, tstart, w);
}

extern "C" void kernel_launch(void* const* d_in, const int* in_sizes, int n_in,
                              void* d_out, int out_size, void* d_ws, size_t ws_size,
                              hipStream_t stream) {
  const u16* x = (const u16*)d_in[0];
  const u16* Are = (const u16*)d_in[1];
  const u16* Aim = (const u16*)d_in[2];
  const u16* Bre = (const u16*)d_in[3];
  const u16* Bim = (const u16*)d_in[4];
  const u16* Cre = (const u16*)d_in[5];
  const u16* Cim = (const u16*)d_in[6];
  const u16* logdt = (const u16*)d_in[7];
  const u16* Dskip = (const u16*)d_in[8];
  const u16* wr = (const u16*)d_in[10];
  const u16* wg = (const u16*)d_in[11];
  const u16* wu = (const u16*)d_in[12];
  const u16* wd = (const u16*)d_in[13];
  u16* out = (u16*)d_out;
  const KaArgs ka = {x, Are, Aim, Bre, Bim, Cre, Cim, logdt, Dskip, (char*)d_ws, wr, wg, wu, wd, out, nullptr};
  k_all<<<256, 768, 0, stream>>>(ka);
}
```
